```python
import torch

def _softmax(x, axis=-1):
    return torch.softmax(x, dim=axis)
def _gelu(x):
    return torch.nn.functional.gelu(x, approximate="tanh")
def _silu(x):
    return torch.nn.functional.silu(x)
def _sigmoid(x):
    return torch.sigmoid(x)
def _layernorm(x, axis=-1, eps=1e-5):
    m = x.mean(dim=axis, keepdim=True)
    v = x.var(dim=axis, keepdim=True, unbiased=False)
    return (x - m) / torch.sqrt(v + eps)
def _scatter_add(tgt, idx, src, axis=0):
    return tgt.clone().index_add_(axis, idx, src)
def _segment_sum(src, seg, n):
    out = torch.zeros((n,) + src.shape[1:], dtype=src.dtype)
    return out.index_add_(0, seg, src)
def _pad(x, pw):
    flat = tuple(v for pair in reversed(pw) for v in pair)
    return torch.nn.functional.pad(x, flat)

kernel_name = 'standalone__mha_block_cached__near_ridge'

def setup_inputs():

    _w = (lambda a: (lambda t: t.to(torch.bfloat16) if t.is_floating_point() else t)(torch.as_tensor(a)))
    B, Sq, D = 8, 256, 448
    H, Sk, Dh = 16, 1024, 256
    return {
        "x": _w((lambda *s: torch.randn(*s, dtype=torch.bfloat16) * 0.8)(B, Sq, D)),
        "k_cache": _w((lambda *s: torch.randn(*s, dtype=torch.bfloat16) * 0.8)(B, H, Sk, Dh)),
        "v_cache": _w((lambda *s: torch.randn(*s, dtype=torch.bfloat16) * 0.05)(B, H, Sk, Dh)),
        "wq": _w((lambda *s: torch.randn(*s, dtype=torch.bfloat16) * 0.8)(D, H * Dh) * (D ** -0.5)),
        "wo": _w((lambda *s: torch.randn(*s, dtype=torch.bfloat16) * 0.05)(H * Dh, D) * ((H * Dh) ** -0.5)),
    }

def reference(x, k_cache, v_cache, wq, wo):
    B, Sq, D = x.shape
    H, Dh = 16, 256
    q = (lambda x, *d: x.permute(*d))((x @ wq).reshape(B, Sq, H, Dh), 0, 2, 1, 3)
    scores = torch.einsum("bhqd,bhkd->bhqk", q, k_cache) * (Dh ** -0.5)
    p = _softmax(scores, axis=-1)
    o = torch.einsum("bhqk,bhkd->bhqd", p, v_cache)
    return (lambda x, *d: x.permute(*d))(o, 0, 2, 1, 3).reshape(B, Sq, H * Dh) @ wo
```

```cpp
#include <hip/hip_runtime.h>
#include <hip/hip_bf16.h>
typedef unsigned short u16;
__device__ __forceinline__ float b2f(u16 v){ return __uint_as_float(((unsigned)v)<<16); }
__device__ __forceinline__ u16 f2b(float f){ __hip_bfloat16 h = __float2bfloat16(f); return *(u16*)&h; }
constexpr int B=8,SQ=256,D=448,H=16,SK=1024,DH=256;
__global__ void gemm(const u16* A,const u16* W,u16* C,int M,int N,int K){
  int n = blockIdx.x*64 + (threadIdx.x&63); int m = blockIdx.y*4 + (threadIdx.x>>6);
  if(n>=N||m>=M) return; float acc=0;
  for(int k=0;k<K;k++) acc += b2f(A[(size_t)m*K+k])*b2f(W[(size_t)k*N+n]);
  C[(size_t)m*N+n]=f2b(acc);
}
__global__ void attn(const u16* Q,const u16* Kc,const u16* Vc,u16* O){
  int q=blockIdx.x%SQ, h=(blockIdx.x/SQ)%H, b=blockIdx.x/(SQ*H); int t=threadIdx.x;
  __shared__ float qs[DH]; __shared__ float s[SK]; __shared__ float red[256];
  qs[t]=b2f(Q[((size_t)b*SQ+q)*(H*DH)+h*DH+t]); __syncthreads();
  const u16* Kp=Kc+((size_t)b*H+h)*SK*DH; const u16* Vp=Vc+((size_t)b*H+h)*SK*DH;
  float mx=-1e30f;
  for(int k=t;k<SK;k+=256){ float a=0; for(int d=0;d<DH;d++) a+=qs[d]*b2f(Kp[(size_t)k*DH+d]); a*=0.0625f; s[k]=a; mx=fmaxf(mx,a);}
  red[t]=mx; __syncthreads(); for(int o=128;o;o>>=1){ if(t<o) red[t]=fmaxf(red[t],red[t+o]); __syncthreads(); } mx=red[0]; __syncthreads();
  float sm=0; for(int k=t;k<SK;k+=256){ float e=__expf(s[k]-mx); s[k]=e; sm+=e; }
  red[t]=sm; __syncthreads(); for(int o=128;o;o>>=1){ if(t<o) red[t]+=red[t+o]; __syncthreads(); } sm=red[0];
  float a=0; for(int k=0;k<SK;k++) a+=s[k]*b2f(Vp[(size_t)k*DH+t]);
  O[((size_t)b*SQ+q)*(H*DH)+h*DH+t]=f2b(a/sm);
}
extern "C" void kernel_launch(void* const* d_in, const int* in_sizes, int n_in, void* d_out, int out_size, void* d_ws, size_t ws_size, hipStream_t stream){
  const u16* x=(const u16*)d_in[0]; const u16* k=(const u16*)d_in[1]; const u16* v=(const u16*)d_in[2]; const u16* wq=(const u16*)d_in[3]; const u16* wo=(const u16*)d_in[4];
  u16* Q=(u16*)d_ws; u16* O=Q+(size_t)B*SQ*H*DH;
  gemm<<<dim3(H*DH/64,B*SQ/4),256,0,stream>>>(x,wq,Q,B*SQ,H*DH,D);
  attn<<<B*H*SQ,256,0,stream>>>(Q,k,v,O);
  gemm<<<dim3(D/64,B*SQ/4),256,0,stream>>>(O,wo,(u16*)d_out,B*SQ,D,H*DH);
}
```

```cpp
#include <hip/hip_runtime.h>
#include <hip/hip_bf16.h>

__global__ __launch_bounds__(256, 1) void mha_main(const char* __restrict__ x, const char* __restrict__ kc, const char* __restrict__ vc,
                                                   const char* __restrict__ wq, const char* __restrict__ wo, char* __restrict__ ws, char* __restrict__ out,
                                                   const char* pad) {
  __shared__ __attribute__((aligned(16))) char lds[163840];
  asm volatile("" ::"s"((unsigned)(size_t)lds));
  const unsigned w = __builtin_amdgcn_readfirstlane(threadIdx.x >> 6);
  asm volatile(
    "LSTART%=:\n"
    "s_load_dwordx16 s[80:95], %0, 0x0\n"
    "s_getpc_b64 s[44:45]\n"
    "v_mbcnt_lo_u32_b32 v245, -1, 0\n"
    "v_mbcnt_hi_u32_b32 v245, -1, v245\n"
    "s_mov_b32 s68, %2\n"
    "s_lshr_b32 s17, %1, 3\n"
    "s_and_b32 s70, s17, 1\n"
    "s_bfe_u32 s57, %1, 0x20004\n"
    "s_lshr_b32 s41, %1, 6\n"
    "s_lshl_b32 s71, s41, 1\n"
    "s_add_u32 s71, s71, s70\n"
    "s_add_u32 s69, s71, s57\n"
    "s_mul_i32 s43, s69, 11\n"
    "s_lshr_b32 s43, s43, 5\n"
    "s_mul_i32 s43, s43, 3\n"
    "s_sub_u32 s69, s69, s43\n"
    "s_lshl_b32 s50, s69, 8\n"
    "s_and_b32 s43, %1, 1\n"
    "s_lshl_b32 s43, s43, 2\n"
    "s_add_u32 s18, s43, s41\n"
    "s_lshl_b32 s16, s18, 1\n"
    "s_add_u32 s16, s16, s70\n"
    "s_and_b32 s41, %1, 6\n"
    "s_lshl_b32 s41, s41, 1\n"
    "s_add_u32 s57, s57, s41\n"
    "s_lshl_b32 s18, s18, 4\n"
    "s_add_u32 s18, s18, s57\n"
    "s_lshl_b32 s36, s68, 12\n"
    "s_lshl_b32 s42, s68, 13\n"
    "s_and_b32 s45, s45, 0xffff\n"
    "s_mov_b32 s46, LRARE%=-LSTART%=-128\n"
    "s_mov_b32 s47, 0x20000\n"
    "v_and_b32 v252, 7, v245\n"
    "v_lshlrev_b32 v252, 6, v252\n"
    "s_lshl_b32 s41, s17, 2\n"
    "s_add_u32 s41, s41, s68\n"
    "s_lshl_b32 s41, s41, 9\n"
    "buffer_load_dword v255, v252, s[44:47], s41 offen offset:0\n"
    "v_lshrrev_b32 v250, 5, v245\n"
    "v_and_b32 v251, 31, v245\n"
    "v_lshrrev_b32 v246, 4, v245\n"
    "v_and_b32 v247, 15, v245\n"
    "v_lshrrev_b32 v252, 3, v245\n"
    "v_mul_u32_u24 v252, 896, v252\n"
    "v_and_b32 v253, 7, v245\n"
    "v_xor_b32 v253, v253, v246\n"
    "v_lshl_add_u32 v237, v253, 4, v252\n"
    "v_xor_b32 v253, 4, v253\n"
    "v_lshl_add_u32 v238, v253, 4, v252\n"
    "v_add_u32 v239, 0x3000, v237\n"
    "v_add_u32 v240, 0x4800, v238\n"
    "v_add_u32 v238, 0x1800, v238\n"
    "s_and_b32 s41, s68, 1\n"
    "s_lshl_b32 s41, s41, 3\n"
    "s_lshl_b32 s43, s68, 16\n"
    "v_lshlrev_b32 v252, 1, v250\n"
    "v_xor_b32 v252, s41, v252\n"
    "v_xor_b32 v252, v251, v252\n"
    "v_lshlrev_b32 v253, 13, v250\n"
    "v_add_u32 v253, s43, v253\n"
    "v_xor_b32 v233, 0, v252\n"
    "v_lshl_add_u32 v233, v233, 4, v253\n"
    "v_xor_b32 v234, 4, v252\n"
    "v_lshl_add_u32 v234, v234, 4, v253\n"
    "v_add_u32 v234, 0x3c00, v234\n"
    "v_xor_b32 v235, 0, v252\n"
    "v_lshl_add_u32 v235, v235, 4, v253\n"
    "v_add_u32 v235, 0x7800, v235\n"
    "v_xor_b32 v236, 4, v252\n"
    "v_lshl_add_u32 v236, v236, 4, v253\n"
    "v_add_u32 v236, 0xb400, v236\n"
    "s_lshl_b32 s41, s16, 2\n"
    "s_add_u32 s41, s41, s68\n"
    "s_mul_i32 s41, s41, 28672\n"
    "s_lshl_b32 s43, s57, 9\n"
    "s_mov_b32 s6, 28672\n"
    "s_mov_b32 s7, 0x20000\n"
    "s_sub_u32 s10, 3670016, s43\n"
    "s_mov_b32 s11, 0x20000\n"
    "s_waitcnt lgkmcnt(0)\n"
    "s_add_u32 s4, s80, s41\n"
    "s_addc_u32 s5, s81, 0\n"
    "s_and_b32 s5, s5, 0xffff\n"
    "s_add_u32 s8, s86, s43\n"
    "s_addc_u32 s9, s87, 0\n"
    "s_and_b32 s9, s9, 0xffff\n"
    "s_add_u32 m0, s36, 0x18000\n"
    "s_nop 0\n"
    "buffer_load_dwordx4 v237, s[4:7], s50 offen offset:0 lds\n"
    "buffer_load_dwordx4 v238, s[4:7], s50 offen offset:1024 lds\n"
    "buffer_load_dwordx4 v239, s[4:7], s50 offen offset:2048 lds\n"
    "buffer_load_dwordx4 v240, s[4:7], s50 offen offset:3072 lds\n"
    "s_lshl_b32 s51, s50, 12\n"
    "s_add_u32 m0, s36, 0x0\n"
    "s_nop 0\n"
    "buffer_load_dwordx4 v233, s[8:11], s51 offen offset:0 lds\n"
    "buffer_load_dwordx4 v234, s[8:11], s51 offen offset:1024 lds\n"
    "buffer_load_dwordx4 v235, s[8:11], s51 offen offset:2048 lds\n"
    "buffer_load_dwordx4 v236, s[8:11], s51 offen offset:3072 lds\n"
    "s_mov_b64 s[22:23], s[82:83]\n"
    "s_mov_b64 s[24:25], s[84:85]\n"
    "s_mov_b64 s[28:29], s[88:89]\n"
    "s_mov_b64 s[30:31], s[90:91]\n"
    "s_mov_b64 s[34:35], s[92:93]\n"
    "s_add_u32 s51, s51, 0x40000\n"
    "s_add_u32 m0, s36, 0x10000\n"
    "s_nop 0\n"
    "buffer_load_dwordx4 v233, s[8:11], s51 offen offset:0 lds\n"
    "buffer_load_dwordx4 v234, s[8:11], s51 offen offset:1024 lds\n"
    "buffer_load_dwordx4 v235, s[8:11], s51 offen offset:2048 lds\n"
    "buffer_load_dwordx4 v236, s[8:11], s51 offen offset:3072 lds\n"
    "s_add_u32 s50, s50, 0x80\n"
    "s_cmp_eq_u32 s50, 0x380\n"
    "s_cselect_b32 s50, 0, s50\n"
    "s_lshl_b32 s41, s16, 14\n"
    "s_add_u32 s41, s41, 0x1f20000\n"
    "s_add_u32 s52, s30, s41\n"
    "s_addc_u32 s53, s31, 0\n"
    "s_and_b32 s53, s53, 0xffff\n"
    "s_mov_b32 s54, 0x2100\n"
    "s_mov_b32 s55, 0x20000\n"
    "s_lshl_b32 s41, s57, 2\n"
    "s_add_u32 s41, s41, s68\n"
    "s_lshl_b32 s43, s41, 7\n"
    "s_add_u32 s43, s43, 256\n"
    "v_lshlrev_b32 v252, 2, v251\n"
    "v_add_u32 v252, s43, v252\n"
    "s_lshl_b32 s56, s41, 2\n"
    "v_mov_b32 v163, s56\n"
    "v_cmp_gt_u32 vcc, 32, v245\n"
    "v_bfe_u32 v248, v245, 2, 2\n"
    "v_and_b32 v249, 3, v245\n"
    "v_cndmask_b32 v252, v163, v252, vcc\n"
    "buffer_load_dword v162, v252, s[52:55], 0 offen offset:0 sc1\n"
    "v_lshl_add_u32 v252, v246, 2, v248\n"
    "v_lshlrev_b32 v253, 9, v252\n"
    "v_and_b32 v254, 1, v249\n"
    "v_lshl_add_u32 v253, v254, 3, v253\n"
    "v_and_b32 v252, 7, v252\n"
    "v_lshlrev_b32 v252, 1, v252\n"
    "v_lshrrev_b32 v254, 1, v249\n"
    "v_xor_b32 v252, v252, v254\n"
    "v_xor_b32 v254, 0, v252\n"
    "v_lshl_add_u32 v192, v254, 4, v253\n"
    "v_xor_b32 v254, 2, v252\n"
    "v_lshl_add_u32 v193, v254, 4, v253\n"
    "v_xor_b32 v254, 4, v252\n"
    "v_lshl_add_u32 v194, v254, 4, v253\n"
    "v_xor_b32 v254, 6, v252\n"
    "v_lshl_add_u32 v195, v254, 4, v253\n"
    "v_xor_b32 v254, 8, v252\n"
    "v_lshl_add_u32 v196, v254, 4, v253\n"
    "v_xor_b32 v254, 10, v252\n"
    "v_lshl_add_u32 v197, v254, 4, v253\n"
    "v_xor_b32 v254, 12, v252\n"
    "v_lshl_add_u32 v198, v254, 4, v253\n"
    "v_xor_b32 v254, 14, v252\n"
    "v_lshl_add_u32 v199, v254, 4, v253\n"
    "v_lshlrev_b32 v254, 11, v246\n"
    "v_add_u32 v128, v192, v254\n"
    "v_add_u32 v136, 0x10000, v128\n"
    "v_add_u32 v144, 0x20000, v128\n"
    "v_add_u32 v192, 0x10000, v192\n"
    "v_add_u32 v129, v193, v254\n"
    "v_add_u32 v137, 0x10000, v129\n"
    "v_add_u32 v145, 0x20000, v129\n"
    "v_add_u32 v193, 0x10000, v193\n"
    "v_add_u32 v130, v194, v254\n"
    "v_add_u32 v138, 0x10000, v130\n"
    "v_add_u32 v146, 0x20000, v130\n"
    "v_add_u32 v194, 0x10000, v194\n"
    "v_add_u32 v131, v195, v254\n"
    "v_add_u32 v139, 0x10000, v131\n"
    "v_add_u32 v147, 0x20000, v131\n"
    "v_add_u32 v195, 0x10000, v195\n"
    "v_add_u32 v132, v196, v254\n"
    "v_add_u32 v140, 0x10000, v132\n"
    "v_add_u32 v148, 0x20000, v132\n"
    "v_add_u32 v196, 0x10000, v196\n"
    "v_add_u32 v133, v197, v254\n"
    "v_add_u32 v141, 0x10000, v133\n"
    "v_add_u32 v149, 0x20000, v133\n"
    "v_add_u32 v197, 0x10000, v197\n"
    "v_add_u32 v134, v198, v254\n"
    "v_add_u32 v142, 0x10000, v134\n"
    "v_add_u32 v150, 0x20000, v134\n"
    "v_add_u32 v198, 0x10000, v198\n"
    "v_add_u32 v135, v199, v254\n"
    "v_add_u32 v143, 0x10000, v135\n"
    "v_add_u32 v151, 0x20000, v135\n"
    "v_add_u32 v199, 0x10000, v199\n"
    "v_lshlrev_b32 v253, 7, v247\n"
    "v_add_u32 v253, s36, v253\n"
    "v_lshrrev_b32 v252, 1, v247\n"
    "v_xor_b32 v252, v252, v246\n"
    "v_xor_b32 v254, 0, v252\n"
    "v_lshl_add_u32 v152, v254, 4, v253\n"
    "v_add_u32 v154, 0x10000, v152\n"
    "v_add_u32 v156, 0x20000, v152\n"
    "v_xor_b32 v254, 4, v252\n"
    "v_lshl_add_u32 v153, v254, 4, v253\n"
    "v_add_u32 v155, 0x10000, v153\n"
    "v_add_u32 v157, 0x20000, v153\n"
    "s_and_b32 s43, s68, 1\n"
    "s_lshl_b32 s43, s43, 3\n"
    "s_lshl_b32 s41, s68, 10\n"
    "v_lshlrev_b32 v253, 9, v250\n"
    "v_add_u32 v253, s41, v253\n"
    "v_xor_b32 v252, v251, v250\n"
    "v_xor_b32 v252, s43, v252\n"
    "v_xor_b32 v254, 0, v252\n"
    "v_lshl_add_u32 v200, v254, 4, v253\n"
    "v_xor_b32 v254, 2, v252\n"
    "v_lshl_add_u32 v201, v254, 4, v253\n"
    "v_add_u32 v201, 3072, v201\n"
    "v_xor_b32 v254, 4, v252\n"
    "v_lshl_add_u32 v202, v254, 4, v253\n"
    "v_add_u32 v202, 6144, v202\n"
    "v_xor_b32 v254, 6, v252\n"
    "v_lshl_add_u32 v203, v254, 4, v253\n"
    "v_add_u32 v203, 9216, v203\n"
    "v_lshlrev_b32 v252, 1, v250\n"
    "v_xor_b32 v252, v251, v252\n"
    "v_xor_b32 v254, 0, v252\n"
    "v_lshl_add_u32 v204, v254, 4, v253\n"
    "v_xor_b32 v254, 4, v252\n"
    "v_lshl_add_u32 v205, v254, 4, v253\n"
    "v_add_u32 v205, 3072, v205\n"
    "v_xor_b32 v254, 8, v252\n"
    "v_lshl_add_u32 v206, v254, 4, v253\n"
    "v_add_u32 v206, 6144, v206\n"
    "v_xor_b32 v254, 12, v252\n"
    "v_lshl_add_u32 v207, v254, 4, v253\n"
    "v_add_u32 v207, 9216, v207\n"
    "s_lshl_b32 s41, s18, 19\n"
    "s_add_u32 s80, s22, s41\n"
    "s_addc_u32 s81, s23, 0\n"
    "s_and_b32 s81, s81, 0xffff\n"
    "s_add_u32 s84, s24, s41\n"
    "s_addc_u32 s85, s25, 0\n"
    "s_and_b32 s85, s85, 0xffff\n"
    "s_and_b32 s41, s57, 14\n"
    "s_mul_i32 s41, s41, 229376\n"
    "s_add_u32 s12, s28, s41\n"
    "s_addc_u32 s13, s29, 0\n"
    "s_and_b32 s13, s13, 0xffff\n"
    "s_mov_b32 s14, 458752\n"
    "s_mov_b32 s15, 0x20000\n"
    "s_mov_b32 s82, 0x80000\n"
    "s_mov_b32 s83, 0x20000\n"
    "s_mov_b32 s86, 0x80000\n"
    "s_mov_b32 s87, 0x20000\n"
    "v_lshlrev_b32 v253, 9, v247\n"
    "v_and_b32 v254, 1, v246\n"
    "v_lshl_add_u32 v254, v254, 1, v250\n"
    "v_xor_b32 v254, v254, v247\n"
    "v_xor_b32 v252, 0, v254\n"
    "v_lshl_add_u32 v188, v252, 4, v253\n"
    "v_xor_b32 v252, 4, v254\n"
    "v_lshl_add_u32 v189, v252, 4, v253\n"
    "v_xor_b32 v252, 8, v254\n"
    "v_lshl_add_u32 v190, v252, 4, v253\n"
    "v_xor_b32 v252, 12, v254\n"
    "v_lshl_add_u32 v191, v252, 4, v253\n"
    "v_mov_b32 v184, 0x3f803f80\n"
    "v_mov_b32 v185, 0x3f803f80\n"
    "v_mov_b32 v186, 0x3f803f80\n"
    "v_mov_b32 v187, 0x3f803f80\n"
    "s_mov_b32 s40, 0x40004000\n"
    "s_mov_b32 s66, 0x3db8aa3b\n"
    "s_mov_b32 s67, 0x3db8aa3b\n"
    "s_add_u32 s92, s36, 0x0\n"
    "s_add_u32 s72, s36, 0x18000\n"
    "s_add_u32 s93, s36, 0x4000\n"
    "s_add_u32 s73, s36, 0x1c000\n"
    "s_add_u32 s94, s36, 0x8000\n"
    "s_add_u32 s74, s36, 0x10000\n"
    "s_add_u32 s95, s36, 0xc000\n"
    "s_add_u32 s75, s36, 0x14000\n"
    "s_mov_b32 s37, 0x10000\n"
    "s_mov_b32 s38, 0x8000\n"
    "s_lshl_b32 s71, s71, 2\n"
    "s_add_u32 s71, s71, s68\n"
    "s_mul_i32 s71, s71, 7168\n"
    "s_mul_i32 s43, s68, 28672\n"
    "v_lshrrev_b32 v252, 3, v245\n"
    "v_mul_u32_u24 v252, 896, v252\n"
    "v_add_u32 v252, s43, v252\n"
    "v_and_b32 v253, 7, v245\n"
    "v_lshlrev_b32 v254, 1, v246\n"
    "v_xor_b32 v253, v253, v254\n"
    "v_lshl_add_u32 v241, v253, 4, v252\n"
    "v_add_u32 v242, 0x1800, v241\n"
    "v_add_u32 v243, 0x3000, v241\n"
    "v_add_u32 v244, 0x4800, v241\n"
    "v_lshl_add_u32 v252, v246, 2, v248\n"
    "v_lshlrev_b32 v253, 7, v252\n"
    "v_and_b32 v254, 1, v249\n"
    "v_lshl_add_u32 v253, v254, 3, v253\n"
    "v_and_b32 v252, 6, v252\n"
    "v_lshrrev_b32 v254, 1, v249\n"
    "v_xor_b32 v252, v252, v254\n"
    "s_and_b32 s41, s68, 2\n"
    "s_lshl_b32 s41, s41, 1\n"
    "v_xor_b32 v252, s41, v252\n"
    "v_xor_b32 v254, 0, v252\n"
    "v_lshl_add_u32 v209, v254, 4, v253\n"
    "v_add_u32 v211, 0x10000, v209\n"
    "v_add_u32 v213, 0x20000, v209\n"
    "v_xor_b32 v254, 2, v252\n"
    "v_lshl_add_u32 v210, v254, 4, v253\n"
    "v_add_u32 v212, 0x10000, v210\n"
    "v_add_u32 v214, 0x20000, v210\n"
    "s_and_b32 s60, s68, 1\n"
    "s_lshr_b32 s61, s68, 1\n"
    "s_and_b32 s62, s57, 1\n"
    "v_lshlrev_b32 v252, 4, v245\n"
    "s_lshl_b32 s41, s60, 12\n"
    "s_lshl_b32 s43, s61, 10\n"
    "s_add_u32 s41, s41, s43\n"
    "v_add_u32 v229, s41, v252\n"
    "s_lshl_b32 s41, s60, 14\n"
    "v_add_u32 v223, s41, v252\n"
    "v_add_u32 v225, 0x18000, v223\n"
    "v_add_u32 v222, s42, v252\n"
    "v_add_u32 v224, 0x18000, v222\n"
    "s_mov_b32 s48, s62\n"
    "v_lshlrev_b32 v219, 2, v245\n"
    "v_lshlrev_b32 v252, 7, v245\n"
    "s_lshr_b32 s41, s57, 1\n"
    "s_lshl_b32 s41, s41, 2\n"
    "s_add_u32 s41, s41, 256\n"
    "s_cmp_eq_u32 s68, 0\n"
    "s_cselect_b32 s43, 0, 0x20000000\n"
    "v_add_u32 v252, s43, v252\n"
    "v_bfe_u32 v253, v245, 2, 1\n"
    "v_xor_b32 v253, s62, v253\n"
    "v_lshl_add_u32 v215, v253, 30, v252\n"
    "v_add_u32 v215, s41, v215\n"
    "s_xor_b32 s41, s57, 1\n"
    "v_lshrrev_b32 v253, 2, v245\n"
    "v_xor_b32 v253, s41, v253\n"
    "v_min_u32 v253, 1, v253\n"
    "v_lshl_add_u32 v216, v253, 30, v252\n"
    "v_add_u32 v216, 352, v216\n"
    "s_lshl_b32 s41, s57, 2\n"
    "s_add_u32 s41, s41, s68\n"
    "s_lshl_b32 s41, s41, 7\n"
    "s_add_u32 s41, s41, 256\n"
    "v_mov_b32 v218, s41\n"
    "v_and_b32 v253, 7, v245\n"
    "v_lshl_add_u32 v217, v253, 2, v218\n"
    "s_lshr_b32 s41, s57, 2\n"
    "s_lshl_b32 s41, s41, 11\n"
    "s_lshl_b32 s43, s60, 10\n"
    "s_add_u32 s41, s41, s43\n"
    "s_lshl_b32 s43, s61, 9\n"
    "s_add_u32 s41, s41, s43\n"
    "s_and_b32 s43, s57, 2\n"
    "s_lshl_b32 s43, s43, 6\n"
    "s_add_u32 s41, s41, s43\n"
    "v_bfe_u32 v254, v245, 3, 1\n"
    "v_lshlrev_b32 v220, 8, v254\n"
    "v_lshl_add_u32 v220, v253, 4, v220\n"
    "v_add_u32 v220, s41, v220\n"
    "v_mul_u32_u24 v221, 896, v253\n"
    "v_lshl_add_u32 v221, v254, 3, v221\n"
    "v_lshl_add_u32 v221, v250, 5, v221\n"
    "v_and_b32 v254, 1, v246\n"
    "v_lshl_add_u32 v221, v254, 2, v221\n"
    "s_lshl_b32 s41, s60, 6\n"
    "s_lshl_b32 s43, s61, 4\n"
    "s_add_u32 s41, s41, s43\n"
    "v_add_u32 v221, s41, v221\n"
    "s_lshl_b32 s41, s68, 7\n"
    "s_add_u32 s41, s41, 0x8000\n"
    "v_lshlrev_b32 v252, 2, v247\n"
    "v_add_u32 v230, s41, v252\n"
    "s_mul_i32 s65, s62, 229376\n"
    "v_lshlrev_b32 v231, 7, v245\n"
    "v_add_u32 v231, s71, v231\n"
    "v_add_u32 v231, s65, v231\n"
    "s_and_b32 s41, s16, 14\n"
    "s_lshl_b32 s41, s41, 14\n"
    "s_add_u32 s41, s41, 0x1f22400\n"
    "s_add_u32 s60, s30, s41\n"
    "s_addc_u32 s61, s31, 0\n"
    "s_and_b32 s61, s61, 0xffff\n"
    "s_mov_b32 s62, 0x5c00\n"
    "s_mov_b32 s63, 0x20000\n"
    "s_lshl_b32 s41, s57, 2\n"
    "s_add_u32 s41, s41, s68\n"
    "s_lshl_b32 s41, s41, 6\n"
    "s_lshl_b32 s43, s70, 14\n"
    "s_add_u32 s41, s41, s43\n"
    "v_mov_b32 v165, s41\n"
    "v_xor_b32 v166, 0x4000, v165\n"
    "s_add_u32 m0, s36, 0x4000\n"
    "s_nop 0\n"
    "buffer_load_dwordx4 v237, s[4:7], s50 offen offset:0 lds\n"
    "buffer_load_dwordx4 v238, s[4:7], s50 offen offset:1024 lds\n"
    "buffer_load_dwordx4 v239, s[4:7], s50 offen offset:2048 lds\n"
    "buffer_load_dwordx4 v240, s[4:7], s50 offen offset:3072 lds\n"
    "s_lshl_b32 s51, s50, 12\n"
    "s_add_u32 m0, s36, 0x8000\n"
    "s_nop 0\n"
    "buffer_load_dwordx4 v233, s[8:11], s51 offen offset:0 lds\n"
    "buffer_load_dwordx4 v234, s[8:11], s51 offen offset:1024 lds\n"
    "buffer_load_dwordx4 v235, s[8:11], s51 offen offset:2048 lds\n"
    "buffer_load_dwordx4 v236, s[8:11], s51 offen offset:3072 lds\n"
    "s_waitcnt vmcnt(13)\n"
    "s_barrier\n"
    "ds_read_b128 a[192:195], v154 offset:32768\n"
    "ds_read_b128 a[196:199], v154 offset:34816\n"
    "ds_read_b64_tr_b16 a[128:129], v128 offset:0\n"
    "ds_read_b64_tr_b16 a[130:131], v128 offset:2048\n"
    "ds_read_b64_tr_b16 a[132:133], v129 offset:0\n"
    "ds_read_b64_tr_b16 a[134:135], v129 offset:2048\n"
    "ds_read_b64_tr_b16 a[136:137], v130 offset:0\n"
    "ds_read_b64_tr_b16 a[138:139], v130 offset:2048\n"
    "ds_read_b64_tr_b16 a[140:141], v131 offset:0\n"
    "ds_read_b64_tr_b16 a[142:143], v131 offset:2048\n"
    "ds_read_b64_tr_b16 a[144:145], v132 offset:0\n"
    "ds_read_b64_tr_b16 a[146:147], v132 offset:2048\n"
    "ds_read_b64_tr_b16 a[148:149], v133 offset:0\n"
    "ds_read_b64_tr_b16 a[150:151], v133 offset:2048\n"
    "s_waitcnt lgkmcnt(8)\n"
    "v_mfma_f32_16x16x32_bf16 v[0:3], a[128:131], a[192:195], 0\n"
    "s_nop 2\n"
    "ds_read_b64_tr_b16 a[152:153], v134 offset:0\n"
    "v_mfma_f32_16x16x32_bf16 v[8:11], a[128:131], a[196:199], 0\n"
    "s_nop 2\n"
    "ds_read_b64_tr_b16 a[154:155], v134 offset:2048\n"
    "s_add_u32 s51, s51, 0x40000\n"
    "v_mfma_f32_16x16x32_bf16 v[12:15], a[132:135], a[196:199], 0\n"
    "s_nop 2\n"
    "ds_read_b64_tr_b16 a[156:157], v135 offset:0\n"
    "s_add_u32 m0, s36, 0xc000\n"
    "v_mfma_f32_16x16x32_bf16 v[4:7], a[132:135], a[192:195], 0\n"
    "s_nop 2\n"
    "s_waitcnt lgkmcnt(7)\n"
    "buffer_load_dwordx4 v233, s[8:11], s51 offen offset:0 lds\n"
    "v_mfma_f32_16x16x32_bf16 v[16:19], a[136:139], a[192:195], 0\n"
    "s_nop 2\n"
    "ds_read_b64_tr_b16 a[158:159], v135 offset:2048\n"
    "ds_read_b64_tr_b16 a[160:161], v128 offset:256\n"
    "v_mfma_f32_16x16x32_bf16 v[24:27], a[136:139], a[196:199], 0\n"
    "s_nop 2\n"
    "ds_read_b64_tr_b16 a[162:163], v128 offset:2304\n"
    "v_mfma_f32_16x16x32_bf16 v[28:31], a[140:143], a[196:199], 0\n"
    "s_nop 2\n"
    "ds_read_b64_tr_b16 a[164:165], v129 offset:256\n"
    "ds_read_b64_tr_b16 a[166:167], v129 offset:2304\n"
    "v_mfma_f32_16x16x32_bf16 v[20:23], a[140:143], a[192:195], 0\n"
    "s_nop 2\n"
    "s_waitcnt lgkmcnt(8)\n"
    "buffer_load_dwordx4 v234, s[8:11], s51 offen offset:1024 lds\n"
    "v_mfma_f32_16x16x32_bf16 v[32:35], a[144:147], a[192:195], 0\n"
    "s_nop 2\n"
    "ds_read_b64_tr_b16 a[168:169], v130 offset:256\n"
    "ds_read_b64_tr_b16 a[170:171], v130 offset:2304\n"
    "v_mfma_f32_16x16x32_bf16 v[40:43], a[144:147], a[196:199], 0\n"
    "s_nop 2\n"
    "ds_read_b64_tr_b16 a[172:173], v131 offset:256\n"
    "v_mfma_f32_16x16x32_bf16 v[44:47], a[148:151], a[196:199], 0\n"
    "s_nop 2\n"
    "ds_read_b64_tr_b16 a[174:175], v131 offset:2304\n"
    "ds_read_b64_tr_b16 a[176:177], v132 offset:256\n"
    "v_mfma_f32_16x16x32_bf16 v[36:39], a[148:151], a[192:195], 0\n"
    "s_nop 2\n"
    "s_waitcnt lgkmcnt(9)\n"
    "buffer_load_dwordx4 v235, s[8:11], s51 offen offset:2048 lds\n"
    "v_mfma_f32_16x16x32_bf16 v[48:51], a[152:155], a[192:195], 0\n"
    "s_nop 2\n"
    "s_waitcnt vmcnt(12)\n"
    "s_barrier\n"
    "v_mfma_f32_16x16x32_bf16 v[56:59], a[152:155], a[196:199], 0\n"
    "s_nop 2\n"
    "ds_read_b64_tr_b16 a[178:179], v132 offset:2304\n"
    "v_mfma_f32_16x16x32_bf16 v[60:63], a[156:159], a[196:199], 0\n"
    "s_nop 2\n"
    "ds_read_b64_tr_b16 a[180:181], v133 offset:256\n"
    "ds_read_b64_tr_b16 a[182:183], v133 offset:2304\n"
    "v_mfma_f32_16x16x32_bf16 v[52:55], a[156:159], a[192:195], 0\n"
    "s_nop 2\n"
    "s_waitcnt lgkmcnt(8)\n"
    "buffer_load_dwordx4 v236, s[8:11], s51 offen offset:3072 lds\n"
    "v_mfma_f32_16x16x32_bf16 v[64:67], a[160:163], a[192:195], 0\n"
    "s_nop 2\n"
    "ds_read_b64_tr_b16 a[184:185], v134 offset:256\n"
    "ds_read_b64_tr_b16 a[186:187], v134 offset:2304\n"
    "v_mfma_f32_16x16x32_bf16 v[72:75], a[160:163], a[196:199], 0\n"
    "s_nop 2\n"
    "ds_read_b64_tr_b16 a[188:189], v135 offset:256\n"
    "s_add_u32 s50, s50, 0x80\n"
    "v_mfma_f32_16x16x32_bf16 v[76:79], a[164:167], a[196:199], 0\n"
    "s_nop 2\n"
    "ds_read_b64_tr_b16 a[190:191], v135 offset:2304\n"
    "s_cmp_eq_u32 s50, 0x380\n"
    "s_cselect_b32 s50, 0, s50\n"
    "v_mfma_f32_16x16x32_bf16 v[68:71], a[164:167], a[192:195], 0\n"
    "s_nop 2\n"
    "s_waitcnt lgkmcnt(8)\n"
    "ds_read_b128 a[200:203], v155 offset:32768\n"
    "s_add_u32 m0, s36, 0x24000\n"
    "v_mfma_f32_16x16x32_bf16 v[80:83], a[168:171], a[192:195], 0\n"
    "s_nop 2\n"
    "buffer_load_dwordx4 v237, s[4:7], s50 offen offset:0 lds\n"
    "v_mfma_f32_16x16x32_bf16 v[88:91], a[168:171], a[196:199], 0\n"
    "s_nop 2\n"
    "ds_read_b128 a[204:207], v155 offset:34816\n"
    "v_mfma_f32_16x16x32_bf16 v[92:95], a[172:175], a[196:199], 0\n"
    "s_nop 2\n"
    "ds_read_b64_tr_b16 a[128:129], v136 offset:0\n"
    "v_mfma_f32_16x16x32_bf16 v[84:87], a[172:175], a[192:195], 0\n"
    "s_nop 2\n"
    "s_waitcnt lgkmcnt(7)\n"
    "ds_read_b64_tr_b16 a[130:131], v136 offset:2048\n"
    "ds_read_b64_tr_b16 a[132:133], v137 offset:0\n"
    "v_mfma_f32_16x16x32_bf16 v[96:99], a[176:179], a[192:195], 0\n"
    "s_nop 2\n"
    "buffer_load_dwordx4 v238, s[4:7], s50 offen offset:1024 lds\n"
    "v_mfma_f32_16x16x32_bf16 v[104:107], a[176:179], a[196:199], 0\n"
    "s_nop 2\n"
    "ds_read_b64_tr_b16 a[134:135], v137 offset:2048\n"
    "ds_read_b64_tr_b16 a[136:137], v138 offset:0\n"
    "v_mfma_f32_16x16x32_bf16 v[108:111], a[180:183], a[196:199], 0\n"
    "s_nop 2\n"
    "ds_read_b64_tr_b16 a[138:139], v138 offset:2048\n"
    "v_mfma_f32_16x16x32_bf16 v[100:103], a[180:183], a[192:195], 0\n"
    "s_nop 2\n"
    "s_waitcnt lgkmcnt(8)\n"
    "ds_read_b64_tr_b16 a[140:141], v139 offset:0\n"
    "ds_read_b64_tr_b16 a[142:143], v139 offset:2048\n"
    "v_mfma_f32_16x16x32_bf16 v[112:115], a[184:187], a[192:195], 0\n"
    "s_nop 2\n"
    "buffer_load_dwordx4 v239, s[4:7], s50 offen offset:2048 lds\n"
    "v_mfma_f32_16x16x32_bf16 v[120:123], a[184:187], a[196:199], 0\n"
    "s_nop 2\n"
    "ds_read_b64_tr_b16 a[144:145], v140 offset:0\n"
    "ds_read_b64_tr_b16 a[146:147], v140 offset:2048\n"
    "v_mfma_f32_16x16x32_bf16 v[124:127], a[188:191], a[196:199], 0\n"
    "s_nop 2\n"
    "ds_read_b64_tr_b16 a[148:149], v141 offset:0\n"
    "v_mfma_f32_16x16x32_bf16 v[116:119], a[188:191], a[192:195], 0\n"
    "s_nop 2\n"
    "s_waitcnt lgkmcnt(3)\n"
    "ds_read_b64_tr_b16 a[150:151], v141 offset:2048\n"
    "v_mfma_f32_16x16x32_bf16 v[0:3], a[128:131], a[200:203], v[0:3]\n"
    "s_nop 2\n"
    "s_waitcnt vmcnt(7)\n"
    "s_barrier\n"
    "v_mfma_f32_16x16x32_bf16 v[8:11], a[128:131], a[204:207], v[8:11]\n"
    "s_nop 2\n"
    "buffer_load_dwordx4 v240, s[4:7], s50 offen offset:3072 lds\n"
    "v_mfma_f32_16x16x32_bf16 v[12:15], a[132:135], a[204:207], v[12:15]\n"
    "s_nop 2\n"
    "ds_read_b64_tr_b16 a[152:153], v142 offset:0\n"
    "s_lshl_b32 s51, s50, 12\n"
    "v_mfma_f32_16x16x32_bf16 v[4:7], a[132:135], a[200:203], v[4:7]\n"
    "s_nop 2\n"
    "ds_read_b64_tr_b16 a[154:155], v142 offset:2048\n"
    "s_add_u32 m0, s36, 0x20000\n"
    "v_mfma_f32_16x16x32_bf16 v[16:19], a[136:139], a[200:203], v[16:19]\n"
    "s_nop 2\n"
    "ds_read_b64_tr_b16 a[156:157], v143 offset:0\n"
    "ds_read_b64_tr_b16 a[158:159], v143 offset:2048\n"
    "v_mfma_f32_16x16x32_bf16 v[24:27], a[136:139], a[204:207], v[24:27]\n"
    "s_nop 2\n"
    "buffer_load_dwordx4 v233, s[8:11], s51 offen offset:0 lds\n"
    "v_mfma_f32_16x16x32_bf16 v[28:31], a[140:143], a[204:207], v[28:31]\n"
    "s_nop 2\n"
    "ds_read_b64_tr_b16 a[160:161], v136 offset:256\n"
    "ds_read_b64_tr_b16 a[162:163], v136 offset:2304\n"
    "v_mfma_f32_16x16x32_bf16 v[20:23], a[140:143], a[200:203], v[20:23]\n"
    "s_nop 2\n"
    "s_waitcnt lgkmcnt(2)\n"
    "ds_read_b64_tr_b16 a[164:165], v137 offset:256\n"
    "v_mfma_f32_16x16x32_bf16 v[32:35], a[144:147], a[200:203], v[32:35]\n"
    "s_nop 2\n"
    "ds_read_b64_tr_b16 a[166:167], v137 offset:2304\n"
    "ds_read_b64_tr_b16 a[168:169], v138 offset:256\n"
    "v_mfma_f32_16x16x32_bf16 v[40:43], a[144:147], a[204:207], v[40:43]\n"
    "s_nop 2\n"
    "buffer_load_dwordx4 v234, s[8:11], s51 offen offset:1024 lds\n"
    "v_mfma_f32_16x16x32_bf16 v[44:47], a[148:151], a[204:207], v[44:47]\n"
    "s_nop 2\n"
    "ds_read_b64_tr_b16 a[170:171], v138 offset:2304\n"
    "ds_read_b64_tr_b16 a[172:173], v139 offset:256\n"
    "v_mfma_f32_16x16x32_bf16 v[36:39], a[148:151], a[200:203], v[36:39]\n"
    "s_nop 2\n"
    "ds_read_b64_tr_b16 a[174:175], v139 offset:2304\n"
    "v_mfma_f32_16x16x32_bf16 v[48:51], a[152:155], a[200:203], v[48:51]\n"
    "s_nop 2\n"
    "ds_read_b64_tr_b16 a[176:177], v140 offset:256\n"
    "ds_read_b64_tr_b16 a[178:179], v140 offset:2304\n"
    "v_mfma_f32_16x16x32_bf16 v[56:59], a[152:155], a[204:207], v[56:59]\n"
    "s_nop 2\n"
    "buffer_load_dwordx4 v235, s[8:11], s51 offen offset:2048 lds\n"
    "v_mfma_f32_16x16x32_bf16 v[60:63], a[156:159], a[204:207], v[60:63]\n"
    "s_nop 2\n"
    "ds_read_b64_tr_b16 a[180:181], v141 offset:256\n"
    "ds_read_b64_tr_b16 a[182:183], v141 offset:2304\n"
    "v_mfma_f32_16x16x32_bf16 v[52:55], a[156:159], a[200:203], v[52:55]\n"
    "s_nop 2\n"
    "s_waitcnt lgkmcnt(4)\n"
    "ds_read_b64_tr_b16 a[184:185], v142 offset:256\n"
    "v_mfma_f32_16x16x32_bf16 v[64:67], a[160:163], a[200:203], v[64:67]\n"
    "s_nop 2\n"
    "ds_read_b64_tr_b16 a[186:187], v142 offset:2304\n"
    "ds_read_b64_tr_b16 a[188:189], v143 offset:256\n"
    "v_mfma_f32_16x16x32_bf16 v[72:75], a[160:163], a[204:207], v[72:75]\n"
    "s_nop 2\n"
    "buffer_load_dwordx4 v236, s[8:11], s51 offen offset:3072 lds\n"
    "v_mfma_f32_16x16x32_bf16 v[76:79], a[164:167], a[204:207], v[76:79]\n"
    "s_nop 2\n"
    "ds_read_b64_tr_b16 a[190:191], v143 offset:2304\n"
    "s_add_u32 s51, s51, 0x40000\n"
    "v_mfma_f32_16x16x32_bf16 v[68:71], a[164:167], a[200:203], v[68:71]\n"
    "s_nop 2\n"
    "ds_read_b128 a[208:211], v152 offset:16384\n"
    "s_add_u32 m0, s36, 0x14000\n"
    "v_mfma_f32_16x16x32_bf16 v[80:83], a[168:171], a[200:203], v[80:83]\n"
    "s_nop 2\n"
    "ds_read_b64_tr_b16 a[128:129], v128 offset:32768\n"
    "ds_read_b64_tr_b16 a[130:131], v128 offset:34816\n"
    "v_mfma_f32_16x16x32_bf16 v[88:91], a[168:171], a[204:207], v[88:91]\n"
    "s_nop 2\n"
    "buffer_load_dwordx4 v233, s[8:11], s51 offen offset:0 lds\n"
    "v_mfma_f32_16x16x32_bf16 v[92:95], a[172:175], a[204:207], v[92:95]\n"
    "s_nop 2\n"
    "ds_read_b128 a[212:215], v152 offset:18432\n"
    "v_mfma_f32_16x16x32_bf16 v[84:87], a[172:175], a[200:203], v[84:87]\n"
    "s_nop 2\n"
    "s_waitcnt lgkmcnt(4)\n"
    "ds_read_b64_tr_b16 a[132:133], v129 offset:32768\n"
    "v_mfma_f32_16x16x32_bf16 v[96:99], a[176:179], a[200:203], v[96:99]\n"
    "s_nop 2\n"
    "ds_read_b64_tr_b16 a[134:135], v129 offset:34816\n"
    "ds_read_b64_tr_b16 a[136:137], v130 offset:32768\n"
    "v_mfma_f32_16x16x32_bf16 v[104:107], a[176:179], a[204:207], v[104:107]\n"
    "s_nop 2\n"
    "buffer_load_dwordx4 v234, s[8:11], s51 offen offset:1024 lds\n"
    "v_mfma_f32_16x16x32_bf16 v[108:111], a[180:183], a[204:207], v[108:111]\n"
    "s_nop 2\n"
    "ds_read_b64_tr_b16 a[138:139], v130 offset:34816\n"
    "ds_read_b64_tr_b16 a[140:141], v131 offset:32768\n"
    "v_mfma_f32_16x16x32_bf16 v[100:103], a[180:183], a[200:203], v[100:103]\n"
    "s_nop 2\n"
    "ds_read_b128 a[216:219], v153 offset:16384\n"
    "v_mfma_f32_16x16x32_bf16 v[112:115], a[184:187], a[200:203], v[112:115]\n"
    "s_nop 2\n"
    "ds_read_b128 a[220:223], v153 offset:18432\n"
    "v_mfma_f32_16x16x32_bf16 v[120:123], a[184:187], a[204:207], v[120:123]\n"
    "s_nop 2\n"
    "buffer_load_dwordx4 v235, s[8:11], s51 offen offset:2048 lds\n"
    "v_mfma_f32_16x16x32_bf16 v[124:127], a[188:191], a[204:207], v[124:127]\n"
    "s_nop 2\n"
    "ds_read_b64_tr_b16 a[142:143], v131 offset:34816\n"
    "ds_read_b64_tr_b16 a[144:145], v132 offset:32768\n"
    "v_mfma_f32_16x16x32_bf16 v[116:119], a[188:191], a[200:203], v[116:119]\n"
    "s_nop 2\n"
    "s_waitcnt lgkmcnt(1)\n"
    "ds_read_b64_tr_b16 a[146:147], v132 offset:34816\n"
    "v_mfma_f32_16x16x32_bf16 v[0:3], a[128:131], a[208:211], v[0:3]\n"
    "s_nop 2\n"
    "s_waitcnt vmcnt(11)\n"
    "s_barrier\n"
    "v_mfma_f32_16x16x32_bf16 v[8:11], a[128:131], a[212:215], v[8:11]\n"
    "s_nop 2\n"
    "buffer_load_dwordx4 v236, s[8:11], s51 offen offset:3072 lds\n"
    "v_mfma_f32_16x16x32_bf16 v[12:15], a[132:135], a[212:215], v[12:15]\n"
    "s_nop 2\n"
    "ds_read_b64_tr_b16 a[148:149], v133 offset:32768\n"
    "ds_read_b64_tr_b16 a[150:151], v133 offset:34816\n"
    "v_mfma_f32_16x16x32_bf16 v[4:7], a[132:135], a[208:211], v[4:7]\n"
    "s_nop 2\n"
    "ds_read_b64_tr_b16 a[152:153], v134 offset:32768\n"
    "s_add_u32 s50, s50, 0x80\n"
    "v_mfma_f32_16x16x32_bf16 v[16:19], a[136:139], a[208:211], v[16:19]\n"
    "s_nop 2\n"
    "ds_read_b64_tr_b16 a[154:155], v134 offset:34816\n"
    "s_cmp_eq_u32 s50, 0x380\n"
    "s_cselect_b32 s50, 0, s50\n"
    "v_mfma_f32_16x16x32_bf16 v[24:27], a[136:139], a[212:215], v[24:27]\n"
    "s_nop 2\n"
    "ds_read_b64_tr_b16 a[156:157], v135 offset:32768\n"
    "s_add_u32 m0, s36, 0x1c000\n"
    "v_mfma_f32_16x16x32_bf16 v[28:31], a[140:143], a[212:215], v[28:31]\n"
    "s_nop 2\n"
    "buffer_load_dwordx4 v237, s[4:7], s50 offen offset:0 lds\n"
    "v_mfma_f32_16x16x32_bf16 v[20:23], a[140:143], a[208:211], v[20:23]\n"
    "s_nop 2\n"
    "s_waitcnt lgkmcnt(0)\n"
    "ds_read_b64_tr_b16 a[158:159], v135 offset:34816\n"
    "ds_read_b64_tr_b16 a[160:161], v128 offset:33024\n"
    "v_mfma_f32_16x16x32_bf16 v[32:35], a[144:147], a[208:211], v[32:35]\n"
    "s_nop 2\n"
    "ds_read_b64_tr_b16 a[162:163], v128 offset:35072\n"
    "v_mfma_f32_16x16x32_bf16 v[40:43], a[144:147], a[212:215], v[40:43]\n"
    "s_nop 2\n"
    "ds_read_b64_tr_b16 a[164:165], v129 offset:33024\n"
    "ds_read_b64_tr_b16 a[166:167], v129 offset:35072\n"
    "v_mfma_f32_16x16x32_bf16 v[44:47], a[148:151], a[212:215], v[44:47]\n"
    "s_nop 2\n"
    "buffer_load_dwordx4 v238, s[4:7], s50 offen offset:1024 lds\n"
    "v_mfma_f32_16x16x32_bf16 v[36:39], a[148:151], a[208:211], v[36:39]\n"
    "s_nop 2\n"
    "ds_read_b64_tr_b16 a[168:169], v130 offset:33024\n"
    "ds_read_b64_tr_b16 a[170:171], v130 offset:35072\n"
    "v_mfma_f32_16x16x32_bf16 v[48:51], a[152:155], a[208:211], v[48:51]\n"
    "s_nop 2\n"
    "ds_read_b64_tr_b16 a[172:173], v131 offset:33024\n"
    "v_mfma_f32_16x16x32_bf16 v[56:59], a[152:155], a[212:215], v[56:59]\n"
    "s_nop 2\n"
    "ds_read_b64_tr_b16 a[174:175], v131 offset:35072\n"
    "ds_read_b64_tr_b16 a[176:177], v132 offset:33024\n"
    "s_waitcnt lgkmcnt(9)\n"
    "v_mfma_f32_16x16x32_bf16 v[60:63], a[156:159], a[212:215], v[60:63]\n"
    "s_nop 2\n"
    "buffer_load_dwordx4 v239, s[4:7], s50 offen offset:2048 lds\n"
    "v_mfma_f32_16x16x32_bf16 v[52:55], a[156:159], a[208:211], v[52:55]\n"
    "s_nop 2\n"
    "s_waitcnt lgkmcnt(1)\n"
    "ds_read_b64_tr_b16 a[178:179], v132 offset:35072\n"
    "ds_read_b64_tr_b16 a[180:181], v133 offset:33024\n"
    "v_mfma_f32_16x16x32_bf16 v[64:67], a[160:163], a[208:211], v[64:67]\n"
    "s_nop 2\n"
    "ds_read_b64_tr_b16 a[182:183], v133 offset:35072\n"
    "v_mfma_f32_16x16x32_bf16 v[72:75], a[160:163], a[212:215], v[72:75]\n"
    "s_nop 2\n"
    "ds_read_b64_tr_b16 a[184:185], v134 offset:33024\n"
    "ds_read_b64_tr_b16 a[186:187], v134 offset:35072\n"
    "v_mfma_f32_16x16x32_bf16 v[76:79], a[164:167], a[212:215], v[76:79]\n"
    "s_nop 2\n"
    "buffer_load_dwordx4 v240, s[4:7], s50 offen offset:3072 lds\n"
    "v_mfma_f32_16x16x32_bf16 v[68:71], a[164:167], a[208:211], v[68:71]\n"
    "s_nop 2\n"
    "ds_read_b64_tr_b16 a[188:189], v135 offset:33024\n"
    "s_lshl_b32 s51, s50, 12\n"
    "v_mfma_f32_16x16x32_bf16 v[80:83], a[168:171], a[208:211], v[80:83]\n"
    "s_nop 2\n"
    "ds_read_b64_tr_b16 a[190:191], v135 offset:35072\n"
    "s_add_u32 m0, s36, 0x18000\n"
    "v_mfma_f32_16x16x32_bf16 v[88:91], a[168:171], a[212:215], v[88:91]\n"
    "s_nop 2\n"
    "ds_read_b64_tr_b16 a[128:129], v128 offset:49152\n"
    "ds_read_b64_tr_b16 a[130:131], v128 offset:51200\n"
    "v_mfma_f32_16x16x32_bf16 v[92:95], a[172:175], a[212:215], v[92:95]\n"
    "s_nop 2\n"
    "buffer_load_dwordx4 v233, s[8:11], s51 offen offset:0 lds\n"
    "v_mfma_f32_16x16x32_bf16 v[84:87], a[172:175], a[208:211], v[84:87]\n"
    "s_nop 2\n"
    "s_waitcnt lgkmcnt(2)\n"
    "ds_read_b64_tr_b16 a[132:133], v129 offset:49152\n"
    "ds_read_b64_tr_b16 a[134:135], v129 offset:51200\n"
    "v_mfma_f32_16x16x32_bf16 v[96:99], a[176:179], a[208:211], v[96:99]\n"
    "s_nop 2\n"
    "ds_read_b64_tr_b16 a[136:137], v130 offset:49152\n"
    "v_mfma_f32_16x16x32_bf16 v[104:107], a[176:179], a[212:215], v[104:107]\n"
    "s_nop 2\n"
    "ds_read_b64_tr_b16 a[138:139], v130 offset:51200\n"
    "ds_read_b64_tr_b16 a[140:141], v131 offset:49152\n"
    "v_mfma_f32_16x16x32_bf16 v[108:111], a[180:183], a[212:215], v[108:111]\n"
    "s_nop 2\n"
    "buffer_load_dwordx4 v234, s[8:11], s51 offen offset:1024 lds\n"
    "v_mfma_f32_16x16x32_bf16 v[100:103], a[180:183], a[208:211], v[100:103]\n"
    "s_nop 2\n"
    "ds_read_b64_tr_b16 a[142:143], v131 offset:51200\n"
    "ds_read_b64_tr_b16 a[144:145], v132 offset:49152\n"
    "v_mfma_f32_16x16x32_bf16 v[112:115], a[184:187], a[208:211], v[112:115]\n"
    "s_nop 2\n"
    "ds_read_b64_tr_b16 a[146:147], v132 offset:51200\n"
    "v_mfma_f32_16x16x32_bf16 v[120:123], a[184:187], a[212:215], v[120:123]\n"
    "s_nop 2\n"
    "ds_read_b64_tr_b16 a[148:149], v133 offset:49152\n"
    "ds_read_b64_tr_b16 a[150:151], v133 offset:51200\n"
    "v_mfma_f32_16x16x32_bf16 v[124:127], a[188:191], a[212:215], v[124:127]\n"
    "s_nop 2\n"
    "buffer_load_dwordx4 v235, s[8:11], s51 offen offset:2048 lds\n"
    "v_mfma_f32_16x16x32_bf16 v[116:119], a[188:191], a[208:211], v[116:119]\n"
    "s_nop 2\n"
    "s_waitcnt lgkmcnt(4)\n"
    "ds_read_b64_tr_b16 a[152:153], v134 offset:49152\n"
    "ds_read_b64_tr_b16 a[154:155], v134 offset:51200\n"
    "v_mfma_f32_16x16x32_bf16 v[0:3], a[128:131], a[216:219], v[0:3]\n"
    "s_nop 1\n"
    "s_waitcnt vmcnt(11)\n"
    "s_barrier\n"
    "v_mfma_f32_16x16x32_bf16 v[8:11], a[128:131], a[220:223], v[8:11]\n"
    "s_nop 1\n"
    "ds_read_b64_tr_b16 a[156:157], v135 offset:49152\n"
    "ds_read_b64_tr_b16 a[158:159], v135 offset:51200\n"
    "v_mfma_f32_16x16x32_bf16 v[12:15], a[132:135], a[220:223], v[12:15]\n"
    "s_nop 1\n"
    "buffer_load_dwordx4 v236, s[8:11], s51 offen offset:3072 lds\n"
    "v_mfma_f32_16x16x32_bf16 v[4:7], a[132:135], a[216:219], v[4:7]\n"
    "s_nop 1\n"
    "ds_read_b64_tr_b16 a[160:161], v128 offset:49408\n"
    "s_add_u32 s51, s51, 0x40000\n"
    "v_mfma_f32_16x16x32_bf16 v[16:19], a[136:139], a[216:219], v[16:19]\n"
    "s_nop 1\n"
    "ds_read_b64_tr_b16 a[162:163], v128 offset:51456\n"
    "s_add_u32 m0, s36, 0x0\n"
    "v_mfma_f32_16x16x32_bf16 v[24:27], a[136:139], a[220:223], v[24:27]\n"
    "s_nop 1\n"
    "ds_read_b64_tr_b16 a[164:165], v129 offset:49408\n"
    "ds_read_b64_tr_b16 a[166:167], v129 offset:51456\n"
    "v_mfma_f32_16x16x32_bf16 v[28:31], a[140:143], a[220:223], v[28:31]\n"
    "s_nop 1\n"
    "buffer_load_dwordx4 v233, s[8:11], s51 offen offset:0 lds\n"
    "v_mfma_f32_16x16x32_bf16 v[20:23], a[140:143], a[216:219], v[20:23]\n"
    "s_nop 1\n"
    "s_waitcnt lgkmcnt(4)\n"
    "ds_read_b64_tr_b16 a[168:169], v130 offset:49408\n"
    "ds_read_b64_tr_b16 a[170:171], v130 offset:51456\n"
    "v_mfma_f32_16x16x32_bf16 v[32:35], a[144:147], a[216:219], v[32:35]\n"
    "s_nop 1\n"
    "ds_read_b64_tr_b16 a[172:173], v131 offset:49408\n"
    "v_mfma_f32_16x16x32_bf16 v[40:43], a[144:147], a[220:223], v[40:43]\n"
    "s_nop 1\n"
    "ds_read_b64_tr_b16 a[174:175], v131 offset:51456\n"
    "ds_read_b64_tr_b16 a[176:177], v132 offset:49408\n"
    "v_mfma_f32_16x16x32_bf16 v[44:47], a[148:151], a[220:223], v[44:47]\n"
    "s_nop 1\n"
    "buffer_load_dwordx4 v234, s[8:11], s51 offen offset:1024 lds\n"
    "v_mfma_f32_16x16x32_bf16 v[36:39], a[148:151], a[216:219], v[36:39]\n"
    "s_nop 1\n"
    "ds_read_b64_tr_b16 a[178:179], v132 offset:51456\n"
    "ds_read_b64_tr_b16 a[180:181], v133 offset:49408\n"
    "v_mfma_f32_16x16x32_bf16 v[48:51], a[152:155], a[216:219], v[48:51]\n"
    "s_nop 1\n"
    "ds_read_b64_tr_b16 a[182:183], v133 offset:51456\n"
    "v_mfma_f32_16x16x32_bf16 v[56:59], a[152:155], a[220:223], v[56:59]\n"
    "s_nop 1\n"
    "ds_read_b64_tr_b16 a[184:185], v134 offset:49408\n"
    "ds_read_b64_tr_b16 a[186:187], v134 offset:51456\n"
    "v_mfma_f32_16x16x32_bf16 v[60:63], a[156:159], a[220:223], v[60:63]\n"
    "s_nop 1\n"
    "buffer_load_dwordx4 v235, s[8:11], s51 offen offset:2048 lds\n"
    "v_mfma_f32_16x16x32_bf16 v[52:55], a[156:159], a[216:219], v[52:55]\n"
    "s_nop 1\n"
    "s_waitcnt lgkmcnt(6)\n"
    "ds_read_b64_tr_b16 a[188:189], v135 offset:49408\n"
    "ds_read_b64_tr_b16 a[190:191], v135 offset:51456\n"
    "v_mfma_f32_16x16x32_bf16 v[64:67], a[160:163], a[216:219], v[64:67]\n"
    "s_nop 1\n"
    "ds_read_b128 a[224:227], v156 offset:16384\n"
    "v_mfma_f32_16x16x32_bf16 v[72:75], a[160:163], a[220:223], v[72:75]\n"
    "s_nop 1\n"
    "ds_read_b64_tr_b16 a[128:129], v144 offset:0\n"
    "ds_read_b64_tr_b16 a[130:131], v144 offset:2048\n"
    "v_mfma_f32_16x16x32_bf16 v[76:79], a[164:167], a[220:223], v[76:79]\n"
    "s_nop 1\n"
    "buffer_load_dwordx4 v236, s[8:11], s51 offen offset:3072 lds\n"
    "v_mfma_f32_16x16x32_bf16 v[68:71], a[164:167], a[216:219], v[68:71]\n"
    "s_nop 1\n"
    "ds_read_b128 a[228:231], v156 offset:18432\n"
    "s_add_u32 s50, s50, 0x80\n"
    "v_mfma_f32_16x16x32_bf16 v[80:83], a[168:171], a[216:219], v[80:83]\n"
    "s_nop 1\n"
    "ds_read_b64_tr_b16 a[132:133], v145 offset:0\n"
    "s_cmp_eq_u32 s50, 0x380\n"
    "s_cselect_b32 s50, 0, s50\n"
    "v_mfma_f32_16x16x32_bf16 v[88:91], a[168:171], a[220:223], v[88:91]\n"
    "s_nop 1\n"
    "ds_read_b64_tr_b16 a[134:135], v145 offset:2048\n"
    "s_add_u32 m0, s36, 0x10000\n"
    "v_mfma_f32_16x16x32_bf16 v[92:95], a[172:175], a[220:223], v[92:95]\n"
    "s_nop 1\n"
    "ds_read_b64_tr_b16 a[136:137], v146 offset:0\n"
    "ds_read_b64_tr_b16 a[138:139], v146 offset:2048\n"
    "v_mfma_f32_16x16x32_bf16 v[84:87], a[172:175], a[216:219], v[84:87]\n"
    "s_nop 1\n"
    "s_waitcnt lgkmcnt(8)\n"
    "buffer_load_dwordx4 v237, s[4:7], s50 offen offset:0 lds\n"
    "v_mfma_f32_16x16x32_bf16 v[96:99], a[176:179], a[216:219], v[96:99]\n"
    "s_nop 1\n"
    "ds_read_b128 a[232:235], v157 offset:16384\n"
    "s_memrealtime s[76:77]\n"
    "v_mfma_f32_16x16x32_bf16 v[104:107], a[176:179], a[220:223], v[104:107]\n"
    "s_nop 1\n"
    "ds_read_b128 a[236:239], v157 offset:18432\n"
    "v_mfma_f32_16x16x32_bf16 v[108:111], a[180:183], a[220:223], v[108:111]\n"
    "s_nop 1\n"
    "ds_read_b64_tr_b16 a[140:141], v147 offset:0\n"
    "ds_read_b64_tr_b16 a[142:143], v147 offset:2048\n"
    "v_mfma_f32_16x16x32_bf16 v[100:103], a[180:183], a[216:219], v[100:103]\n"
    "s_nop 1\n"
    "buffer_load_dwordx4 v238, s[4:7], s50 offen offset:1024 lds\n"
    "v_mfma_f32_16x16x32_bf16 v[112:115], a[184:187], a[216:219], v[112:115]\n"
    "s_nop 1\n"
    "ds_read_b64_tr_b16 a[144:145], v148 offset:0\n"
    "ds_read_b64_tr_b16 a[146:147], v148 offset:2048\n"
    "v_mfma_f32_16x16x32_bf16 v[120:123], a[184:187], a[220:223], v[120:123]\n"
    "s_nop 1\n"
    "ds_read_b64_tr_b16 a[148:149], v149 offset:0\n"
    "v_mfma_f32_16x16x32_bf16 v[124:127], a[188:191], a[220:223], v[124:127]\n"
    "s_nop 1\n"
    "ds_read_b64_tr_b16 a[150:151], v149 offset:2048\n"
    "ds_read_b64_tr_b16 a[152:153], v150 offset:0\n"
    "v_mfma_f32_16x16x32_bf16 v[116:119], a[188:191], a[216:219], v[116:119]\n"
    "s_nop 1\n"
    "s_waitcnt lgkmcnt(5)\n"
    "buffer_load_dwordx4 v239, s[4:7], s50 offen offset:2048 lds\n"
    "v_mfma_f32_16x16x32_bf16 v[0:3], a[128:131], a[224:227], v[0:3]\n"
    "s_nop 1\n"
    "s_waitcnt vmcnt(15)\n"
    "s_barrier\n"
    "v_mfma_f32_16x16x32_bf16 v[8:11], a[128:131], a[228:231], v[8:11]\n"
    "s_nop 1\n"
    "ds_read_b64_tr_b16 a[154:155], v150 offset:2048\n"
    "v_mfma_f32_16x16x32_bf16 v[12:15], a[132:135], a[228:231], v[12:15]\n"
    "s_nop 1\n"
    "ds_read_b64_tr_b16 a[156:157], v151 offset:0\n"
    "ds_read_b64_tr_b16 a[158:159], v151 offset:2048\n"
    "v_mfma_f32_16x16x32_bf16 v[4:7], a[132:135], a[224:227], v[4:7]\n"
    "s_nop 1\n"
    "buffer_load_dwordx4 v240, s[4:7], s50 offen offset:3072 lds\n"
    "v_mfma_f32_16x16x32_bf16 v[16:19], a[136:139], a[224:227], v[16:19]\n"
    "s_nop 1\n"
    "ds_read_b64_tr_b16 a[160:161], v144 offset:256\n"
    "s_lshl_b32 s51, s50, 12\n"
    "v_mfma_f32_16x16x32_bf16 v[24:27], a[136:139], a[228:231], v[24:27]\n"
    "s_nop 1\n"
    "ds_read_b64_tr_b16 a[162:163], v144 offset:2304\n"
    "s_add_u32 m0, s36, 0x4000\n"
    "v_mfma_f32_16x16x32_bf16 v[28:31], a[140:143], a[228:231], v[28:31]\n"
    "s_nop 1\n"
    "ds_read_b64_tr_b16 a[164:165], v145 offset:256\n"
    "ds_read_b64_tr_b16 a[166:167], v145 offset:2304\n"
    "v_mfma_f32_16x16x32_bf16 v[20:23], a[140:143], a[224:227], v[20:23]\n"
    "s_nop 1\n"
    "s_waitcnt lgkmcnt(4)\n"
    "buffer_load_dwordx4 v233, s[8:11], s51 offen offset:0 lds\n"
    "v_mfma_f32_16x16x32_bf16 v[32:35], a[144:147], a[224:227], v[32:35]\n"
    "s_nop 1\n"
    "ds_read_b64_tr_b16 a[168:169], v146 offset:256\n"
    "ds_read_b64_tr_b16 a[170:171], v146 offset:2304\n"
    "v_mfma_f32_16x16x32_bf16 v[40:43], a[144:147], a[228:231], v[40:43]\n"
    "s_nop 1\n"
    "ds_read_b64_tr_b16 a[172:173], v147 offset:256\n"
    "v_mov_b32 v167, s76\n"
    "v_mfma_f32_16x16x32_bf16 v[44:47], a[148:151], a[228:231], v[44:47]\n"
    "s_nop 1\n"
    "ds_read_b64_tr_b16 a[174:175], v147 offset:2304\n"
    "ds_read_b64_tr_b16 a[176:177], v148 offset:256\n"
    "v_mfma_f32_16x16x32_bf16 v[36:39], a[148:151], a[224:227], v[36:39]\n"
    "s_nop 1\n"
    "buffer_load_dwordx4 v234, s[8:11], s51 offen offset:1024 lds\n"
    "v_mfma_f32_16x16x32_bf16 v[48:51], a[152:155], a[224:227], v[48:51]\n"
    "s_nop 1\n"
    "ds_read_b64_tr_b16 a[178:179], v148 offset:2304\n"
    "ds_read_b64_tr_b16 a[180:181], v149 offset:256\n"
    "v_mfma_f32_16x16x32_bf16 v[56:59], a[152:155], a[228:231], v[56:59]\n"
    "s_nop 1\n"
    "buffer_store_dword v167, v166, s[60:63], 0 offen offset:0 sc1\n"
    "v_mfma_f32_16x16x32_bf16 v[60:63], a[156:159], a[228:231], v[60:63]\n"
    "s_nop 1\n"
    "ds_read_b64_tr_b16 a[182:183], v149 offset:2304\n"
    "ds_read_b64_tr_b16 a[184:185], v150 offset:256\n"
    "v_mfma_f32_16x16x32_bf16 v[52:55], a[156:159], a[224:227], v[52:55]\n"
    "s_nop 1\n"
    "s_waitcnt lgkmcnt(5)\n"
    "buffer_load_dwordx4 v235, s[8:11], s51 offen offset:2048 lds\n"
    "v_mfma_f32_16x16x32_bf16 v[64:67], a[160:163], a[224:227], v[64:67]\n"
    "s_nop 1\n"
    "ds_read_b64_tr_b16 a[186:187], v150 offset:2304\n"
    "ds_read_b64_tr_b16 a[188:189], v151 offset:256\n"
    "v_mfma_f32_16x16x32_bf16 v[72:75], a[160:163], a[228:231], v[72:75]\n"
    "s_nop 1\n"
    "ds_read_b64_tr_b16 a[190:191], v151 offset:2304\n"
    "v_mfma_f32_16x16x32_bf16 v[76:79], a[164:167], a[228:231], v[76:79]\n"
    "s_nop 1\n"
    "ds_read_b64_tr_b16 a[128:129], v136 offset:16384\n"
    "ds_read_b64_tr_b16 a[130:131], v136 offset:18432\n"
    "v_mfma_f32_16x16x32_bf16 v[68:71], a[164:167], a[224:227], v[68:71]\n"
    "s_nop 1\n"
    "buffer_load_dwordx4 v236, s[8:11], s51 offen offset:3072 lds\n"
    "v_mfma_f32_16x16x32_bf16 v[80:83], a[168:171], a[224:227], v[80:83]\n"
    "s_nop 1\n"
    "ds_read_b64_tr_b16 a[132:133], v137 offset:16384\n"
    "s_add_u32 s51, s51, 0x40000\n"
    "v_mfma_f32_16x16x32_bf16 v[88:91], a[168:171], a[228:231], v[88:91]\n"
    "s_nop 1\n"
    "ds_read_b64_tr_b16 a[134:135], v137 offset:18432\n"
    "s_add_u32 m0, s36, 0x8000\n"
    "v_mfma_f32_16x16x32_bf16 v[92:95], a[172:175], a[228:231], v[92:95]\n"
    "s_nop 1\n"
    "ds_read_b64_tr_b16 a[136:137], v138 offset:16384\n"
    "ds_read_b64_tr_b16 a[138:139], v138 offset:18432\n"
    "v_mfma_f32_16x16x32_bf16 v[84:87], a[172:175], a[224:227], v[84:87]\n"
    "s_nop 1\n"
    "s_waitcnt lgkmcnt(6)\n"
    "buffer_load_dwordx4 v233, s[8:11], s51 offen offset:0 lds\n"
    "v_mfma_f32_16x16x32_bf16 v[96:99], a[176:179], a[224:227], v[96:99]\n"
    "s_nop 1\n"
    "ds_read_b64_tr_b16 a[140:141], v139 offset:16384\n"
    "ds_read_b64_tr_b16 a[142:143], v139 offset:18432\n"
    "v_mfma_f32_16x16x32_bf16 v[104:107], a[176:179], a[228:231], v[104:107]\n"
    "s_nop 1\n"
    "ds_read_b64_tr_b16 a[144:145], v140 offset:16384\n"
    "v_mfma_f32_16x16x32_bf16 v[108:111], a[180:183], a[228:231], v[108:111]\n"
    "s_nop 1\n"
    "ds_read_b64_tr_b16 a[146:147], v140 offset:18432\n"
    "ds_read_b64_tr_b16 a[148:149], v141 offset:16384\n"
    "v_mfma_f32_16x16x32_bf16 v[100:103], a[180:183], a[224:227], v[100:103]\n"
    "s_nop 1\n"
    "buffer_load_dwordx4 v234, s[8:11], s51 offen offset:1024 lds\n"
    "v_mfma_f32_16x16x32_bf16 v[112:115], a[184:187], a[224:227], v[112:115]\n"
    "s_nop 1\n"
    "ds_read_b64_tr_b16 a[150:151], v141 offset:18432\n"
    "ds_read_b64_tr_b16 a[152:153], v142 offset:16384\n"
    "v_mfma_f32_16x16x32_bf16 v[120:123], a[184:187], a[228:231], v[120:123]\n"
    "s_nop 1\n"
    "ds_read_b64_tr_b16 a[154:155], v142 offset:18432\n"
    "v_mfma_f32_16x16x32_bf16 v[124:127], a[188:191], a[228:231], v[124:127]\n"
    "s_nop 1\n"
    "ds_read_b64_tr_b16 a[156:157], v143 offset:16384\n"
    "ds_read_b64_tr_b16 a[158:159], v143 offset:18432\n"
    "v_mfma_f32_16x16x32_bf16 v[116:119], a[188:191], a[224:227], v[116:119]\n"
    "s_nop 1\n"
    "s_waitcnt lgkmcnt(8)\n"
    "buffer_load_dwordx4 v235, s[8:11], s51 offen offset:2048 lds\n"
    "v_mfma_f32_16x16x32_bf16 v[0:3], a[128:131], a[232:235], v[0:3]\n"
    "s_nop 1\n"
    "s_waitcnt vmcnt(16)\n"
    "s_barrier\n"
    "v_mfma_f32_16x16x32_bf16 v[8:11], a[128:131], a[236:239], v[8:11]\n"
    "s_nop 1\n"
    "ds_read_b64_tr_b16 a[160:161], v136 offset:16640\n"
    "v_mfma_f32_16x16x32_bf16 v[12:15], a[132:135], a[236:239], v[12:15]\n"
    "s_nop 1\n"
    "ds_read_b64_tr_b16 a[162:163], v136 offset:18688\n"
    "ds_read_b64_tr_b16 a[164:165], v137 offset:16640\n"
    "v_mfma_f32_16x16x32_bf16 v[4:7], a[132:135], a[232:235], v[4:7]\n"
    "s_nop 1\n"
    "buffer_load_dwordx4 v236, s[8:11], s51 offen offset:3072 lds\n"
    "v_mfma_f32_16x16x32_bf16 v[16:19], a[136:139], a[232:235], v[16:19]\n"
    "s_nop 1\n"
    "ds_read_b64_tr_b16 a[166:167], v137 offset:18688\n"
    "ds_read_b64_tr_b16 a[168:169], v138 offset:16640\n"
    "v_mfma_f32_16x16x32_bf16 v[24:27], a[136:139], a[236:239], v[24:27]\n"
    "s_nop 1\n"
    "ds_read_b64_tr_b16 a[170:171], v138 offset:18688\n"
    "s_add_u32 s50, s50, 0x80\n"
    "v_mfma_f32_16x16x32_bf16 v[28:31], a[140:143], a[236:239], v[28:31]\n"
    "s_nop 1\n"
    "ds_read_b64_tr_b16 a[172:173], v139 offset:16640\n"
    "s_cmp_eq_u32 s50, 0x380\n"
    "s_cselect_b32 s50, 0, s50\n"
    "v_mfma_f32_16x16x32_bf16 v[20:23], a[140:143], a[232:235], v[20:23]\n"
    "s_nop 1\n"
    "s_waitcnt lgkmcnt(7)\n"
    "ds_read_b64_tr_b16 a[174:175], v139 offset:18688\n"
    "s_add_u32 m0, s36, 0xc000\n"
    "v_mfma_f32_16x16x32_bf16 v[32:35], a[144:147], a[232:235], v[32:35]\n"
    "s_nop 1\n"
    "buffer_load_dwordx4 v237, s[4:7], s50 offen offset:0 lds\n"
    "v_mfma_f32_16x16x32_bf16 v[40:43], a[144:147], a[236:239], v[40:43]\n"
    "s_nop 1\n"
    "ds_read_b64_tr_b16 a[176:177], v140 offset:16640\n"
    "ds_read_b64_tr_b16 a[178:179], v140 offset:18688\n"
    "v_mfma_f32_16x16x32_bf16 v[44:47], a[148:151], a[236:239], v[44:47]\n"
    "s_nop 1\n"
    "ds_read_b64_tr_b16 a[180:181], v141 offset:16640\n"
    "v_mfma_f32_16x16x32_bf16 v[36:39], a[148:151], a[232:235], v[36:39]\n"
    "s_nop 1\n"
    "ds_read_b64_tr_b16 a[182:183], v141 offset:18688\n"
    "ds_read_b64_tr_b16 a[184:185], v142 offset:16640\n"
    "v_mfma_f32_16x16x32_bf16 v[48:51], a[152:155], a[232:235], v[48:51]\n"
    "s_nop 1\n"
    "buffer_load_dwordx4 v238, s[4:7], s50 offen offset:1024 lds\n"
    "v_mfma_f32_16x16x32_bf16 v[56:59], a[152:155], a[236:239], v[56:59]\n"
    "s_nop 1\n"
    "ds_read_b64_tr_b16 a[186:187], v142 offset:18688\n"
    "ds_read_b64_tr_b16 a[188:189], v143 offset:16640\n"
    "v_mfma_f32_16x16x32_bf16 v[60:63], a[156:159], a[236:239], v[60:63]\n"
    "s_nop 1\n"
    "ds_read_b64_tr_b16 a[190:191], v143 offset:18688\n"
    "v_mfma_f32_16x16x32_bf16 v[52:55], a[156:159], a[232:235], v[52:55]\n"
    "s_nop 1\n"
    "s_waitcnt lgkmcnt(8)\n"
    "ds_read_b128 a[240:243], v154 offset:49152\n"
    "v_mfma_f32_16x16x32_bf16 v[64:67], a[160:163], a[232:235], v[64:67]\n"
    "s_nop 1\n"
    "buffer_load_dwordx4 v239, s[4:7], s50 offen offset:2048 lds\n"
    "v_mfma_f32_16x16x32_bf16 v[72:75], a[160:163], a[236:239], v[72:75]\n"
    "s_nop 1\n"
    "ds_read_b64_tr_b16 a[128:129], v136 offset:32768\n"
    "ds_read_b64_tr_b16 a[130:131], v136 offset:34816\n"
    "v_mfma_f32_16x16x32_bf16 v[76:79], a[164:167], a[236:239], v[76:79]\n"
    "s_nop 1\n"
    "ds_read_b128 a[244:247], v154 offset:51200\n"
    "v_mfma_f32_16x16x32_bf16 v[68:71], a[164:167], a[232:235], v[68:71]\n"
    "s_nop 1\n"
    "ds_read_b64_tr_b16 a[132:133], v137 offset:32768\n"
    "ds_read_b64_tr_b16 a[134:135], v137 offset:34816\n"
    "v_mfma_f32_16x16x32_bf16 v[80:83], a[168:171], a[232:235], v[80:83]\n"
    "s_nop 1\n"
    "buffer_load_dwordx4 v240, s[4:7], s50 offen offset:3072 lds\n"
    "v_mfma_f32_16x16x32_bf16 v[88:91], a[168:171], a[236:239], v[88:91]\n"
    "s_nop 1\n"
    "ds_read_b64_tr_b16 a[136:137], v138 offset:32768\n"
    "s_lshl_b32 s51, s50, 12\n"
    "v_mfma_f32_16x16x32_bf16 v[92:95], a[172:175], a[236:239], v[92:95]\n"
    "s_nop 1\n"
    "ds_read_b64_tr_b16 a[138:139], v138 offset:34816\n"
    "s_add_u32 m0, s36, 0x24000\n"
    "v_mfma_f32_16x16x32_bf16 v[84:87], a[172:175], a[232:235], v[84:87]\n"
    "s_nop 1\n"
    "s_waitcnt lgkmcnt(8)\n"
    "ds_read_b128 a[248:251], v155 offset:49152\n"
    "v_mfma_f32_16x16x32_bf16 v[96:99], a[176:179], a[232:235], v[96:99]\n"
    "s_nop 1\n"
    "buffer_load_dwordx4 v233, s[8:11], s51 offen offset:0 lds\n"
    "v_mfma_f32_16x16x32_bf16 v[104:107], a[176:179], a[236:239], v[104:107]\n"
    "s_nop 1\n"
    "ds_read_b128 a[252:255], v155 offset:51200\n"
    "v_mfma_f32_16x16x32_bf16 v[108:111], a[180:183], a[236:239], v[108:111]\n"
    "s_nop 1\n"
    "ds_read_b64_tr_b16 a[140:141], v139 offset:32768\n"
    "v_mfma_f32_16x16x32_bf16 v[100:103], a[180:183], a[232:235], v[100:103]\n"
    "s_nop 1\n"
    "ds_read_b64_tr_b16 a[142:143], v139 offset:34816\n"
    "ds_read_b64_tr_b16 a[144:145], v140 offset:32768\n"
    "v_mfma_f32_16x16x32_bf16 v[112:115], a[184:187], a[232:235], v[112:115]\n"
    "s_nop 1\n"
    "buffer_load_dwordx4 v234, s[8:11], s51 offen offset:1024 lds\n"
    "v_mfma_f32_16x16x32_bf16 v[120:123], a[184:187], a[236:239], v[120:123]\n"
    "s_nop 1\n"
    "ds_read_b64_tr_b16 a[146:147], v140 offset:34816\n"
    "ds_read_b64_tr_b16 a[148:149], v141 offset:32768\n"
    "v_mfma_f32_16x16x32_bf16 v[124:127], a[188:191], a[236:239], v[124:127]\n"
    "s_nop 1\n"
    "ds_read_b64_tr_b16 a[150:151], v141 offset:34816\n"
    "v_mfma_f32_16x16x32_bf16 v[116:119], a[188:191], a[232:235], v[116:119]\n"
    "s_nop 1\n"
    "s_waitcnt lgkmcnt(4)\n"
    "ds_read_b64_tr_b16 a[152:153], v142 offset:32768\n"
    "v_mfma_f32_16x16x32_bf16 v[0:3], a[128:131], a[240:243], v[0:3]\n"
    "s_nop 0\n"
    "s_waitcnt vmcnt(19)\n"
    "s_barrier\n"
    "v_mfma_f32_16x16x32_bf16 v[8:11], a[128:131], a[244:247], v[8:11]\n"
    "s_nop 0\n"
    "buffer_load_dwordx4 v235, s[8:11], s51 offen offset:2048 lds\n"
    "v_mfma_f32_16x16x32_bf16 v[12:15], a[132:135], a[244:247], v[12:15]\n"
    "s_nop 0\n"
    "ds_read_b64_tr_b16 a[154:155], v142 offset:34816\n"
    "ds_read_b64_tr_b16 a[156:157], v143 offset:32768\n"
    "v_mfma_f32_16x16x32_bf16 v[4:7], a[132:135], a[240:243], v[4:7]\n"
    "s_nop 0\n"
    "buffer_load_dword v167, v165, s[60:63], 0 offen offset:0 sc1\n"
    "v_mfma_f32_16x16x32_bf16 v[16:19], a[136:139], a[240:243], v[16:19]\n"
    "s_nop 0\n"
    "ds_read_b64_tr_b16 a[158:159], v143 offset:34816\n"
    "ds_read_b64_tr_b16 a[160:161], v136 offset:33024\n"
    "v_mfma_f32_16x16x32_bf16 v[24:27], a[136:139], a[244:247], v[24:27]\n"
    "s_nop 0\n"
    "buffer_load_dwordx4 v236, s[8:11], s51 offen offset:3072 lds\n"
    "v_mfma_f32_16x16x32_bf16 v[28:31], a[140:143], a[244:247], v[28:31]\n"
    "s_nop 0\n"
    "ds_read_b64_tr_b16 a[162:163], v136 offset:35072\n"
    "s_add_u32 s51, s51, 0x40000\n"
    "v_mfma_f32_16x16x32_bf16 v[20:23], a[140:143], a[240:243], v[20:23]\n"
    "s_nop 0\n"
    "s_waitcnt lgkmcnt(2)\n"
    "ds_read_b64_tr_b16 a[164:165], v137 offset:33024\n"
    "s_add_u32 m0, s36, 0x20000\n"
    "v_mfma_f32_16x16x32_bf16 v[32:35], a[144:147], a[240:243], v[32:35]\n"
    "s_nop 0\n"
    "ds_read_b64_tr_b16 a[166:167], v137 offset:35072\n"
    "ds_read_b64_tr_b16 a[168:169], v138 offset:33024\n"
    "v_mfma_f32_16x16x32_bf16 v[40:43], a[144:147], a[244:247], v[40:43]\n"
    "s_nop 0\n"
    "buffer_load_dwordx4 v233, s[8:11], s51 offen offset:0 lds\n"
    "v_mfma_f32_16x16x32_bf16 v[44:47], a[148:151], a[244:247], v[44:47]\n"
    "s_nop 0\n"
    "ds_read_b64_tr_b16 a[170:171], v138 offset:35072\n"
    "ds_read_b64_tr_b16 a[172:173], v139 offset:33024\n"
    "v_mfma_f32_16x16x32_bf16 v[36:39], a[148:151], a[240:243], v[36:39]\n"
    "s_nop 0\n"
    "ds_read_b64_tr_b16 a[174:175], v139 offset:35072\n"
    "v_mfma_f32_16x16x32_bf16 v[48:51], a[152:155], a[240:243], v[48:51]\n"
    "s_nop 0\n"
    "ds_read_b64_tr_b16 a[176:177], v140 offset:33024\n"
    "ds_read_b64_tr_b16 a[178:179], v140 offset:35072\n"
    "v_mfma_f32_16x16x32_bf16 v[56:59], a[152:155], a[244:247], v[56:59]\n"
    "s_nop 0\n"
    "buffer_load_dwordx4 v234, s[8:11], s51 offen offset:1024 lds\n"
    "v_mfma_f32_16x16x32_bf16 v[60:63], a[156:159], a[244:247], v[60:63]\n"
    "s_nop 0\n"
    "ds_read_b64_tr_b16 a[180:181], v141 offset:33024\n"
    "ds_read_b64_tr_b16 a[182:183], v141 offset:35072\n"
    "v_mfma_f32_16x16x32_bf16 v[52:55], a[156:159], a[240:243], v[52:55]\n"
    "s_nop 0\n"
    "s_waitcnt lgkmcnt(4)\n"
    "ds_read_b64_tr_b16 a[184:185], v142 offset:33024\n"
    "v_mfma_f32_16x16x32_bf16 v[64:67], a[160:163], a[240:243], v[64:67]\n"
    "s_nop 0\n"
    "ds_read_b64_tr_b16 a[186:187], v142 offset:35072\n"
    "ds_read_b64_tr_b16 a[188:189], v143 offset:33024\n"
    "v_mfma_f32_16x16x32_bf16 v[72:75], a[160:163], a[244:247], v[72:75]\n"
    "s_nop 0\n"
    "buffer_load_dwordx4 v235, s[8:11], s51 offen offset:2048 lds\n"
    "v_mfma_f32_16x16x32_bf16 v[76:79], a[164:167], a[244:247], v[76:79]\n"
    "s_nop 0\n"
    "ds_read_b64_tr_b16 a[190:191], v143 offset:35072\n"
    "ds_read_b64_tr_b16 a[128:129], v128 offset:0\n"
    "v_mfma_f32_16x16x32_bf16 v[68:71], a[164:167], a[240:243], v[68:71]\n"
    "s_nop 0\n"
    "ds_read_b64_tr_b16 a[130:131], v128 offset:2048\n"
    "v_mfma_f32_16x16x32_bf16 v[80:83], a[168:171], a[240:243], v[80:83]\n"
    "s_nop 0\n"
    "ds_read_b64_tr_b16 a[132:133], v129 offset:0\n"
    "ds_read_b64_tr_b16 a[134:135], v129 offset:2048\n"
    "v_mfma_f32_16x16x32_bf16 v[88:91], a[168:171], a[244:247], v[88:91]\n"
    "s_nop 0\n"
    "buffer_load_dwordx4 v236, s[8:11], s51 offen offset:3072 lds\n"
    "v_mfma_f32_16x16x32_bf16 v[92:95], a[172:175], a[244:247], v[92:95]\n"
    "s_nop 0\n"
    "ds_read_b64_tr_b16 a[136:137], v130 offset:0\n"
    "ds_read_b64_tr_b16 a[138:139], v130 offset:2048\n"
    "v_mfma_f32_16x16x32_bf16 v[84:87], a[172:175], a[240:243], v[84:87]\n"
    "s_nop 0\n"
    "s_waitcnt lgkmcnt(6)\n"
    "ds_read_b64_tr_b16 a[140:141], v131 offset:0\n"
    "s_add_u32 s50, s50, 0x80\n"
    "v_mfma_f32_16x16x32_bf16 v[96:99], a[176:179], a[240:243], v[96:99]\n"
    "s_nop 0\n"
    "ds_read_b64_tr_b16 a[142:143], v131 offset:2048\n"
    "s_cmp_eq_u32 s50, 0x380\n"
    "s_cselect_b32 s50, 0, s50\n"
    "v_mfma_f32_16x16x32_bf16 v[104:107], a[176:179], a[244:247], v[104:107]\n"
    "s_nop 0\n"
    "ds_read_b64_tr_b16 a[144:145], v132 offset:0\n"
    "s_add_u32 m0, s36, 0x14000\n"
    "v_mfma_f32_16x16x32_bf16 v[108:111], a[180:183], a[244:247], v[108:111]\n"
    "s_nop 0\n"
    "buffer_load_dwordx4 v237, s[4:7], s50 offen offset:0 lds\n"
    "v_mfma_f32_16x16x32_bf16 v[100:103], a[180:183], a[240:243], v[100:103]\n"
    "s_nop 0\n"
    "ds_read_b64_tr_b16 a[146:147], v132 offset:2048\n"
    "ds_read_b64_tr_b16 a[148:149], v133 offset:0\n"
    "v_mfma_f32_16x16x32_bf16 v[112:115], a[184:187], a[240:243], v[112:115]\n"
    "s_nop 0\n"
    "ds_read_b64_tr_b16 a[150:151], v133 offset:2048\n"
    "v_mfma_f32_16x16x32_bf16 v[120:123], a[184:187], a[244:247], v[120:123]\n"
    "s_nop 0\n"
    "ds_read_b64_tr_b16 a[152:153], v134 offset:0\n"
    "ds_read_b64_tr_b16 a[154:155], v134 offset:2048\n"
    "v_mfma_f32_16x16x32_bf16 v[124:127], a[188:191], a[244:247], v[124:127]\n"
    "s_nop 0\n"
    "buffer_load_dwordx4 v238, s[4:7], s50 offen offset:1024 lds\n"
    "v_mfma_f32_16x16x32_bf16 v[116:119], a[188:191], a[240:243], v[116:119]\n"
    "s_nop 0\n"
    "s_waitcnt lgkmcnt(6)\n"
    "ds_read_b64_tr_b16 a[156:157], v135 offset:0\n"
    "ds_read_b64_tr_b16 a[158:159], v135 offset:2048\n"
    "v_mfma_f32_16x16x32_bf16 v[0:3], a[128:131], a[248:251], v[0:3]\n"
    "s_nop 0\n"
    "s_waitcnt vmcnt(19)\n"
    "s_barrier\n"
    "v_mfma_f32_16x16x32_bf16 v[8:11], a[128:131], a[252:255], v[8:11]\n"
    "s_nop 0\n"
    "ds_read_b64_tr_b16 a[160:161], v128 offset:256\n"
    "ds_read_b64_tr_b16 a[162:163], v128 offset:2304\n"
    "v_mfma_f32_16x16x32_bf16 v[12:15], a[132:135], a[252:255], v[12:15]\n"
    "s_nop 0\n"
    "buffer_load_dwordx4 v239, s[4:7], s50 offen offset:2048 lds\n"
    "v_mfma_f32_16x16x32_bf16 v[4:7], a[132:135], a[248:251], v[4:7]\n"
    "s_nop 0\n"
    "ds_read_b64_tr_b16 a[164:165], v129 offset:256\n"
    "ds_read_b64_tr_b16 a[166:167], v129 offset:2304\n"
    "v_mfma_f32_16x16x32_bf16 v[16:19], a[136:139], a[248:251], v[16:19]\n"
    "s_nop 0\n"
    "ds_read_b64_tr_b16 a[168:169], v130 offset:256\n"
    "v_mfma_f32_16x16x32_bf16 v[24:27], a[136:139], a[252:255], v[24:27]\n"
    "s_nop 0\n"
    "ds_read_b64_tr_b16 a[170:171], v130 offset:2304\n"
    "ds_read_b64_tr_b16 a[172:173], v131 offset:256\n"
    "v_mfma_f32_16x16x32_bf16 v[28:31], a[140:143], a[252:255], v[28:31]\n"
    "s_nop 0\n"
    "buffer_load_dwordx4 v240, s[4:7], s50 offen offset:3072 lds\n"
    "v_mfma_f32_16x16x32_bf16 v[20:23], a[140:143], a[248:251], v[20:23]\n"
    "s_nop 0\n"
    "s_waitcnt lgkmcnt(7)\n"
    "ds_read_b64_tr_b16 a[174:175], v131 offset:2304\n"
    "s_lshl_b32 s51, s50, 12\n"
    "v_mfma_f32_16x16x32_bf16 v[32:35], a[144:147], a[248:251], v[32:35]\n"
    "s_nop 0\n"
    "ds_read_b64_tr_b16 a[176:177], v132 offset:256\n"
    "s_add_u32 m0, s36, 0x18000\n"
    "v_mfma_f32_16x16x32_bf16 v[40:43], a[144:147], a[252:255], v[40:43]\n"
    "s_nop 0\n"
    "ds_read_b64_tr_b16 a[178:179], v132 offset:2304\n"
    "ds_read_b64_tr_b16 a[180:181], v133 offset:256\n"
    "v_mfma_f32_16x16x32_bf16 v[44:47], a[148:151], a[252:255], v[44:47]\n"
    "s_nop 0\n"
    "buffer_load_dwordx4 v233, s[8:11], s51 offen offset:0 lds\n"
    "v_mfma_f32_16x16x32_bf16 v[36:39], a[148:151], a[248:251], v[36:39]\n"
    "s_nop 0\n"
    "ds_read_b64_tr_b16 a[182:183], v133 offset:2304\n"
    "ds_read_b64_tr_b16 a[184:185], v134 offset:256\n"
    "v_mfma_f32_16x16x32_bf16 v[48:51], a[152:155], a[248:251], v[48:51]\n"
    "s_nop 0\n"
    "ds_read_b64_tr_b16 a[186:187], v134 offset:2304\n"
    "v_mfma_f32_16x16x32_bf16 v[56:59], a[152:155], a[252:255], v[56:59]\n"
    "s_nop 0\n"
    "ds_read_b64_tr_b16 a[188:189], v135 offset:256\n"
    "ds_read_b64_tr_b16 a[190:191], v135 offset:2304\n"
    "v_mfma_f32_16x16x32_bf16 v[60:63], a[156:159], a[252:255], v[60:63]\n"
    "s_nop 0\n"
    "buffer_load_dwordx4 v234, s[8:11], s51 offen offset:1024 lds\n"
    "v_mfma_f32_16x16x32_bf16 v[52:55], a[156:159], a[248:251], v[52:55]\n"
    "s_nop 0\n"
    "s_waitcnt lgkmcnt(8)\n"
    "ds_read_b128 a[192:195], v154 offset:0\n"
    "v_mfma_f32_16x16x32_bf16 v[64:67], a[160:163], a[248:251], v[64:67]\n"
    "s_nop 0\n"
    "ds_read_b64_tr_b16 a[128:129], v128 offset:16384\n"
    "v_mfma_f32_16x16x32_bf16 v[72:75], a[160:163], a[252:255], v[72:75]\n"
    "s_nop 0\n"
    "ds_read_b128 a[196:199], v154 offset:2048\n"
    "v_mfma_f32_16x16x32_bf16 v[76:79], a[164:167], a[252:255], v[76:79]\n"
    "s_nop 0\n"
    "buffer_load_dwordx4 v235, s[8:11], s51 offen offset:2048 lds\n"
    "v_mfma_f32_16x16x32_bf16 v[68:71], a[164:167], a[248:251], v[68:71]\n"
    "s_nop 0\n"
    "ds_read_b64_tr_b16 a[130:131], v128 offset:18432\n"
    "ds_read_b64_tr_b16 a[132:133], v129 offset:16384\n"
    "v_mfma_f32_16x16x32_bf16 v[80:83], a[168:171], a[248:251], v[80:83]\n"
    "s_nop 0\n"
    "ds_read_b64_tr_b16 a[134:135], v129 offset:18432\n"
    "v_mfma_f32_16x16x32_bf16 v[88:91], a[168:171], a[252:255], v[88:91]\n"
    "s_nop 0\n"
    "ds_read_b64_tr_b16 a[136:137], v130 offset:16384\n"
    "ds_read_b64_tr_b16 a[138:139], v130 offset:18432\n"
    "v_mfma_f32_16x16x32_bf16 v[92:95], a[172:175], a[252:255], v[92:95]\n"
    "s_nop 0\n"
    "buffer_load_dwordx4 v236, s[8:11], s51 offen offset:3072 lds\n"
    "v_mfma_f32_16x16x32_bf16 v[84:87], a[172:175], a[248:251], v[84:87]\n"
    "s_nop 0\n"
    "s_waitcnt lgkmcnt(8)\n"
    "ds_read_b128 a[200:203], v155 offset:0\n"
    "s_add_u32 s51, s51, 0x40000\n"
    "v_mfma_f32_16x16x32_bf16 v[96:99], a[176:179], a[248:251], v[96:99]\n"
    "s_nop 0\n"
    "ds_read_b128 a[204:207], v155 offset:2048\n"
    "s_add_u32 m0, s36, 0x1c000\n"
    "v_mfma_f32_16x16x32_bf16 v[104:107], a[176:179], a[252:255], v[104:107]\n"
    "s_nop 0\n"
    "ds_read_b64_tr_b16 a[140:141], v131 offset:16384\n"
    "ds_read_b64_tr_b16 a[142:143], v131 offset:18432\n"
    "v_mfma_f32_16x16x32_bf16 v[108:111], a[180:183], a[252:255], v[108:111]\n"
    "s_nop 0\n"
    "buffer_load_dwordx4 v233, s[8:11], s51 offen offset:0 lds\n"
    "v_mfma_f32_16x16x32_bf16 v[100:103], a[180:183], a[248:251], v[100:103]\n"
    "s_nop 0\n"
    "ds_read_b64_tr_b16 a[144:145], v132 offset:16384\n"
    "ds_read_b64_tr_b16 a[146:147], v132 offset:18432\n"
    "v_mfma_f32_16x16x32_bf16 v[112:115], a[184:187], a[248:251], v[112:115]\n"
    "s_nop 0\n"
    "ds_read_b64_tr_b16 a[148:149], v133 offset:16384\n"
    "v_mfma_f32_16x16x32_bf16 v[120:123], a[184:187], a[252:255], v[120:123]\n"
    "s_nop 0\n"
    "ds_read_b64_tr_b16 a[150:151], v133 offset:18432\n"
    "ds_read_b64_tr_b16 a[152:153], v134 offset:16384\n"
    "v_mfma_f32_16x16x32_bf16 v[124:127], a[188:191], a[252:255], v[124:127]\n"
    "s_nop 0\n"
    "buffer_load_dwordx4 v234, s[8:11], s51 offen offset:1024 lds\n"
    "v_mfma_f32_16x16x32_bf16 v[116:119], a[188:191], a[248:251], v[116:119]\n"
    "s_nop 0\n"
    "s_waitcnt lgkmcnt(5)\n"
    "ds_read_b64_tr_b16 a[154:155], v134 offset:18432\n"
    "ds_read_b64_tr_b16 a[156:157], v135 offset:16384\n"
    "v_mfma_f32_16x16x32_bf16 v[0:3], a[128:131], a[192:195], v[0:3]\n"
    "s_waitcnt vmcnt(23)\n"
    "s_barrier\n"
    "v_mfma_f32_16x16x32_bf16 v[8:11], a[128:131], a[196:199], v[8:11]\n"
    "ds_read_b64_tr_b16 a[158:159], v135 offset:18432\n"
    "v_mfma_f32_16x16x32_bf16 v[12:15], a[132:135], a[196:199], v[12:15]\n"
    "buffer_load_dwordx4 v235, s[8:11], s51 offen offset:2048 lds\n"
    "v_mfma_f32_16x16x32_bf16 v[4:7], a[132:135], a[192:195], v[4:7]\n"
    "ds_read_b64_tr_b16 a[160:161], v128 offset:16640\n"
    "v_mfma_f32_16x16x32_bf16 v[16:19], a[136:139], a[192:195], v[16:19]\n"
    "ds_read_b64_tr_b16 a[162:163], v128 offset:18688\n"
    "v_mfma_f32_16x16x32_bf16 v[24:27], a[136:139], a[196:199], v[24:27]\n"
    "ds_read_b64_tr_b16 a[164:165], v129 offset:16640\n"
    "v_mfma_f32_16x16x32_bf16 v[28:31], a[140:143], a[196:199], v[28:31]\n"
    "s_waitcnt vmcnt(16)\n"
    "buffer_load_dwordx4 v236, s[8:11], s51 offen offset:3072 lds\n"
    "v_mfma_f32_16x16x32_bf16 v[20:23], a[140:143], a[192:195], v[20:23]\n"
    "s_waitcnt lgkmcnt(3)\n"
    "ds_read_b64_tr_b16 a[166:167], v129 offset:18688\n"
    "ds_read_b64_tr_b16 a[168:169], v130 offset:16640\n"
    "v_mfma_f32_16x16x32_bf16 v[32:35], a[144:147], a[192:195], v[32:35]\n"
    "ds_read_b64_tr_b16 a[170:171], v130 offset:18688\n"
    "v_readfirstlane_b32 s78, v167\n"
    "v_mfma_f32_16x16x32_bf16 v[40:43], a[144:147], a[196:199], v[40:43]\n"
    "ds_read_b64_tr_b16 a[172:173], v131 offset:16640\n"
    "s_mov_b32 s41, 0x0\n"
    "v_mfma_f32_16x16x32_bf16 v[44:47], a[148:151], a[196:199], v[44:47]\n"
    "ds_read_b64_tr_b16 a[174:175], v131 offset:18688\n"
    "s_add_u32 m0, s36, 0x0\n"
    "v_mfma_f32_16x16x32_bf16 v[36:39], a[148:151], a[192:195], v[36:39]\n"
    "buffer_load_dwordx4 v200, s[80:83], s41 offen offset:0 nt lds\n"
    "v_mfma_f32_16x16x32_bf16 v[48:51], a[152:155], a[192:195], v[48:51]\n"
    "ds_read_b64_tr_b16 a[176:177], v132 offset:16640\n"
    "s_sub_u32 s78, s78, s76\n"
    "s_add_u32 s78, s78, 400\n"
    "s_cmp_gt_u32 s78, 404\n"
    "s_cbranch_scc1 LPS%=\n"
    "LPB%=:\n"
    "v_mfma_f32_16x16x32_bf16 v[56:59], a[152:155], a[196:199], v[56:59]\n"
    "ds_read_b64_tr_b16 a[178:179], v132 offset:18688\n"
    "ds_read_b64_tr_b16 a[180:181], v133 offset:16640\n"
    "v_mfma_f32_16x16x32_bf16 v[60:63], a[156:159], a[196:199], v[60:63]\n"
    "buffer_load_dwordx4 v201, s[80:83], s41 offen offset:1024 nt lds\n"
    "v_mfma_f32_16x16x32_bf16 v[52:55], a[156:159], a[192:195], v[52:55]\n"
    "s_waitcnt lgkmcnt(3)\n"
    "ds_read_b64_tr_b16 a[182:183], v133 offset:18688\n"
    "v_mfma_f32_16x16x32_bf16 v[64:67], a[160:163], a[192:195], v[64:67]\n"
    "ds_read_b64_tr_b16 a[184:185], v134 offset:16640\n"
    "v_mfma_f32_16x16x32_bf16 v[72:75], a[160:163], a[196:199], v[72:75]\n"
    "ds_read_b64_tr_b16 a[186:187], v134 offset:18688\n"
    "ds_read_b64_tr_b16 a[188:189], v135 offset:16640\n"
    "v_mfma_f32_16x16x32_bf16 v[76:79], a[164:167], a[196:199], v[76:79]\n"
    "buffer_load_dwordx4 v202, s[80:83], s41 offen offset:2048 nt lds\n"
    "v_mfma_f32_16x16x32_bf16 v[68:71], a[164:167], a[192:195], v[68:71]\n"
    "ds_read_b64_tr_b16 a[190:191], v135 offset:18688\n"
    "ds_read_b64_tr_b16 a[128:129], v128 offset:32768\n"
    "v_mfma_f32_16x16x32_bf16 v[80:83], a[168:171], a[192:195], v[80:83]\n"
    "ds_read_b64_tr_b16 a[130:131], v128 offset:34816\n"
    "v_mfma_f32_16x16x32_bf16 v[88:91], a[168:171], a[196:199], v[88:91]\n"
    "ds_read_b64_tr_b16 a[132:133], v129 offset:32768\n"
    "v_mfma_f32_16x16x32_bf16 v[92:95], a[172:175], a[196:199], v[92:95]\n"
    "ds_read_b64_tr_b16 a[134:135], v129 offset:34816\n"
    "v_mfma_f32_16x16x32_bf16 v[84:87], a[172:175], a[192:195], v[84:87]\n"
    "s_waitcnt lgkmcnt(4)\n"
    "ds_read_b64_tr_b16 a[136:137], v130 offset:32768\n"
    "v_mfma_f32_16x16x32_bf16 v[96:99], a[176:179], a[192:195], v[96:99]\n"
    "ds_read_b64_tr_b16 a[138:139], v130 offset:34816\n"
    "ds_read_b64_tr_b16 a[140:141], v131 offset:32768\n"
    "v_mfma_f32_16x16x32_bf16 v[104:107], a[176:179], a[196:199], v[104:107]\n"
    "buffer_load_dwordx4 v203, s[80:83], s41 offen offset:3072 nt lds\n"
    "v_mfma_f32_16x16x32_bf16 v[108:111], a[180:183], a[196:199], v[108:111]\n"
    "ds_read_b64_tr_b16 a[142:143], v131 offset:34816\n"
    "ds_read_b64_tr_b16 a[144:145], v132 offset:32768\n"
    "v_mfma_f32_16x16x32_bf16 v[100:103], a[180:183], a[192:195], v[100:103]\n"
    "ds_read_b64_tr_b16 a[146:147], v132 offset:34816\n"
    "v_mfma_f32_16x16x32_bf16 v[112:115], a[184:187], a[192:195], v[112:115]\n"
    "ds_read_b64_tr_b16 a[148:149], v133 offset:32768\n"
    "v_mfma_f32_16x16x32_bf16 v[120:123], a[184:187], a[196:199], v[120:123]\n"
    "ds_read_b64_tr_b16 a[150:151], v133 offset:34816\n"
    "v_mfma_f32_16x16x32_bf16 v[124:127], a[188:191], a[196:199], v[124:127]\n"
    "ds_read_b64_tr_b16 a[152:153], v134 offset:32768\n"
    "v_mfma_f32_16x16x32_bf16 v[116:119], a[188:191], a[192:195], v[116:119]\n"
    "s_waitcnt lgkmcnt(5)\n"
    "ds_read_b64_tr_b16 a[154:155], v134 offset:34816\n"
    "v_mfma_f32_16x16x32_bf16 v[0:3], a[128:131], a[200:203], v[0:3]\n"
    "s_waitcnt vmcnt(20)\n"
    "s_barrier\n"
    "v_mfma_f32_16x16x32_bf16 v[8:11], a[128:131], a[204:207], v[8:11]\n"
    "ds_read_b64_tr_b16 a[156:157], v135 offset:32768\n"
    "s_mov_b32 s41, 0x4000\n"
    "v_mfma_f32_16x16x32_bf16 v[12:15], a[132:135], a[204:207], v[12:15]\n"
    "ds_read_b64_tr_b16 a[158:159], v135 offset:34816\n"
    "s_add_u32 m0, s36, 0x4000\n"
    "v_mfma_f32_16x16x32_bf16 v[4:7], a[132:135], a[200:203], v[4:7]\n"
    "ds_read_b64_tr_b16 a[160:161], v128 offset:33024\n"
    "v_mfma_f32_16x16x32_bf16 v[16:19], a[136:139], a[200:203], v[16:19]\n"
    "ds_read_b64_tr_b16 a[162:163], v128 offset:35072\n"
    "v_mfma_f32_16x16x32_bf16 v[24:27], a[136:139], a[204:207], v[24:27]\n"
    "ds_read_b64_tr_b16 a[164:165], v129 offset:33024\n"
    "v_mfma_f32_16x16x32_bf16 v[28:31], a[140:143], a[204:207], v[28:31]\n"
    "ds_read_b64_tr_b16 a[166:167], v129 offset:35072\n"
    "v_mfma_f32_16x16x32_bf16 v[20:23], a[140:143], a[200:203], v[20:23]\n"
    "s_waitcnt lgkmcnt(4)\n"
    "ds_read_b64_tr_b16 a[168:169], v130 offset:33024\n"
    "v_mfma_f32_16x16x32_bf16 v[32:35], a[144:147], a[200:203], v[32:35]\n"
    "ds_read_b64_tr_b16 a[170:171], v130 offset:35072\n"
    "v_mfma_f32_16x16x32_bf16 v[40:43], a[144:147], a[204:207], v[40:43]\n"
    "ds_read_b64_tr_b16 a[172:173], v131 offset:33024\n"
    "v_mfma_f32_16x16x32_bf16 v[44:47], a[148:151], a[204:207], v[44:47]\n"
    "ds_read_b64_tr_b16 a[174:175], v131 offset:35072\n"
    "v_mfma_f32_16x16x32_bf16 v[36:39], a[148:151], a[200:203], v[36:39]\n"
    "ds_read_b64_tr_b16 a[176:177], v132 offset:33024\n"
    "v_mfma_f32_16x16x32_bf16 v[48:51], a[152:155], a[200:203], v[48:51]\n"
    "ds_read_b64_tr_b16 a[178:179], v132 offset:35072\n"
    "v_readlane_b32 s56, v162, 32\n"
    "v_mfma_f32_16x16x32_bf16 v[56:59], a[152:155], a[204:207], v[56:59]\n"
    "ds_read_b64_tr_b16 a[180:181], v133 offset:33024\n"
    "ds_read_b64_tr_b16 a[182:183], v133 offset:35072\n"
    "v_mfma_f32_16x16x32_bf16 v[60:63], a[156:159], a[204:207], v[60:63]\n"
    "buffer_load_dwordx4 v200, s[80:83], s41 offen offset:0 nt lds\n"
    "v_mfma_f32_16x16x32_bf16 v[52:55], a[156:159], a[200:203], v[52:55]\n"
    "s_waitcnt lgkmcnt(4)\n"
    "ds_read_b64_tr_b16 a[184:185], v134 offset:33024\n"
    "ds_read_b64_tr_b16 a[186:187], v134 offset:35072\n"
    "v_mfma_f32_16x16x32_bf16 v[64:67], a[160:163], a[200:203], v[64:67]\n"
    "ds_read_b64_tr_b16 a[188:189], v135 offset:33024\n"
    "s_add_u32 s56, s56, 1\n"
    "v_mfma_f32_16x16x32_bf16 v[72:75], a[160:163], a[204:207], v[72:75]\n"
    "ds_read_b64_tr_b16 a[190:191], v135 offset:35072\n"
    "v_cmp_eq_u32 vcc, s56, v162\n"
    "v_mfma_f32_16x16x32_bf16 v[76:79], a[164:167], a[204:207], v[76:79]\n"
    "ds_read_b128 a[208:211], v152 offset:49152\n"
    "v_mfma_f32_16x16x32_bf16 v[68:71], a[164:167], a[200:203], v[68:71]\n"
    "ds_read_b64_tr_b16 a[128:129], v144 offset:16384\n"
    "s_cbranch_vccnz LNON%=\n"
    "LNOB%=:\n"
    "v_mfma_f32_16x16x32_bf16 v[80:83], a[168:171], a[200:203], v[80:83]\n"
    "buffer_load_dwordx4 v201, s[80:83], s41 offen offset:1024 nt lds\n"
    "v_mfma_f32_16x16x32_bf16 v[88:91], a[168:171], a[204:207], v[88:91]\n"
    "ds_read_b128 a[212:215], v152 offset:51200\n"
    "v_mov_b32 v162, s56\n"
    "v_mfma_f32_16x16x32_bf16 v[92:95], a[172:175], a[204:207], v[92:95]\n"
    "ds_read_b64_tr_b16 a[130:131], v144 offset:18432\n"
    "ds_read_b64_tr_b16 a[132:133], v145 offset:16384\n"
    "v_mfma_f32_16x16x32_bf16 v[84:87], a[172:175], a[200:203], v[84:87]\n"
    "s_waitcnt lgkmcnt(5)\n"
    "ds_read_b64_tr_b16 a[134:135], v145 offset:18432\n"
    "v_mfma_f32_16x16x32_bf16 v[96:99], a[176:179], a[200:203], v[96:99]\n"
    "ds_read_b64_tr_b16 a[136:137], v146 offset:16384\n"
    "v_mfma_f32_16x16x32_bf16 v[104:107], a[176:179], a[204:207], v[104:107]\n"
    "ds_read_b64_tr_b16 a[138:139], v146 offset:18432\n"
    "v_mfma_f32_16x16x32_bf16 v[108:111], a[180:183], a[204:207], v[108:111]\n"
    "ds_read_b128 a[216:219], v153 offset:49152\n"
    "v_mfma_f32_16x16x32_bf16 v[100:103], a[180:183], a[200:203], v[100:103]\n"
    "ds_read_b128 a[220:223], v153 offset:51200\n"
    "v_mfma_f32_16x16x32_bf16 v[112:115], a[184:187], a[200:203], v[112:115]\n"
    "buffer_load_dwordx4 v202, s[80:83], s41 offen offset:2048 nt lds\n"
    "v_mfma_f32_16x16x32_bf16 v[120:123], a[184:187], a[204:207], v[120:123]\n"
    "ds_read_b64_tr_b16 a[140:141], v147 offset:16384\n"
    "ds_read_b64_tr_b16 a[142:143], v147 offset:18432\n"
    "v_mfma_f32_16x16x32_bf16 v[124:127], a[188:191], a[204:207], v[124:127]\n"
    "ds_read_b64_tr_b16 a[144:145], v148 offset:16384\n"
    "ds_read_b64_tr_b16 a[146:147], v148 offset:18432\n"
    "v_mfma_f32_16x16x32_bf16 v[116:119], a[188:191], a[200:203], v[116:119]\n"
    "s_waitcnt lgkmcnt(2)\n"
    "ds_read_b64_tr_b16 a[148:149], v149 offset:16384\n"
    "v_mfma_f32_16x16x32_bf16 v[0:3], a[128:131], a[208:211], v[0:3]\n"
    "s_waitcnt vmcnt(7)\n"
    "s_barrier\n"
    "v_mfma_f32_16x16x32_bf16 v[8:11], a[128:131], a[212:215], v[8:11]\n"
    "ds_read_b64_tr_b16 a[150:151], v149 offset:18432\n"
    "v_mfma_f32_16x16x32_bf16 v[12:15], a[132:135], a[212:215], v[12:15]\n"
    "ds_read_b64_tr_b16 a[152:153], v150 offset:16384\n"
    "v_mfma_f32_16x16x32_bf16 v[4:7], a[132:135], a[208:211], v[4:7]\n"
    "ds_read_b64_tr_b16 a[154:155], v150 offset:18432\n"
    "ds_read_b64_tr_b16 a[156:157], v151 offset:16384\n"
    "v_mfma_f32_16x16x32_bf16 v[16:19], a[136:139], a[208:211], v[16:19]\n"
    "buffer_load_dwordx4 v203, s[80:83], s41 offen offset:3072 nt lds\n"
    "v_mfma_f32_16x16x32_bf16 v[24:27], a[136:139], a[212:215], v[24:27]\n"
    "ds_read_b64_tr_b16 a[158:159], v151 offset:18432\n"
    "ds_read_b64_tr_b16 a[160:161], v144 offset:16640\n"
    "v_mfma_f32_16x16x32_bf16 v[28:31], a[140:143], a[212:215], v[28:31]\n"
    "ds_read_b64_tr_b16 a[162:163], v144 offset:18688\n"
    "ds_read_b64_tr_b16 a[164:165], v145 offset:16640\n"
    "v_mfma_f32_16x16x32_bf16 v[20:23], a[140:143], a[208:211], v[20:23]\n"
    "s_waitcnt lgkmcnt(3)\n"
    "buffer_store_dword v162, v163, s[52:55], 0 offen offset:0 sc1\n"
    "v_mfma_f32_16x16x32_bf16 v[32:35], a[144:147], a[208:211], v[32:35]\n"
    "ds_read_b64_tr_b16 a[166:167], v145 offset:18688\n"
    "ds_read_b64_tr_b16 a[168:169], v146 offset:16640\n"
    "v_mfma_f32_16x16x32_bf16 v[40:43], a[144:147], a[212:215], v[40:43]\n"
    "ds_read_b64_tr_b16 a[170:171], v146 offset:18688\n"
    "s_mov_b32 s41, 0x0\n"
    "v_mfma_f32_16x16x32_bf16 v[44:47], a[148:151], a[212:215], v[44:47]\n"
    "ds_read_b64_tr_b16 a[172:173], v147 offset:16640\n"
    "s_add_u32 m0, s36, 0x10000\n"
    "v_mfma_f32_16x16x32_bf16 v[36:39], a[148:151], a[208:211], v[36:39]\n"
    "ds_read_b64_tr_b16 a[174:175], v147 offset:18688\n"
    "v_mfma_f32_16x16x32_bf16 v[48:51], a[152:155], a[208:211], v[48:51]\n"
    "ds_read_b64_tr_b16 a[176:177], v148 offset:16640\n"
    "v_mfma_f32_16x16x32_bf16 v[56:59], a[152:155], a[212:215], v[56:59]\n"
    "ds_read_b64_tr_b16 a[178:179], v148 offset:18688\n"
    "v_mfma_f32_16x16x32_bf16 v[60:63], a[156:159], a[212:215], v[60:63]\n"
    "ds_read_b64_tr_b16 a[180:181], v149 offset:16640\n"
    "v_mfma_f32_16x16x32_bf16 v[52:55], a[156:159], a[208:211], v[52:55]\n"
    "s_waitcnt lgkmcnt(3)\n"
    "ds_read_b64_tr_b16 a[182:183], v149 offset:18688\n"
    "v_mfma_f32_16x16x32_bf16 v[64:67], a[160:163], a[208:211], v[64:67]\n"
    "ds_read_b64_tr_b16 a[184:185], v150 offset:16640\n"
    "v_mfma_f32_16x16x32_bf16 v[72:75], a[160:163], a[212:215], v[72:75]\n"
    "ds_read_b64_tr_b16 a[186:187], v150 offset:18688\n"
    "v_mfma_f32_16x16x32_bf16 v[76:79], a[164:167], a[212:215], v[76:79]\n"
    "ds_read_b64_tr_b16 a[188:189], v151 offset:16640\n"
    "v_mfma_f32_16x16x32_bf16 v[68:71], a[164:167], a[208:211], v[68:71]\n"
    "ds_read_b64_tr_b16 a[190:191], v151 offset:18688\n"
    "v_mfma_f32_16x16x32_bf16 v[80:83], a[168:171], a[208:211], v[80:83]\n"
    "ds_read_b64_tr_b16 a[128:129], v144 offset:0\n"
    "v_mfma_f32_16x16x32_bf16 v[88:91], a[168:171], a[212:215], v[88:91]\n"
    "ds_read_b128 a[224:227], v154 offset:16384\n"
    "v_mfma_f32_16x16x32_bf16 v[92:95], a[172:175], a[212:215], v[92:95]\n"
    "buffer_load_dwordx4 v204, s[84:87], s41 offen offset:0 nt lds\n"
    "v_mfma_f32_16x16x32_bf16 v[84:87], a[172:175], a[208:211], v[84:87]\n"
    "s_waitcnt lgkmcnt(2)\n"
    "ds_read_b64_tr_b16 a[130:131], v144 offset:2048\n"
    "ds_read_b64_tr_b16 a[132:133], v136 offset:32768\n"
    "v_mfma_f32_16x16x32_bf16 v[96:99], a[176:179], a[208:211], v[96:99]\n"
    "ds_read_b128 a[228:231], v154 offset:18432\n"
    "v_mfma_f32_16x16x32_bf16 v[104:107], a[176:179], a[212:215], v[104:107]\n"
    "ds_read_b128 a[232:235], v155 offset:16384\n"
    "v_mfma_f32_16x16x32_bf16 v[108:111], a[180:183], a[212:215], v[108:111]\n"
    "ds_read_b64_tr_b16 a[134:135], v136 offset:34816\n"
    "v_mfma_f32_16x16x32_bf16 v[100:103], a[180:183], a[208:211], v[100:103]\n"
    "ds_read_b64_tr_b16 a[136:137], v136 offset:49152\n"
    "ds_read_b64_tr_b16 a[138:139], v136 offset:51200\n"
    "v_mfma_f32_16x16x32_bf16 v[112:115], a[184:187], a[208:211], v[112:115]\n"
    "buffer_load_dwordx4 v205, s[84:87], s41 offen offset:1024 nt lds\n"
    "v_mfma_f32_16x16x32_bf16 v[120:123], a[184:187], a[212:215], v[120:123]\n"
    "ds_read_b128 a[236:239], v155 offset:18432\n"
    "v_mfma_f32_16x16x32_bf16 v[124:127], a[188:191], a[212:215], v[124:127]\n"
    "ds_read_b64_tr_b16 a[140:141], v145 offset:0\n"
    "v_mfma_f32_16x16x32_bf16 v[116:119], a[188:191], a[208:211], v[116:119]\n"
    "s_waitcnt lgkmcnt(0)\n"
    "ds_read_b64_tr_b16 a[142:143], v145 offset:2048\n"
    "v_mfma_f32_16x16x32_bf16 v[0:3], a[128:131], a[216:219], v[0:3]\n"
    "s_barrier\n"
    "v_mfma_f32_16x16x32_bf16 v[0:3], a[132:135], a[224:227], v[0:3]\n"
    "ds_read_b64_tr_b16 a[144:145], v137 offset:32768\n"
    "v_mfma_f32_16x16x32_bf16 v[0:3], a[136:139], a[232:235], v[0:3]\n"
    "ds_read_b64_tr_b16 a[146:147], v137 offset:34816\n"
    "v_mfma_f32_16x16x32_bf16 v[8:11], a[128:131], a[220:223], v[8:11]\n"
    "ds_read_b64_tr_b16 a[148:149], v137 offset:49152\n"
    "ds_read_b64_tr_b16 a[150:151], v137 offset:51200\n"
    "v_mfma_f32_16x16x32_bf16 v[8:11], a[132:135], a[228:231], v[8:11]\n"
    "buffer_load_dwordx4 v206, s[84:87], s41 offen offset:2048 nt lds\n"
    "v_mfma_f32_16x16x32_bf16 v[8:11], a[136:139], a[236:239], v[8:11]\n"
    "ds_read_b64_tr_b16 a[152:153], v146 offset:0\n"
    "ds_read_b64_tr_b16 a[154:155], v146 offset:2048\n"
    "s_waitcnt lgkmcnt(6)\n"
    "v_mfma_f32_16x16x32_bf16 v[4:7], a[140:143], a[216:219], v[4:7]\n"
    "ds_read_b64_tr_b16 a[156:157], v138 offset:32768\n"
    "v_mul_f32 v0, s66, v0\n"
    "s_waitcnt lgkmcnt(5)\n"
    "v_mfma_f32_16x16x32_bf16 v[4:7], a[144:147], a[224:227], v[4:7]\n"
    "s_waitcnt lgkmcnt(0)\n"
    "ds_read_b64_tr_b16 a[158:159], v138 offset:34816\n"
    "v_mul_f32 v1, s66, v1\n"
    "v_mfma_f32_16x16x32_bf16 v[4:7], a[148:151], a[232:235], v[4:7]\n"
    "ds_read_b64_tr_b16 a[160:161], v138 offset:49152\n"
    "v_mul_f32 v2, s66, v2\n"
    "v_mfma_f32_16x16x32_bf16 v[12:15], a[140:143], a[220:223], v[12:15]\n"
    "ds_read_b64_tr_b16 a[162:163], v138 offset:51200\n"
    "v_mul_f32 v3, s66, v3\n"
    "v_mfma_f32_16x16x32_bf16 v[12:15], a[144:147], a[228:231], v[12:15]\n"
    "ds_read_b64_tr_b16 a[164:165], v147 offset:0\n"
    "v_mul_f32 v8, s66, v8\n"
    "v_mfma_f32_16x16x32_bf16 v[12:15], a[148:151], a[236:239], v[12:15]\n"
    "ds_read_b64_tr_b16 a[166:167], v147 offset:2048\n"
    "ds_read_b64_tr_b16 a[168:169], v139 offset:32768\n"
    "v_mfma_f32_16x16x32_bf16 v[16:19], a[152:155], a[216:219], v[16:19]\n"
    "buffer_load_dwordx4 v207, s[84:87], s41 offen offset:3072 nt lds\n"
    "s_waitcnt lgkmcnt(5)\n"
    "v_mfma_f32_16x16x32_bf16 v[16:19], a[156:159], a[224:227], v[16:19]\n"
    "ds_read_b64_tr_b16 a[170:171], v139 offset:34816\n"
    "ds_read_b64_tr_b16 a[172:173], v139 offset:49152\n"
    "s_waitcnt lgkmcnt(5)\n"
    "v_mfma_f32_16x16x32_bf16 v[16:19], a[160:163], a[232:235], v[16:19]\n"
    "ds_read_b64_tr_b16 a[174:175], v139 offset:51200\n"
    "s_mov_b32 s41, 0x4000\n"
    "v_mfma_f32_16x16x32_bf16 v[24:27], a[152:155], a[220:223], v[24:27]\n"
    "s_waitcnt lgkmcnt(0)\n"
    "ds_read_b64_tr_b16 a[176:177], v148 offset:0\n"
    "s_add_u32 m0, s36, 0x14000\n"
    "v_mfma_f32_16x16x32_bf16 v[24:27], a[156:159], a[228:231], v[24:27]\n"
    "ds_read_b64_tr_b16 a[178:179], v148 offset:2048\n"
    "v_mul_f32 v9, s66, v9\n"
    "v_mfma_f32_16x16x32_bf16 v[24:27], a[160:163], a[236:239], v[24:27]\n"
    "ds_read_b64_tr_b16 a[180:181], v140 offset:32768\n"
    "v_mul_f32 v10, s66, v10\n"
    "v_mfma_f32_16x16x32_bf16 v[20:23], a[164:167], a[216:219], v[20:23]\n"
    "ds_read_b64_tr_b16 a[182:183], v140 offset:34816\n"
    "v_mul_f32 v11, s66, v11\n"
    "v_mfma_f32_16x16x32_bf16 v[20:23], a[168:171], a[224:227], v[20:23]\n"
    "ds_read_b64_tr_b16 a[184:185], v140 offset:49152\n"
    "v_mul_f32 v4, s66, v4\n"
    "v_mfma_f32_16x16x32_bf16 v[20:23], a[172:175], a[232:235], v[20:23]\n"
    "ds_read_b64_tr_b16 a[186:187], v140 offset:51200\n"
    "v_mul_f32 v5, s66, v5\n"
    "v_mfma_f32_16x16x32_bf16 v[28:31], a[164:167], a[220:223], v[28:31]\n"
    "ds_read_b64_tr_b16 a[188:189], v149 offset:0\n"
    "v_mul_f32 v6, s66, v6\n"
    "v_mfma_f32_16x16x32_bf16 v[28:31], a[168:171], a[228:231], v[28:31]\n"
    "ds_read_b64_tr_b16 a[190:191], v149 offset:2048\n"
    "v_mul_f32 v7, s66, v7\n"
    "v_mfma_f32_16x16x32_bf16 v[28:31], a[172:175], a[236:239], v[28:31]\n"
    "s_waitcnt lgkmcnt(0)\n"
    "ds_read_b64_tr_b16 a[128:129], v141 offset:32768\n"
    "v_mul_f32 v12, s66, v12\n"
    "v_mfma_f32_16x16x32_bf16 v[32:35], a[176:179], a[216:219], v[32:35]\n"
    "ds_read_b64_tr_b16 a[130:131], v141 offset:34816\n"
    "v_mul_f32 v13, s66, v13\n"
    "v_mfma_f32_16x16x32_bf16 v[32:35], a[180:183], a[224:227], v[32:35]\n"
    "ds_read_b64_tr_b16 a[132:133], v141 offset:49152\n"
    "v_mul_f32 v14, s66, v14\n"
    "v_mfma_f32_16x16x32_bf16 v[32:35], a[184:187], a[232:235], v[32:35]\n"
    "ds_read_b64_tr_b16 a[134:135], v141 offset:51200\n"
    "v_mul_f32 v15, s66, v15\n"
    "v_mfma_f32_16x16x32_bf16 v[40:43], a[176:179], a[220:223], v[40:43]\n"
    "ds_read_b64_tr_b16 a[136:137], v150 offset:0\n"
    "v_mul_f32 v16, s66, v16\n"
    "v_mfma_f32_16x16x32_bf16 v[40:43], a[180:183], a[228:231], v[40:43]\n"
    "ds_read_b64_tr_b16 a[138:139], v150 offset:2048\n"
    "v_mul_f32 v17, s66, v17\n"
    "v_mfma_f32_16x16x32_bf16 v[40:43], a[184:187], a[236:239], v[40:43]\n"
    "ds_read_b64_tr_b16 a[140:141], v142 offset:32768\n"
    "v_mul_f32 v18, s66, v18\n"
    "v_mfma_f32_16x16x32_bf16 v[36:39], a[188:191], a[216:219], v[36:39]\n"
    "ds_read_b64_tr_b16 a[142:143], v142 offset:34816\n"
    "v_mul_f32 v19, s66, v19\n"
    "s_waitcnt lgkmcnt(6)\n"
    "v_mfma_f32_16x16x32_bf16 v[36:39], a[128:131], a[224:227], v[36:39]\n"
    "s_waitcnt lgkmcnt(0)\n"
    "ds_read_b64_tr_b16 a[144:145], v142 offset:49152\n"
    "v_mul_f32 v24, s66, v24\n"
    "v_mfma_f32_16x16x32_bf16 v[36:39], a[132:135], a[232:235], v[36:39]\n"
    "ds_read_b64_tr_b16 a[146:147], v142 offset:51200\n"
    "v_mul_f32 v25, s66, v25\n"
    "v_mfma_f32_16x16x32_bf16 v[44:47], a[188:191], a[220:223], v[44:47]\n"
    "ds_read_b64_tr_b16 a[148:149], v151 offset:0\n"
    "v_mul_f32 v26, s66, v26\n"
    "v_mfma_f32_16x16x32_bf16 v[44:47], a[128:131], a[228:231], v[44:47]\n"
    "ds_read_b64_tr_b16 a[150:151], v151 offset:2048\n"
    "ds_read_b64_tr_b16 a[152:153], v143 offset:32768\n"
    "v_mfma_f32_16x16x32_bf16 v[44:47], a[132:135], a[236:239], v[44:47]\n"
    "buffer_load_dwordx4 v204, s[84:87], s41 offen offset:0 nt lds\n"
    "v_mfma_f32_16x16x32_bf16 v[48:51], a[136:139], a[216:219], v[48:51]\n"
    "ds_read_b64_tr_b16 a[154:155], v143 offset:34816\n"
    "ds_read_b64_tr_b16 a[156:157], v143 offset:49152\n"
    "v_mfma_f32_16x16x32_bf16 v[48:51], a[140:143], a[224:227], v[48:51]\n"
    "ds_read_b64_tr_b16 a[158:159], v143 offset:51200\n"
    "v_mul_f32 v27, s66, v27\n"
    "s_waitcnt lgkmcnt(6)\n"
    "v_mfma_f32_16x16x32_bf16 v[48:51], a[144:147], a[232:235], v[48:51]\n"
    "ds_read_b64_tr_b16 a[160:161], v144 offset:256\n"
    "v_mul_f32 v20, s66, v20\n"
    "v_mfma_f32_16x16x32_bf16 v[56:59], a[136:139], a[220:223], v[56:59]\n"
    "s_waitcnt lgkmcnt(1)\n"
    "ds_read_b64_tr_b16 a[162:163], v144 offset:2304\n"
    "ds_read_b64_tr_b16 a[164:165], v136 offset:33024\n"
    "v_mfma_f32_16x16x32_bf16 v[56:59], a[140:143], a[228:231], v[56:59]\n"
    "ds_read_b64_tr_b16 a[166:167], v136 offset:35072\n"
    "ds_read_b64_tr_b16 a[168:169], v136 offset:49408\n"
    "v_mfma_f32_16x16x32_bf16 v[56:59], a[144:147], a[236:239], v[56:59]\n"
    "buffer_load_dwordx4 v205, s[84:87], s41 offen offset:1024 nt lds\n"
    "v_mfma_f32_16x16x32_bf16 v[52:55], a[148:151], a[216:219], v[52:55]\n"
    "ds_read_b64_tr_b16 a[170:171], v136 offset:51456\n"
    "ds_read_b64_tr_b16 a[172:173], v145 offset:256\n"
    "v_mfma_f32_16x16x32_bf16 v[52:55], a[152:155], a[224:227], v[52:55]\n"
    "ds_read_b64_tr_b16 a[174:175], v145 offset:2304\n"
    "v_mul_f32 v21, s66, v21\n"
    "v_mfma_f32_16x16x32_bf16 v[52:55], a[156:159], a[232:235], v[52:55]\n"
    "ds_read_b64_tr_b16 a[176:177], v137 offset:33024\n"
    "ds_read_b64_tr_b16 a[178:179], v137 offset:35072\n"
    "v_mfma_f32_16x16x32_bf16 v[60:63], a[148:151], a[220:223], v[60:63]\n"
    "ds_read_b64_tr_b16 a[180:181], v137 offset:49408\n"
    "ds_read_b64_tr_b16 a[182:183], v137 offset:51456\n"
    "v_mfma_f32_16x16x32_bf16 v[60:63], a[152:155], a[228:231], v[60:63]\n"
    "ds_read_b64_tr_b16 a[184:185], v146 offset:256\n"
    "ds_read_b64_tr_b16 a[186:187], v146 offset:2304\n"
    "v_mfma_f32_16x16x32_bf16 v[60:63], a[156:159], a[236:239], v[60:63]\n"
    "s_waitcnt lgkmcnt(4)\n"
    "ds_read_b64_tr_b16 a[188:189], v138 offset:33024\n"
    "v_mul_f32 v22, s66, v22\n"
    "v_mfma_f32_16x16x32_bf16 v[64:67], a[160:163], a[216:219], v[64:67]\n"
    "ds_read_b64_tr_b16 a[190:191], v138 offset:35072\n"
    "ds_read_b64_tr_b16 a[128:129], v138 offset:49408\n"
    "v_mfma_f32_16x16x32_bf16 v[64:67], a[164:167], a[224:227], v[64:67]\n"
    "buffer_load_dwordx4 v206, s[84:87], s41 offen offset:2048 nt lds\n"
    "v_mfma_f32_16x16x32_bf16 v[64:67], a[168:171], a[232:235], v[64:67]\n"
    "ds_read_b64_tr_b16 a[130:131], v138 offset:51456\n"
    "ds_read_b64_tr_b16 a[132:133], v147 offset:256\n"
    "v_mfma_f32_16x16x32_bf16 v[72:75], a[160:163], a[220:223], v[72:75]\n"
    "ds_read_b64_tr_b16 a[134:135], v147 offset:2304\n"
    "v_mul_f32 v23, s66, v23\n"
    "v_mfma_f32_16x16x32_bf16 v[72:75], a[164:167], a[228:231], v[72:75]\n"
    "ds_read_b64_tr_b16 a[136:137], v139 offset:33024\n"
    "v_mul_f32 v28, s66, v28\n"
    "v_mfma_f32_16x16x32_bf16 v[72:75], a[168:171], a[236:239], v[72:75]\n"
    "ds_read_b64_tr_b16 a[138:139], v139 offset:35072\n"
    "v_mul_f32 v29, s66, v29\n"
    "v_mfma_f32_16x16x32_bf16 v[68:71], a[172:175], a[216:219], v[68:71]\n"
    "ds_read_b64_tr_b16 a[140:141], v139 offset:49408\n"
    "v_mul_f32 v30, s66, v30\n"
    "v_mfma_f32_16x16x32_bf16 v[68:71], a[176:179], a[224:227], v[68:71]\n"
    "s_waitcnt lgkmcnt(5)\n"
    "ds_read_b64_tr_b16 a[142:143], v139 offset:51456\n"
    "v_mul_f32 v31, s66, v31\n"
    "v_mfma_f32_16x16x32_bf16 v[68:71], a[180:183], a[232:235], v[68:71]\n"
    "ds_read_b64_tr_b16 a[144:145], v148 offset:256\n"
    "ds_read_b64_tr_b16 a[146:147], v148 offset:2304\n"
    "v_mfma_f32_16x16x32_bf16 v[76:79], a[172:175], a[220:223], v[76:79]\n"
    "buffer_load_dwordx4 v207, s[84:87], s41 offen offset:3072 nt lds\n"
    "v_mfma_f32_16x16x32_bf16 v[76:79], a[176:179], a[228:231], v[76:79]\n"
    "ds_read_b64_tr_b16 a[148:149], v140 offset:33024\n"
    "s_mov_b32 s41, 0x8000\n"
    "v_mfma_f32_16x16x32_bf16 v[76:79], a[180:183], a[236:239], v[76:79]\n"
    "ds_read_b64_tr_b16 a[150:151], v140 offset:35072\n"
    "s_add_u32 m0, s36, 0x8000\n"
    "v_mfma_f32_16x16x32_bf16 v[80:83], a[184:187], a[216:219], v[80:83]\n"
    "ds_read_b64_tr_b16 a[152:153], v140 offset:49408\n"
    "ds_read_b64_tr_b16 a[154:155], v140 offset:51456\n"
    "v_mfma_f32_16x16x32_bf16 v[80:83], a[188:191], a[224:227], v[80:83]\n"
    "buffer_load_dwordx4 v200, s[80:83], s41 offen offset:0 nt lds\n"
    "v_mfma_f32_16x16x32_bf16 v[80:83], a[128:131], a[232:235], v[80:83]\n"
    "ds_read_b64_tr_b16 a[156:157], v149 offset:256\n"
    "ds_read_b64_tr_b16 a[158:159], v149 offset:2304\n"
    "v_mfma_f32_16x16x32_bf16 v[88:91], a[184:187], a[220:223], v[88:91]\n"
    "s_waitcnt lgkmcnt(8)\n"
    "ds_read_b64_tr_b16 a[160:161], v141 offset:33024\n"
    "ds_read_b64_tr_b16 a[162:163], v141 offset:35072\n"
    "v_mfma_f32_16x16x32_bf16 v[88:91], a[188:191], a[228:231], v[88:91]\n"
    "s_waitcnt vmcnt(14)\n"
    "s_barrier\n"
    "v_mfma_f32_16x16x32_bf16 v[88:91], a[128:131], a[236:239], v[88:91]\n"
    "ds_read_b128 a[64:67], v188 offset:0\n"
    "v_mul_f32 v32, s66, v32\n"
    "v_mfma_f32_16x16x32_bf16 v[84:87], a[132:135], a[216:219], v[84:87]\n"
    "buffer_load_dwordx4 v201, s[80:83], s41 offen offset:1024 nt lds\n"
    "v_mfma_f32_16x16x32_bf16 v[84:87], a[136:139], a[224:227], v[84:87]\n"
    "ds_read_b128 a[68:71], v189 offset:0\n"
    "v_mul_f32 v33, s66, v33\n"
    "v_mfma_f32_16x16x32_bf16 v[84:87], a[140:143], a[232:235], v[84:87]\n"
    "ds_read_b128 a[72:75], v190 offset:0\n"
    "v_mul_f32 v34, s66, v34\n"
    "v_mfma_f32_16x16x32_bf16 v[92:95], a[132:135], a[220:223], v[92:95]\n"
    "ds_read_b128 a[76:79], v191 offset:0\n"
    "v_mul_f32 v35, s66, v35\n"
    "v_mfma_f32_16x16x32_bf16 v[92:95], a[136:139], a[228:231], v[92:95]\n"
    "ds_read_b64_tr_b16 a[164:165], v141 offset:49408\n"
    "ds_read_b64_tr_b16 a[166:167], v141 offset:51456\n"
    "v_mfma_f32_16x16x32_bf16 v[92:95], a[140:143], a[236:239], v[92:95]\n"
    "s_waitcnt lgkmcnt(6)\n"
    "ds_read_b128 a[80:83], v188 offset:256\n"
    "v_mul_f32 v40, s66, v40\n"
    "v_mfma_f32_16x16x32_bf16 v[96:99], a[144:147], a[216:219], v[96:99]\n"
    "ds_read_b128 a[84:87], v189 offset:256\n"
    "v_mul_f32 v41, s66, v41\n"
    "v_mfma_f32_16x16x32_bf16 v[96:99], a[148:151], a[224:227], v[96:99]\n"
    "ds_read_b64_tr_b16 a[168:169], v150 offset:256\n"
    "ds_read_b64_tr_b16 a[170:171], v150 offset:2304\n"
    "v_mfma_f32_16x16x32_bf16 v[96:99], a[152:155], a[232:235], v[96:99]\n"
    "buffer_load_dwordx4 v202, s[80:83], s41 offen offset:2048 nt lds\n"
    "v_mfma_f32_16x16x32_bf16 v[104:107], a[144:147], a[220:223], v[104:107]\n"
    "ds_read_b128 a[88:91], v190 offset:256\n"
    "v_mul_f32 v42, s66, v42\n"
    "v_mfma_f32_16x16x32_bf16 v[104:107], a[148:151], a[228:231], v[104:107]\n"
    "ds_read_b64_tr_b16 a[172:173], v142 offset:33024\n"
    "ds_read_b64_tr_b16 a[174:175], v142 offset:35072\n"
    "v_mfma_f32_16x16x32_bf16 v[104:107], a[152:155], a[236:239], v[104:107]\n"
    "ds_read_b64_tr_b16 a[176:177], v142 offset:49408\n"
    "ds_read_b64_tr_b16 a[178:179], v142 offset:51456\n"
    "v_mfma_f32_16x16x32_bf16 v[100:103], a[156:159], a[216:219], v[100:103]\n"
    "ds_read_b128 a[92:95], v191 offset:256\n"
    "v_mul_f32 v43, s66, v43\n"
    "v_mfma_f32_16x16x32_bf16 v[100:103], a[160:163], a[224:227], v[100:103]\n"
    "s_waitcnt lgkmcnt(1)\n"
    "ds_read_b128 a[96:99], v188 offset:8192\n"
    "v_mul_f32 v36, s66, v36\n"
    "v_mfma_f32_16x16x32_bf16 v[100:103], a[164:167], a[232:235], v[100:103]\n"
    "ds_read_b64_tr_b16 a[180:181], v151 offset:256\n"
    "v_mul_f32 v37, s66, v37\n"
    "v_mfma_f32_16x16x32_bf16 v[108:111], a[156:159], a[220:223], v[108:111]\n"
    "ds_read_b128 a[100:103], v189 offset:8192\n"
    "v_mul_f32 v38, s66, v38\n"
    "v_mfma_f32_16x16x32_bf16 v[108:111], a[160:163], a[228:231], v[108:111]\n"
    "buffer_load_dwordx4 v203, s[80:83], s41 offen offset:3072 nt lds\n"
    "v_mfma_f32_16x16x32_bf16 v[108:111], a[164:167], a[236:239], v[108:111]\n"
    "ds_read_b64_tr_b16 a[182:183], v151 offset:2304\n"
    "s_mov_b32 s41, 0xc000\n"
    "v_mfma_f32_16x16x32_bf16 v[112:115], a[168:171], a[216:219], v[112:115]\n"
    "ds_read_b64_tr_b16 a[184:185], v143 offset:33024\n"
    "s_add_u32 m0, s36, 0xc000\n"
    "v_mfma_f32_16x16x32_bf16 v[112:115], a[172:175], a[224:227], v[112:115]\n"
    "ds_read_b64_tr_b16 a[186:187], v143 offset:35072\n"
    "ds_read_b64_tr_b16 a[188:189], v143 offset:49408\n"
    "v_mfma_f32_16x16x32_bf16 v[112:115], a[176:179], a[232:235], v[112:115]\n"
    "buffer_load_dwordx4 v200, s[80:83], s41 offen offset:0 nt lds\n"
    "v_mfma_f32_16x16x32_bf16 v[120:123], a[168:171], a[220:223], v[120:123]\n"
    "s_waitcnt lgkmcnt(0)\n"
    "ds_read_b128 a[104:107], v190 offset:8192\n"
    "v_mul_f32 v39, s66, v39\n"
    "v_mfma_f32_16x16x32_bf16 v[120:123], a[172:175], a[228:231], v[120:123]\n"
    "ds_read_b64_tr_b16 a[190:191], v143 offset:51456\n"
    "v_mul_f32 v44, s66, v44\n"
    "v_mfma_f32_16x16x32_bf16 v[120:123], a[176:179], a[236:239], v[120:123]\n"
    "ds_read_b128 a[108:111], v191 offset:8192\n"
    "v_mul_f32 v45, s66, v45\n"
    "v_mfma_f32_16x16x32_bf16 v[116:119], a[180:183], a[216:219], v[116:119]\n"
    "buffer_load_dwordx4 v201, s[80:83], s41 offen offset:1024 nt lds\n"
    "v_mfma_f32_16x16x32_bf16 v[116:119], a[184:187], a[224:227], v[116:119]\n"
    "ds_read_b128 a[112:115], v188 offset:8448\n"
    "v_mul_f32 v46, s66, v46\n"
    "s_waitcnt lgkmcnt(2)\n"
    "v_mfma_f32_16x16x32_bf16 v[116:119], a[188:191], a[232:235], v[116:119]\n"
    "ds_read_b128 a[116:119], v189 offset:8448\n"
    "v_mul_f32 v47, s66, v47\n"
    "v_mfma_f32_16x16x32_bf16 v[124:127], a[180:183], a[220:223], v[124:127]\n"
    "ds_read_b128 a[120:123], v190 offset:8448\n"
    "v_mul_f32 v48, s66, v48\n"
    "v_mfma_f32_16x16x32_bf16 v[124:127], a[184:187], a[228:231], v[124:127]\n"
    "buffer_load_dwordx4 v202, s[80:83], s41 offen offset:2048 nt lds\n"
    "v_mfma_f32_16x16x32_bf16 v[124:127], a[188:191], a[236:239], v[124:127]\n"
    "ds_read_b128 a[124:127], v191 offset:8448\n"
    "v_mul_f32 v49, s66, v49\n"
    "buffer_load_dwordx4 v203, s[80:83], s41 offen offset:3072 nt lds\n"
    "v_pk_mul_f32 v[50:51], v[50:51], s[66:67]\n"
    "v_pk_mul_f32 v[52:53], v[52:53], s[66:67]\n"
    "v_pk_mul_f32 v[54:55], v[54:55], s[66:67]\n"
    "v_pk_mul_f32 v[56:57], v[56:57], s[66:67]\n"
    "v_pk_mul_f32 v[58:59], v[58:59], s[66:67]\n"
    "v_pk_mul_f32 v[60:61], v[60:61], s[66:67]\n"
    "v_pk_mul_f32 v[62:63], v[62:63], s[66:67]\n"
    "v_pk_mul_f32 v[64:65], v[64:65], s[66:67]\n"
    "v_pk_mul_f32 v[66:67], v[66:67], s[66:67]\n"
    "v_pk_mul_f32 v[68:69], v[68:69], s[66:67]\n"
    "v_pk_mul_f32 v[70:71], v[70:71], s[66:67]\n"
    "v_pk_mul_f32 v[72:73], v[72:73], s[66:67]\n"
    "v_pk_mul_f32 v[74:75], v[74:75], s[66:67]\n"
    "v_pk_mul_f32 v[76:77], v[76:77], s[66:67]\n"
    "v_pk_mul_f32 v[78:79], v[78:79], s[66:67]\n"
    "v_pk_mul_f32 v[80:81], v[80:81], s[66:67]\n"
    "v_pk_mul_f32 v[82:83], v[82:83], s[66:67]\n"
    "v_pk_mul_f32 v[84:85], v[84:85], s[66:67]\n"
    "v_pk_mul_f32 v[86:87], v[86:87], s[66:67]\n"
    "v_pk_mul_f32 v[88:89], v[88:89], s[66:67]\n"
    "v_pk_mul_f32 v[90:91], v[90:91], s[66:67]\n"
    "v_pk_mul_f32 v[92:93], v[92:93], s[66:67]\n"
    "v_pk_mul_f32 v[94:95], v[94:95], s[66:67]\n"
    "v_pk_mul_f32 v[96:97], v[96:97], s[66:67]\n"
    "v_pk_mul_f32 v[98:99], v[98:99], s[66:67]\n"
    "v_pk_mul_f32 v[100:101], v[100:101], s[66:67]\n"
    "v_pk_mul_f32 v[102:103], v[102:103], s[66:67]\n"
    "v_pk_mul_f32 v[104:105], v[104:105], s[66:67]\n"
    "v_pk_mul_f32 v[106:107], v[106:107], s[66:67]\n"
    "v_pk_mul_f32 v[108:109], v[108:109], s[66:67]\n"
    "v_pk_mul_f32 v[110:111], v[110:111], s[66:67]\n"
    "v_pk_mul_f32 v[112:113], v[112:113], s[66:67]\n"
    "v_pk_mul_f32 v[114:115], v[114:115], s[66:67]\n"
    "v_pk_mul_f32 v[116:117], v[116:117], s[66:67]\n"
    "v_pk_mul_f32 v[118:119], v[118:119], s[66:67]\n"
    "v_pk_mul_f32 v[120:121], v[120:121], s[66:67]\n"
    "v_pk_mul_f32 v[122:123], v[122:123], s[66:67]\n"
    "v_pk_mul_f32 v[124:125], v[124:125], s[66:67]\n"
    "v_pk_mul_f32 v[126:127], v[126:127], s[66:67]\n"
    "s_sub_u32 s66, 229376, s65\n"
    "s_mov_b32 s67, 0x20000000\n"
    "s_mov_b32 s64, 0x20000000\n"
    "s_waitcnt lgkmcnt(0)\n"
    "s_waitcnt vmcnt(17)\n"
    "s_barrier\n"
    "v_cvt_pk_bf16_f32 v160, v0, v1\n"
    "v_cvt_pk_bf16_f32 v161, v2, v3\n"
    "v_cvt_pk_bf16_f32 v162, v4, v5\n"
    "v_cvt_pk_bf16_f32 v163, v6, v7\n"
    "v_cvt_pk_bf16_f32 v164, v8, v9\n"
    "v_cvt_pk_bf16_f32 v165, v10, v11\n"
    "v_cvt_pk_bf16_f32 v166, v12, v13\n"
    "v_cvt_pk_bf16_f32 v167, v14, v15\n"
    "v_permlane16_swap_b32 v160, v162\n"
    "v_permlane16_swap_b32 v161, v163\n"
    "v_permlane16_swap_b32 v164, v166\n"
    "v_permlane16_swap_b32 v165, v167\n"
    "v_accvgpr_write_b32 a0, v160\n"
    "v_accvgpr_write_b32 a1, v161\n"
    "v_accvgpr_write_b32 a2, v162\n"
    "v_accvgpr_write_b32 a3, v163\n"
    "v_accvgpr_write_b32 a32, v164\n"
    "v_accvgpr_write_b32 a33, v165\n"
    "v_accvgpr_write_b32 a34, v166\n"
    "v_accvgpr_write_b32 a35, v167\n"
    "v_cvt_pk_bf16_f32 v168, v16, v17\n"
    "v_cvt_pk_bf16_f32 v169, v18, v19\n"
    "v_cvt_pk_bf16_f32 v170, v20, v21\n"
    "v_cvt_pk_bf16_f32 v171, v22, v23\n"
    "v_cvt_pk_bf16_f32 v172, v24, v25\n"
    "v_cvt_pk_bf16_f32 v173, v26, v27\n"
    "v_cvt_pk_bf16_f32 v174, v28, v29\n"
    "v_cvt_pk_bf16_f32 v175, v30, v31\n"
    "v_permlane16_swap_b32 v168, v170\n"
    "v_permlane16_swap_b32 v169, v171\n"
    "v_permlane16_swap_b32 v172, v174\n"
    "v_permlane16_swap_b32 v173, v175\n"
    "v_accvgpr_write_b32 a4, v168\n"
    "v_accvgpr_write_b32 a5, v169\n"
    "v_accvgpr_write_b32 a6, v170\n"
    "v_accvgpr_write_b32 a7, v171\n"
    "v_accvgpr_write_b32 a36, v172\n"
    "v_accvgpr_write_b32 a37, v173\n"
    "v_accvgpr_write_b32 a38, v174\n"
    "v_accvgpr_write_b32 a39, v175\n"
    "v_mfma_f32_16x16x32_bf16 v[128:131], a[64:67], a[0:3], 0\n"
    "s_mov_b32 m0, s92\n"
    "v_cvt_pk_bf16_f32 v160, v32, v33\n"
    "v_cvt_pk_bf16_f32 v161, v34, v35\n"
    "v_cvt_pk_bf16_f32 v162, v36, v37\n"
    "v_cvt_pk_bf16_f32 v163, v38, v39\n"
    "v_cvt_pk_bf16_f32 v164, v40, v41\n"
    "v_mfma_f32_16x16x32_bf16 v[132:135], a[64:67], a[32:35], 0\n"
    "v_cvt_pk_bf16_f32 v165, v42, v43\n"
    "v_cvt_pk_bf16_f32 v166, v44, v45\n"
    "v_cvt_pk_bf16_f32 v167, v46, v47\n"
    "v_permlane16_swap_b32 v160, v162\n"
    "v_permlane16_swap_b32 v161, v163\n"
    "v_permlane16_swap_b32 v164, v166\n"
    "v_mfma_f32_16x16x32_bf16 v[136:139], a[96:99], a[0:3], 0\n"
    "v_permlane16_swap_b32 v165, v167\n"
    "v_accvgpr_write_b32 a8, v160\n"
    "v_accvgpr_write_b32 a9, v161\n"
    "v_accvgpr_write_b32 a10, v162\n"
    "v_accvgpr_write_b32 a11, v163\n"
    "v_accvgpr_write_b32 a40, v164\n"
    "v_mfma_f32_16x16x32_bf16 v[140:143], a[96:99], a[32:35], 0\n"
    "v_accvgpr_write_b32 a41, v165\n"
    "v_accvgpr_write_b32 a42, v166\n"
    "v_accvgpr_write_b32 a43, v167\n"
    "buffer_load_dwordx4 v200, s[80:83], s37 offen offset:0 nt lds\n"
    "v_mfma_f32_16x16x32_bf16 v[128:131], a[68:71], a[4:7], v[128:131]\n"
    "v_cvt_pk_bf16_f32 v168, v48, v49\n"
    "v_cvt_pk_bf16_f32 v169, v50, v51\n"
    "v_cvt_pk_bf16_f32 v170, v52, v53\n"
    "v_cvt_pk_bf16_f32 v171, v54, v55\n"
    "v_cvt_pk_bf16_f32 v172, v56, v57\n"
    "v_cvt_pk_bf16_f32 v173, v58, v59\n"
    "v_mfma_f32_16x16x32_bf16 v[132:135], a[68:71], a[36:39], v[132:135]\n"
    "v_cvt_pk_bf16_f32 v174, v60, v61\n"
    "v_cvt_pk_bf16_f32 v175, v62, v63\n"
    "v_permlane16_swap_b32 v168, v170\n"
    "v_permlane16_swap_b32 v169, v171\n"
    "v_permlane16_swap_b32 v172, v174\n"
    "v_permlane16_swap_b32 v173, v175\n"
    "v_mfma_f32_16x16x32_bf16 v[136:139], a[100:103], a[4:7], v[136:139]\n"
    "v_accvgpr_write_b32 a12, v168\n"
    "v_accvgpr_write_b32 a13, v169\n"
    "v_accvgpr_write_b32 a14, v170\n"
    "v_accvgpr_write_b32 a15, v171\n"
    "v_accvgpr_write_b32 a44, v172\n"
    "v_accvgpr_write_b32 a45, v173\n"
    "v_mfma_f32_16x16x32_bf16 v[140:143], a[100:103], a[36:39], v[140:143]\n"
    "v_accvgpr_write_b32 a46, v174\n"
    "v_accvgpr_write_b32 a47, v175\n"
    "ds_read_b128 a[64:67], v188 offset:16384\n"
    "ds_read_b128 a[96:99], v188 offset:24576\n"
    "buffer_load_dwordx4 v201, s[80:83], s37 offen offset:1024 nt lds\n"
    "v_mfma_f32_16x16x32_bf16 v[128:131], a[72:75], a[8:11], v[128:131]\n"
    "v_cvt_pk_bf16_f32 v160, v64, v65\n"
    "v_cvt_pk_bf16_f32 v161, v66, v67\n"
    "v_cvt_pk_bf16_f32 v162, v68, v69\n"
    "v_cvt_pk_bf16_f32 v163, v70, v71\n"
    "v_cvt_pk_bf16_f32 v164, v72, v73\n"
    "v_cvt_pk_bf16_f32 v165, v74, v75\n"
    "v_mfma_f32_16x16x32_bf16 v[132:135], a[72:75], a[40:43], v[132:135]\n"
    "v_cvt_pk_bf16_f32 v166, v76, v77\n"
    "v_cvt_pk_bf16_f32 v167, v78, v79\n"
    "v_permlane16_swap_b32 v160, v162\n"
    "v_permlane16_swap_b32 v161, v163\n"
    "v_permlane16_swap_b32 v164, v166\n"
    "v_permlane16_swap_b32 v165, v167\n"
    "v_mfma_f32_16x16x32_bf16 v[136:139], a[104:107], a[8:11], v[136:139]\n"
    "v_accvgpr_write_b32 a16, v160\n"
    "v_accvgpr_write_b32 a17, v161\n"
    "v_accvgpr_write_b32 a18, v162\n"
    "v_accvgpr_write_b32 a19, v163\n"
    "v_accvgpr_write_b32 a48, v164\n"
    "v_accvgpr_write_b32 a49, v165\n"
    "v_mfma_f32_16x16x32_bf16 v[140:143], a[104:107], a[40:43], v[140:143]\n"
    "v_accvgpr_write_b32 a50, v166\n"
    "v_accvgpr_write_b32 a51, v167\n"
    "ds_read_b128 a[68:71], v189 offset:16384\n"
    "ds_read_b128 a[100:103], v189 offset:24576\n"
    "buffer_load_dwordx4 v202, s[80:83], s37 offen offset:2048 nt lds\n"
    "v_mfma_f32_16x16x32_bf16 v[128:131], a[76:79], a[12:15], v[128:131]\n"
    "v_cvt_pk_bf16_f32 v168, v80, v81\n"
    "v_cvt_pk_bf16_f32 v169, v82, v83\n"
    "v_cvt_pk_bf16_f32 v170, v84, v85\n"
    "v_cvt_pk_bf16_f32 v171, v86, v87\n"
    "v_cvt_pk_bf16_f32 v172, v88, v89\n"
    "v_cvt_pk_bf16_f32 v173, v90, v91\n"
    "v_mfma_f32_16x16x32_bf16 v[132:135], a[76:79], a[44:47], v[132:135]\n"
    "v_cvt_pk_bf16_f32 v174, v92, v93\n"
    "v_cvt_pk_bf16_f32 v175, v94, v95\n"
    "v_permlane16_swap_b32 v168, v170\n"
    "v_permlane16_swap_b32 v169, v171\n"
    "v_permlane16_swap_b32 v172, v174\n"
    "v_permlane16_swap_b32 v173, v175\n"
    "v_mfma_f32_16x16x32_bf16 v[136:139], a[108:111], a[12:15], v[136:139]\n"
    "v_accvgpr_write_b32 a20, v168\n"
    "v_accvgpr_write_b32 a21, v169\n"
    "v_accvgpr_write_b32 a22, v170\n"
    "v_accvgpr_write_b32 a23, v171\n"
    "v_accvgpr_write_b32 a52, v172\n"
    "v_accvgpr_write_b32 a53, v173\n"
    "v_mfma_f32_16x16x32_bf16 v[140:143], a[108:111], a[44:47], v[140:143]\n"
    "v_accvgpr_write_b32 a54, v174\n"
    "v_accvgpr_write_b32 a55, v175\n"
    "ds_read_b128 a[72:75], v190 offset:16384\n"
    "ds_read_b128 a[104:107], v190 offset:24576\n"
    "buffer_load_dwordx4 v203, s[80:83], s37 offen offset:3072 nt lds\n"
    "v_mfma_f32_16x16x32_bf16 v[128:131], a[80:83], a[16:19], v[128:131]\n"
    "s_add_u32 s37, s37, 0x4000\n"
    "s_mov_b32 m0, s72\n"
    "v_cvt_pk_bf16_f32 v160, v96, v97\n"
    "v_cvt_pk_bf16_f32 v161, v98, v99\n"
    "v_cvt_pk_bf16_f32 v162, v100, v101\n"
    "v_cvt_pk_bf16_f32 v163, v102, v103\n"
    "v_cvt_pk_bf16_f32 v164, v104, v105\n"
    "v_mfma_f32_16x16x32_bf16 v[132:135], a[80:83], a[48:51], v[132:135]\n"
    "v_cvt_pk_bf16_f32 v165, v106, v107\n"
    "v_cvt_pk_bf16_f32 v166, v108, v109\n"
    "v_cvt_pk_bf16_f32 v167, v110, v111\n"
    "v_permlane16_swap_b32 v160, v162\n"
    "v_permlane16_swap_b32 v161, v163\n"
    "v_permlane16_swap_b32 v164, v166\n"
    "v_permlane16_swap_b32 v165, v167\n"
    "v_mfma_f32_16x16x32_bf16 v[136:139], a[112:115], a[16:19], v[136:139]\n"
    "v_accvgpr_write_b32 a24, v160\n"
    "v_accvgpr_write_b32 a25, v161\n"
    "v_accvgpr_write_b32 a26, v162\n"
    "v_accvgpr_write_b32 a27, v163\n"
    "v_accvgpr_write_b32 a56, v164\n"
    "v_accvgpr_write_b32 a57, v165\n"
    "v_accvgpr_write_b32 a58, v166\n"
    "v_mfma_f32_16x16x32_bf16 v[140:143], a[112:115], a[48:51], v[140:143]\n"
    "v_accvgpr_write_b32 a59, v167\n"
    "ds_read_b128 a[76:79], v191 offset:16384\n"
    "ds_read_b128 a[108:111], v191 offset:24576\n"
    "buffer_load_dwordx4 v204, s[84:87], s38 offen offset:0 nt lds\n"
    "v_mfma_f32_16x16x32_bf16 v[128:131], a[84:87], a[20:23], v[128:131]\n"
    "v_cvt_pk_bf16_f32 v168, v112, v113\n"
    "v_cvt_pk_bf16_f32 v169, v114, v115\n"
    "v_cvt_pk_bf16_f32 v170, v116, v117\n"
    "v_cvt_pk_bf16_f32 v171, v118, v119\n"
    "v_cvt_pk_bf16_f32 v172, v120, v121\n"
    "v_cvt_pk_bf16_f32 v173, v122, v123\n"
    "v_mfma_f32_16x16x32_bf16 v[132:135], a[84:87], a[52:55], v[132:135]\n"
    "v_cvt_pk_bf16_f32 v174, v124, v125\n"
    "v_cvt_pk_bf16_f32 v175, v126, v127\n"
    "v_permlane16_swap_b32 v168, v170\n"
    "v_permlane16_swap_b32 v169, v171\n"
    "v_permlane16_swap_b32 v172, v174\n"
    "v_permlane16_swap_b32 v173, v175\n"
    "v_mfma_f32_16x16x32_bf16 v[136:139], a[116:119], a[20:23], v[136:139]\n"
    "v_accvgpr_write_b32 a28, v168\n"
    "v_accvgpr_write_b32 a29, v169\n"
    "v_accvgpr_write_b32 a30, v170\n"
    "v_accvgpr_write_b32 a31, v171\n"
    "v_accvgpr_write_b32 a60, v172\n"
    "v_accvgpr_write_b32 a61, v173\n"
    "v_mfma_f32_16x16x32_bf16 v[140:143], a[116:119], a[52:55], v[140:143]\n"
    "v_accvgpr_write_b32 a62, v174\n"
    "v_accvgpr_write_b32 a63, v175\n"
    "ds_read_b128 a[80:83], v188 offset:16640\n"
    "ds_read_b128 a[112:115], v188 offset:24832\n"
    "buffer_load_dwordx4 v205, s[84:87], s38 offen offset:1024 nt lds\n"
    "v_mfma_f32_16x16x32_bf16 v[128:131], a[88:91], a[24:27], v[128:131]\n"
    "ds_read_b128 a[84:87], v189 offset:16640\n"
    "v_mfma_f32_16x16x32_bf16 v[132:135], a[88:91], a[56:59], v[132:135]\n"
    "ds_read_b128 a[116:119], v189 offset:24832\n"
    "v_mfma_f32_16x16x32_bf16 v[136:139], a[120:123], a[24:27], v[136:139]\n"
    "buffer_load_dwordx4 v206, s[84:87], s38 offen offset:2048 nt lds\n"
    "v_mfma_f32_16x16x32_bf16 v[140:143], a[120:123], a[56:59], v[140:143]\n"
    "v_mfma_f32_16x16x32_bf16 v[128:131], a[92:95], a[28:31], v[128:131]\n"
    "ds_read_b128 a[88:91], v190 offset:16640\n"
    "v_mfma_f32_16x16x32_bf16 v[132:135], a[92:95], a[60:63], v[132:135]\n"
    "ds_read_b128 a[120:123], v190 offset:24832\n"
    "v_mfma_f32_16x16x32_bf16 v[136:139], a[124:127], a[28:31], v[136:139]\n"
    "buffer_load_dwordx4 v207, s[84:87], s38 offen offset:3072 nt lds\n"
    "v_mfma_f32_16x16x32_bf16 v[140:143], a[124:127], a[60:63], v[140:143]\n"
    "ds_read_b128 a[92:95], v191 offset:16640\n"
    "ds_read_b128 a[124:127], v191 offset:24832\n"
    "s_add_u32 s38, s38, 0x4000\n"
    "v_max3_f32 v253, v128, v129, v130\n"
    "s_nop 1\n"
    "v_max3_f32 v252, v131, v136, v137\n"
    "v_max3_f32 v253, v253, v138, v139\n"
    "v_max3_f32 v254, v132, v133, v134\n"
    "v_max3_f32 v208, v135, v140, v141\n"
    "v_max3_f32 v254, v254, v142, v143\n"
    "v_max_f32 v253, v253, v252\n"
    "v_max_f32 v254, v254, v208\n"
    "v_mov_b32 v252, v253\n"
    "v_mov_b32 v208, v254\n"
    "s_nop 0\n"
    "v_permlane32_swap_b32 v253, v252\n"
    "v_permlane32_swap_b32 v254, v208\n"
    "v_max_f32 v253, v253, v252\n"
    "v_max_f32 v254, v254, v208\n"
    "v_mov_b32 v252, v253\n"
    "v_mov_b32 v208, v254\n"
    "s_nop 0\n"
    "v_permlane16_swap_b32 v253, v252\n"
    "v_permlane16_swap_b32 v254, v208\n"
    "v_max_f32 v253, v253, v252\n"
    "v_max_f32 v254, v254, v208\n"
    "v_add_f32 v253, 0x40e00000, v253\n"
    "v_add_f32 v254, 0x40e00000, v254\n"
    "v_sub_f32 v128, v128, v253\n"
    "v_sub_f32 v129, v129, v253\n"
    "v_sub_f32 v130, v130, v253\n"
    "v_sub_f32 v131, v131, v253\n"
    "v_exp_f32 v168, v128\n"
    "v_exp_f32 v169, v129\n"
    "v_exp_f32 v170, v130\n"
    "v_exp_f32 v171, v131\n"
    "v_sub_f32 v132, v132, v254\n"
    "v_sub_f32 v133, v133, v254\n"
    "v_sub_f32 v134, v134, v254\n"
    "v_sub_f32 v135, v135, v254\n"
    "v_exp_f32 v172, v132\n"
    "v_exp_f32 v173, v133\n"
    "v_exp_f32 v174, v134\n"
    "v_exp_f32 v175, v135\n"
    "v_cvt_pk_bf16_f32 v152, v168, v169\n"
    "v_cvt_pk_bf16_f32 v153, v170, v171\n"
    "v_sub_f32 v136, v136, v253\n"
    "v_sub_f32 v137, v137, v253\n"
    "v_sub_f32 v138, v138, v253\n"
    "v_sub_f32 v139, v139, v253\n"
    "v_exp_f32 v233, v136\n"
    "v_exp_f32 v234, v137\n"
    "v_exp_f32 v235, v138\n"
    "v_exp_f32 v236, v139\n"
    "v_cvt_pk_bf16_f32 v156, v172, v173\n"
    "v_cvt_pk_bf16_f32 v157, v174, v175\n"
    "v_sub_f32 v140, v140, v254\n"
    "v_sub_f32 v141, v141, v254\n"
    "v_sub_f32 v142, v142, v254\n"
    "v_sub_f32 v143, v143, v254\n"
    "v_exp_f32 v237, v140\n"
    "v_exp_f32 v238, v141\n"
    "v_exp_f32 v239, v142\n"
    "v_exp_f32 v240, v143\n"
    "v_cvt_pk_bf16_f32 v154, v233, v234\n"
    "v_cvt_pk_bf16_f32 v155, v235, v236\n"
    "v_sub_f32 v144, 0, v253\n"
    "v_sub_f32 v145, 0, v253\n"
    "v_sub_f32 v146, 0, v253\n"
    "v_sub_f32 v147, 0, v253\n"
    "v_sub_f32 v148, 0, v254\n"
    "v_sub_f32 v149, 0, v254\n"
    "v_sub_f32 v150, 0, v254\n"
    "v_sub_f32 v151, 0, v254\n"
    "v_cvt_pk_bf16_f32 v158, v237, v238\n"
    "v_cvt_pk_bf16_f32 v159, v239, v240\n"
    "s_waitcnt lgkmcnt(0)\n"
    "v_mfma_f32_16x16x32_bf16 v[128:131], a[64:67], a[0:3], v[144:147]\n"
    "s_waitcnt vmcnt(12)\n"
    "s_barrier\n"
    "v_mfma_f32_16x16x32_bf16 v[128:131], a[68:71], a[4:7], v[128:131]\n"
    "ds_read_b64_tr_b16 a[128:129], v192 offset:0\n"
    "s_mov_b32 m0, s93\n"
    "v_mfma_f32_16x16x32_bf16 v[128:131], a[72:75], a[8:11], v[128:131]\n"
    "buffer_load_dwordx4 v200, s[80:83], s37 offen offset:0 nt lds\n"
    "v_mfma_f32_16x16x32_bf16 v[128:131], a[76:79], a[12:15], v[128:131]\n"
    "ds_read_b64_tr_b16 a[130:131], v192 offset:8192\n"
    "v_mfma_f32_16x16x32_bf16 v[128:131], a[80:83], a[16:19], v[128:131]\n"
    "ds_read_b64_tr_b16 a[132:133], v193 offset:0\n"
    "v_mfma_f32_16x16x32_bf16 v[128:131], a[84:87], a[20:23], v[128:131]\n"
    "ds_read_b64_tr_b16 a[134:135], v193 offset:8192\n"
    "v_mfma_f32_16x16x32_bf16 v[128:131], a[88:91], a[24:27], v[128:131]\n"
    "ds_read_b64_tr_b16 a[136:137], v194 offset:0\n"
    "v_mfma_f32_16x16x32_bf16 v[128:131], a[92:95], a[28:31], v[128:131]\n"
    "ds_read_b64_tr_b16 a[138:139], v194 offset:8192\n"
    "v_mfma_f32_16x16x32_bf16 v[132:135], a[64:67], a[32:35], v[148:151]\n"
    "ds_read_b64_tr_b16 a[140:141], v195 offset:0\n"
    "v_mfma_f32_16x16x32_bf16 v[132:135], a[68:71], a[36:39], v[132:135]\n"
    "ds_read_b64_tr_b16 a[142:143], v195 offset:8192\n"
    "ds_read_b64_tr_b16 a[144:145], v196 offset:0\n"
    "v_mfma_f32_16x16x32_bf16 v[132:135], a[72:75], a[40:43], v[132:135]\n"
    "buffer_load_dwordx4 v201, s[80:83], s37 offen offset:1024 nt lds\n"
    "v_mfma_f32_16x16x32_bf16 v[132:135], a[76:79], a[44:47], v[132:135]\n"
    "v_exp_f32 v168, v128\n"
    "v_mfma_f32_16x16x32_bf16 v[132:135], a[80:83], a[48:51], v[132:135]\n"
    "ds_read_b64_tr_b16 a[146:147], v196 offset:8192\n"
    "ds_read_b64_tr_b16 a[148:149], v197 offset:0\n"
    "v_mfma_f32_16x16x32_bf16 v[132:135], a[84:87], a[52:55], v[132:135]\n"
    "ds_read_b64_tr_b16 a[150:151], v197 offset:8192\n"
    "ds_read_b64_tr_b16 a[152:153], v198 offset:0\n"
    "v_mfma_f32_16x16x32_bf16 v[132:135], a[88:91], a[56:59], v[132:135]\n"
    "v_exp_f32 v169, v129\n"
    "v_mfma_f32_16x16x32_bf16 v[132:135], a[92:95], a[60:63], v[132:135]\n"
    "ds_read_b64_tr_b16 a[154:155], v198 offset:8192\n"
    "ds_read_b64_tr_b16 a[156:157], v199 offset:0\n"
    "v_mfma_f32_16x16x32_bf16 v[136:139], a[96:99], a[0:3], v[144:147]\n"
    "v_exp_f32 v170, v130\n"
    "v_mfma_f32_16x16x32_bf16 v[136:139], a[100:103], a[4:7], v[136:139]\n"
    "ds_read_b64_tr_b16 a[158:159], v199 offset:8192\n"
    "ds_read_b64_tr_b16 a[160:161], v192 offset:256\n"
    "v_mfma_f32_16x16x32_bf16 v[136:139], a[104:107], a[8:11], v[136:139]\n"
    "buffer_load_dwordx4 v202, s[80:83], s37 offen offset:2048 nt lds\n"
    "v_mfma_f32_16x16x32_bf16 v[136:139], a[108:111], a[12:15], v[136:139]\n"
    "v_exp_f32 v171, v131\n"
    "v_mfma_f32_16x16x32_bf16 v[136:139], a[112:115], a[16:19], v[136:139]\n"
    "ds_read_b64_tr_b16 a[162:163], v192 offset:8448\n"
    "ds_read_b64_tr_b16 a[164:165], v193 offset:256\n"
    "v_mfma_f32_16x16x32_bf16 v[136:139], a[116:119], a[20:23], v[136:139]\n"
    "v_exp_f32 v172, v132\n"
    "v_mfma_f32_16x16x32_bf16 v[136:139], a[120:123], a[24:27], v[136:139]\n"
    "ds_read_b128 a[64:67], v188 offset:32768\n"
    "v_cvt_pk_bf16_f32 v160, v168, v169\n"
    "v_mfma_f32_16x16x32_bf16 v[136:139], a[124:127], a[28:31], v[136:139]\n"
    "v_exp_f32 v173, v133\n"
    "v_mfma_f32_16x16x32_bf16 v[140:143], a[96:99], a[32:35], v[148:151]\n"
    "ds_read_b128 a[68:71], v189 offset:32768\n"
    "v_cvt_pk_bf16_f32 v161, v170, v171\n"
    "v_mfma_f32_16x16x32_bf16 v[140:143], a[100:103], a[36:39], v[140:143]\n"
    "v_exp_f32 v174, v134\n"
    "v_mfma_f32_16x16x32_bf16 v[140:143], a[104:107], a[40:43], v[140:143]\n"
    "buffer_load_dwordx4 v203, s[80:83], s37 offen offset:3072 nt lds\n"
    "v_mfma_f32_16x16x32_bf16 v[140:143], a[108:111], a[44:47], v[140:143]\n"
    "s_add_u32 s37, s37, 0x4000\n"
    "v_exp_f32 v175, v135\n"
    "v_mfma_f32_16x16x32_bf16 v[140:143], a[112:115], a[48:51], v[140:143]\n"
    "ds_read_b64_tr_b16 a[166:167], v193 offset:8448\n"
    "ds_read_b64_tr_b16 a[168:169], v194 offset:256\n"
    "v_mfma_f32_16x16x32_bf16 v[140:143], a[116:119], a[52:55], v[140:143]\n"
    "ds_read_b128 a[72:75], v190 offset:32768\n"
    "v_cvt_pk_bf16_f32 v164, v172, v173\n"
    "v_mfma_f32_16x16x32_bf16 v[140:143], a[120:123], a[56:59], v[140:143]\n"
    "s_mov_b32 m0, s73\n"
    "v_exp_f32 v233, v136\n"
    "v_mfma_f32_16x16x32_bf16 v[140:143], a[124:127], a[60:63], v[140:143]\n"
    "ds_read_b128 a[76:79], v191 offset:32768\n"
    "v_cvt_pk_bf16_f32 v165, v174, v175\n"
    "v_mfma_f32_16x16x32_bf16 v[176:179], v[184:187], v[152:155], 0\n"
    "v_exp_f32 v234, v137\n"
    "v_mfma_f32_16x16x32_bf16 v[180:183], v[184:187], v[156:159], 0\n"
    "s_waitcnt lgkmcnt(15)\n"
    "ds_read_b64_tr_b16 a[170:171], v194 offset:8448\n"
    "ds_read_b64_tr_b16 a[172:173], v195 offset:256\n"
    "v_mfma_f32_16x16x32_bf16 v[8:11], a[128:131], v[156:159], 0\n"
    "buffer_load_dwordx4 v204, s[84:87], s38 offen offset:0 nt lds\n"
    "v_mfma_f32_16x16x32_bf16 v[0:3], a[128:131], v[152:155], 0\n"
    "v_exp_f32 v235, v138\n"
    "v_mfma_f32_16x16x32_bf16 v[4:7], a[132:135], v[152:155], 0\n"
    "ds_read_b128 a[80:83], v188 offset:33024\n"
    "v_cvt_pk_bf16_f32 v162, v233, v234\n"
    "v_mfma_f32_16x16x32_bf16 v[12:15], a[132:135], v[156:159], 0\n"
    "ds_read_b128 a[84:87], v189 offset:33024\n"
    "v_or3_b32 v208, v160, v161, v164\n"
    "v_mfma_f32_16x16x32_bf16 v[24:27], a[136:139], v[156:159], 0\n"
    "v_exp_f32 v236, v139\n"
    "v_mfma_f32_16x16x32_bf16 v[16:19], a[136:139], v[152:155], 0\n"
    "ds_read_b64_tr_b16 a[174:175], v195 offset:8448\n"
    "ds_read_b64_tr_b16 a[176:177], v196 offset:256\n"
    "v_mfma_f32_16x16x32_bf16 v[20:23], a[140:143], v[152:155], 0\n"
    "v_exp_f32 v237, v140\n"
    "v_mfma_f32_16x16x32_bf16 v[28:31], a[140:143], v[156:159], 0\n"
    "s_waitcnt lgkmcnt(15)\n"
    "ds_read_b128 a[88:91], v190 offset:33024\n"
    "v_cvt_pk_bf16_f32 v163, v235, v236\n"
    "v_mfma_f32_16x16x32_bf16 v[40:43], a[144:147], v[156:159], 0\n"
    "buffer_load_dwordx4 v205, s[84:87], s38 offen offset:1024 nt lds\n"
    "v_mfma_f32_16x16x32_bf16 v[32:35], a[144:147], v[152:155], 0\n"
    "v_exp_f32 v238, v141\n"
    "v_mfma_f32_16x16x32_bf16 v[36:39], a[148:151], v[152:155], 0\n"
    "ds_read_b128 a[92:95], v191 offset:33024\n"
    "v_cvt_pk_bf16_f32 v166, v237, v238\n"
    "v_mfma_f32_16x16x32_bf16 v[44:47], a[148:151], v[156:159], 0\n"
    "ds_read_b64_tr_b16 a[178:179], v196 offset:8448\n"
    "ds_read_b64_tr_b16 a[180:181], v197 offset:256\n"
    "v_mfma_f32_16x16x32_bf16 v[56:59], a[152:155], v[156:159], 0\n"
    "v_exp_f32 v239, v142\n"
    "v_mfma_f32_16x16x32_bf16 v[48:51], a[152:155], v[152:155], 0\n"
    "ds_read_b128 a[96:99], v188 offset:40960\n"
    "v_or3_b32 v208, v208, v165, v162\n"
    "v_mfma_f32_16x16x32_bf16 v[52:55], a[156:159], v[152:155], 0\n"
    "v_exp_f32 v240, v143\n"
    "v_mfma_f32_16x16x32_bf16 v[60:63], a[156:159], v[156:159], 0\n"
    "s_waitcnt lgkmcnt(6)\n"
    "ds_read_b128 a[100:103], v189 offset:40960\n"
    "v_cvt_pk_bf16_f32 v167, v239, v240\n"
    "v_mfma_f32_16x16x32_bf16 v[72:75], a[160:163], v[156:159], 0\n"
    "buffer_load_dwordx4 v206, s[84:87], s38 offen offset:2048 nt lds\n"
    "v_mfma_f32_16x16x32_bf16 v[64:67], a[160:163], v[152:155], 0\n"
    "ds_read_b64_tr_b16 a[182:183], v197 offset:8448\n"
    "ds_read_b64_tr_b16 a[184:185], v198 offset:256\n"
    "v_mfma_f32_16x16x32_bf16 v[68:71], a[164:167], v[152:155], 0\n"
    "ds_read_b128 a[104:107], v190 offset:40960\n"
    "v_or3_b32 v208, v208, v163, v166\n"
    "v_mfma_f32_16x16x32_bf16 v[76:79], a[164:167], v[156:159], 0\n"
    "ds_read_b128 a[108:111], v191 offset:40960\n"
    "v_bitop3_b32 v208, v208, v167, s40 bitop3:0xa8\n"
    "v_mfma_f32_16x16x32_bf16 v[88:91], a[168:171], v[156:159], 0\n"
    "ds_read_b64_tr_b16 a[186:187], v198 offset:8448\n"
    "ds_read_b64_tr_b16 a[188:189], v199 offset:256\n"
    "v_mfma_f32_16x16x32_bf16 v[80:83], a[168:171], v[152:155], 0\n"
    "ds_read_b128 a[112:115], v188 offset:41216\n"
    "v_cmp_ne_u32 vcc, 0, v208\n"
    "v_mfma_f32_16x16x32_bf16 v[84:87], a[172:175], v[152:155], 0\n"
    "ds_read_b128 a[116:119], v189 offset:41216\n"
    "v_mfma_f32_16x16x32_bf16 v[92:95], a[172:175], v[156:159], 0\n"
    "s_waitcnt lgkmcnt(2)\n"
    "ds_read_b64_tr_b16 a[190:191], v199 offset:8448\n"
    "v_mfma_f32_16x16x32_bf16 v[104:107], a[176:179], v[156:159], 0\n"
    "buffer_load_dwordx4 v207, s[84:87], s38 offen offset:3072 nt lds\n"
    "v_mfma_f32_16x16x32_bf16 v[96:99], a[176:179], v[152:155], 0\n"
    "ds_read_b128 a[120:123], v190 offset:41216\n"
    "s_add_u32 s38, s38, 0x4000\n"
    "v_mfma_f32_16x16x32_bf16 v[100:103], a[180:183], v[152:155], 0\n"
    "ds_read_b128 a[124:127], v191 offset:41216\n"
    "v_mfma_f32_16x16x32_bf16 v[108:111], a[180:183], v[156:159], 0\n"
    "v_mfma_f32_16x16x32_bf16 v[120:123], a[184:187], v[156:159], 0\n"
    "v_mfma_f32_16x16x32_bf16 v[112:115], a[184:187], v[152:155], 0\n"
    "s_waitcnt lgkmcnt(2)\n"
    "v_mfma_f32_16x16x32_bf16 v[116:119], a[188:191], v[152:155], 0\n"
    "v_mfma_f32_16x16x32_bf16 v[124:127], a[188:191], v[156:159], 0\n"
    "s_cbranch_vccnz LR1%=\n"
    "LB1%=:\n"
    "s_waitcnt lgkmcnt(0)\n"
    "v_mfma_f32_16x16x32_bf16 v[128:131], a[64:67], a[0:3], v[144:147]\n"
    "s_waitcnt vmcnt(16)\n"
    "s_barrier\n"
    "v_mfma_f32_16x16x32_bf16 v[128:131], a[68:71], a[4:7], v[128:131]\n"
    "ds_read_b64_tr_b16 a[128:129], v192 offset:16384\n"
    "s_mov_b32 m0, s94\n"
    "v_mfma_f32_16x16x32_bf16 v[128:131], a[72:75], a[8:11], v[128:131]\n"
    "buffer_load_dwordx4 v200, s[80:83], s37 offen offset:0 nt lds\n"
    "v_mfma_f32_16x16x32_bf16 v[128:131], a[76:79], a[12:15], v[128:131]\n"
    "ds_read_b64_tr_b16 a[130:131], v192 offset:24576\n"
    "v_mfma_f32_16x16x32_bf16 v[128:131], a[80:83], a[16:19], v[128:131]\n"
    "ds_read_b64_tr_b16 a[132:133], v193 offset:16384\n"
    "v_mfma_f32_16x16x32_bf16 v[128:131], a[84:87], a[20:23], v[128:131]\n"
    "ds_read_b64_tr_b16 a[134:135], v193 offset:24576\n"
    "v_mfma_f32_16x16x32_bf16 v[128:131], a[88:91], a[24:27], v[128:131]\n"
    "ds_read_b64_tr_b16 a[136:137], v194 offset:16384\n"
    "v_mfma_f32_16x16x32_bf16 v[128:131], a[92:95], a[28:31], v[128:131]\n"
    "ds_read_b64_tr_b16 a[138:139], v194 offset:24576\n"
    "v_mfma_f32_16x16x32_bf16 v[132:135], a[64:67], a[32:35], v[148:151]\n"
    "ds_read_b64_tr_b16 a[140:141], v195 offset:16384\n"
    "v_mfma_f32_16x16x32_bf16 v[132:135], a[68:71], a[36:39], v[132:135]\n"
    "ds_read_b64_tr_b16 a[142:143], v195 offset:24576\n"
    "ds_read_b64_tr_b16 a[144:145], v196 offset:16384\n"
    "v_mfma_f32_16x16x32_bf16 v[132:135], a[72:75], a[40:43], v[132:135]\n"
    "buffer_load_dwordx4 v201, s[80:83], s37 offen offset:1024 nt lds\n"
    "v_mfma_f32_16x16x32_bf16 v[132:135], a[76:79], a[44:47], v[132:135]\n"
    "v_exp_f32 v168, v128\n"
    "v_mfma_f32_16x16x32_bf16 v[132:135], a[80:83], a[48:51], v[132:135]\n"
    "ds_read_b64_tr_b16 a[146:147], v196 offset:24576\n"
    "ds_read_b64_tr_b16 a[148:149], v197 offset:16384\n"
    "v_mfma_f32_16x16x32_bf16 v[132:135], a[84:87], a[52:55], v[132:135]\n"
    "ds_read_b64_tr_b16 a[150:151], v197 offset:24576\n"
    "ds_read_b64_tr_b16 a[152:153], v198 offset:16384\n"
    "v_mfma_f32_16x16x32_bf16 v[132:135], a[88:91], a[56:59], v[132:135]\n"
    "v_exp_f32 v169, v129\n"
    "v_mfma_f32_16x16x32_bf16 v[132:135], a[92:95], a[60:63], v[132:135]\n"
    "ds_read_b64_tr_b16 a[154:155], v198 offset:24576\n"
    "ds_read_b64_tr_b16 a[156:157], v199 offset:16384\n"
    "v_mfma_f32_16x16x32_bf16 v[136:139], a[96:99], a[0:3], v[144:147]\n"
    "v_exp_f32 v170, v130\n"
    "v_mfma_f32_16x16x32_bf16 v[136:139], a[100:103], a[4:7], v[136:139]\n"
    "ds_read_b64_tr_b16 a[158:159], v199 offset:24576\n"
    "ds_read_b64_tr_b16 a[160:161], v192 offset:16640\n"
    "v_mfma_f32_16x16x32_bf16 v[136:139], a[104:107], a[8:11], v[136:139]\n"
    "buffer_load_dwordx4 v202, s[80:83], s37 offen offset:2048 nt lds\n"
    "v_mfma_f32_16x16x32_bf16 v[136:139], a[108:111], a[12:15], v[136:139]\n"
    "v_exp_f32 v171, v131\n"
    "v_mfma_f32_16x16x32_bf16 v[136:139], a[112:115], a[16:19], v[136:139]\n"
    "ds_read_b64_tr_b16 a[162:163], v192 offset:24832\n"
    "ds_read_b64_tr_b16 a[164:165], v193 offset:16640\n"
    "v_mfma_f32_16x16x32_bf16 v[136:139], a[116:119], a[20:23], v[136:139]\n"
    "v_exp_f32 v172, v132\n"
    "v_mfma_f32_16x16x32_bf16 v[136:139], a[120:123], a[24:27], v[136:139]\n"
    "ds_read_b128 a[64:67], v188 offset:49152\n"
    "v_cvt_pk_bf16_f32 v152, v168, v169\n"
    "v_mfma_f32_16x16x32_bf16 v[136:139], a[124:127], a[28:31], v[136:139]\n"
    "v_exp_f32 v173, v133\n"
    "v_mfma_f32_16x16x32_bf16 v[140:143], a[96:99], a[32:35], v[148:151]\n"
    "ds_read_b128 a[68:71], v189 offset:49152\n"
    "v_cvt_pk_bf16_f32 v153, v170, v171\n"
    "v_mfma_f32_16x16x32_bf16 v[140:143], a[100:103], a[36:39], v[140:143]\n"
    "v_exp_f32 v174, v134\n"
    "v_mfma_f32_16x16x32_bf16 v[140:143], a[104:107], a[40:43], v[140:143]\n"
    "buffer_load_dwordx4 v203, s[80:83], s37 offen offset:3072 nt lds\n"
    "v_mfma_f32_16x16x32_bf16 v[140:143], a[108:111], a[44:47], v[140:143]\n"
    "s_add_u32 s37, s37, 0x4000\n"
    "v_exp_f32 v175, v135\n"
    "v_mfma_f32_16x16x32_bf16 v[140:143], a[112:115], a[48:51], v[140:143]\n"
    "ds_read_b64_tr_b16 a[166:167], v193 offset:24832\n"
    "ds_read_b64_tr_b16 a[168:169], v194 offset:16640\n"
    "v_mfma_f32_16x16x32_bf16 v[140:143], a[116:119], a[52:55], v[140:143]\n"
    "ds_read_b128 a[72:75], v190 offset:49152\n"
    "v_cvt_pk_bf16_f32 v156, v172, v173\n"
    "v_mfma_f32_16x16x32_bf16 v[140:143], a[120:123], a[56:59], v[140:143]\n"
    "s_mov_b32 m0, s74\n"
    "v_exp_f32 v233, v136\n"
    "v_mfma_f32_16x16x32_bf16 v[140:143], a[124:127], a[60:63], v[140:143]\n"
    "ds_read_b128 a[76:79], v191 offset:49152\n"
    "v_cvt_pk_bf16_f32 v157, v174, v175\n"
    "v_mfma_f32_16x16x32_bf16 v[176:179], v[184:187], v[160:163], v[176:179]\n"
    "v_exp_f32 v234, v137\n"
    "v_mfma_f32_16x16x32_bf16 v[180:183], v[184:187], v[164:167], v[180:183]\n"
    "s_waitcnt lgkmcnt(15)\n"
    "ds_read_b64_tr_b16 a[170:171], v194 offset:24832\n"
    "ds_read_b64_tr_b16 a[172:173], v195 offset:16640\n"
    "v_mfma_f32_16x16x32_bf16 v[8:11], a[128:131], v[164:167], v[8:11]\n"
    "buffer_load_dwordx4 v204, s[84:87], s38 offen offset:0 nt lds\n"
    "v_mfma_f32_16x16x32_bf16 v[0:3], a[128:131], v[160:163], v[0:3]\n"
    "v_exp_f32 v235, v138\n"
    "v_mfma_f32_16x16x32_bf16 v[4:7], a[132:135], v[160:163], v[4:7]\n"
    "ds_read_b128 a[80:83], v188 offset:49408\n"
    "v_cvt_pk_bf16_f32 v154, v233, v234\n"
    "v_mfma_f32_16x16x32_bf16 v[12:15], a[132:135], v[164:167], v[12:15]\n"
    "ds_read_b128 a[84:87], v189 offset:49408\n"
    "v_or3_b32 v208, v152, v153, v156\n"
    "v_mfma_f32_16x16x32_bf16 v[24:27], a[136:139], v[164:167], v[24:27]\n"
    "v_exp_f32 v236, v139\n"
    "v_mfma_f32_16x16x32_bf16 v[16:19], a[136:139], v[160:163], v[16:19]\n"
    "ds_read_b64_tr_b16 a[174:175], v195 offset:24832\n"
    "ds_read_b64_tr_b16 a[176:177], v196 offset:16640\n"
    "v_mfma_f32_16x16x32_bf16 v[20:23], a[140:143], v[160:163], v[20:23]\n"
    "v_exp_f32 v237, v140\n"
    "v_mfma_f32_16x16x32_bf16 v[28:31], a[140:143], v[164:167], v[28:31]\n"
    "s_waitcnt lgkmcnt(15)\n"
    "ds_read_b128 a[88:91], v190 offset:49408\n"
    "v_cvt_pk_bf16_f32 v155, v235, v236\n"
    "v_mfma_f32_16x16x32_bf16 v[40:43], a[144:147], v[164:167], v[40:43]\n"
    "buffer_load_dwordx4 v205, s[84:87], s38 offen offset:1024 nt lds\n"
    "v_mfma_f32_16x16x32_bf16 v[32:35], a[144:147], v[160:163], v[32:35]\n"
    "v_exp_f32 v238, v141\n"
    "v_mfma_f32_16x16x32_bf16 v[36:39], a[148:151], v[160:163], v[36:39]\n"
    "ds_read_b128 a[92:95], v191 offset:49408\n"
    "v_cvt_pk_bf16_f32 v158, v237, v238\n"
    "v_mfma_f32_16x16x32_bf16 v[44:47], a[148:151], v[164:167], v[44:47]\n"
    "ds_read_b64_tr_b16 a[178:179], v196 offset:24832\n"
    "ds_read_b64_tr_b16 a[180:181], v197 offset:16640\n"
    "v_mfma_f32_16x16x32_bf16 v[56:59], a[152:155], v[164:167], v[56:59]\n"
    "v_exp_f32 v239, v142\n"
    "v_mfma_f32_16x16x32_bf16 v[48:51], a[152:155], v[160:163], v[48:51]\n"
    "ds_read_b128 a[96:99], v188 offset:57344\n"
    "v_or3_b32 v208, v208, v157, v154\n"
    "v_mfma_f32_16x16x32_bf16 v[52:55], a[156:159], v[160:163], v[52:55]\n"
    "v_exp_f32 v240, v143\n"
    "v_mfma_f32_16x16x32_bf16 v[60:63], a[156:159], v[164:167], v[60:63]\n"
    "s_waitcnt lgkmcnt(6)\n"
    "ds_read_b128 a[100:103], v189 offset:57344\n"
    "v_cvt_pk_bf16_f32 v159, v239, v240\n"
    "v_mfma_f32_16x16x32_bf16 v[72:75], a[160:163], v[164:167], v[72:75]\n"
    "buffer_load_dwordx4 v206, s[84:87], s38 offen offset:2048 nt lds\n"
    "v_mfma_f32_16x16x32_bf16 v[64:67], a[160:163], v[160:163], v[64:67]\n"
    "ds_read_b64_tr_b16 a[182:183], v197 offset:24832\n"
    "ds_read_b64_tr_b16 a[184:185], v198 offset:16640\n"
    "v_mfma_f32_16x16x32_bf16 v[68:71], a[164:167], v[160:163], v[68:71]\n"
    "ds_read_b128 a[104:107], v190 offset:57344\n"
    "v_or3_b32 v208, v208, v155, v158\n"
    "v_mfma_f32_16x16x32_bf16 v[76:79], a[164:167], v[164:167], v[76:79]\n"
    "ds_read_b128 a[108:111], v191 offset:57344\n"
    "v_bitop3_b32 v208, v208, v159, s40 bitop3:0xa8\n"
    "v_mfma_f32_16x16x32_bf16 v[88:91], a[168:171], v[164:167], v[88:91]\n"
    "ds_read_b64_tr_b16 a[186:187], v198 offset:24832\n"
    "ds_read_b64_tr_b16 a[188:189], v199 offset:16640\n"
    "v_mfma_f32_16x16x32_bf16 v[80:83], a[168:171], v[160:163], v[80:83]\n"
    "ds_read_b128 a[112:115], v188 offset:57600\n"
    "v_cmp_ne_u32 vcc, 0, v208\n"
    "v_mfma_f32_16x16x32_bf16 v[84:87], a[172:175], v[160:163], v[84:87]\n"
    "ds_read_b128 a[116:119], v189 offset:57600\n"
    "v_mfma_f32_16x16x32_bf16 v[92:95], a[172:175], v[164:167], v[92:95]\n"
    "s_waitcnt lgkmcnt(2)\n"
    "ds_read_b64_tr_b16 a[190:191], v199 offset:24832\n"
    "v_mfma_f32_16x16x32_bf16 v[104:107], a[176:179], v[164:167], v[104:107]\n"
    "buffer_load_dwordx4 v207, s[84:87], s38 offen offset:3072 nt lds\n"
    "v_mfma_f32_16x16x32_bf16 v[96:99], a[176:179], v[160:163], v[96:99]\n"
    "ds_read_b128 a[120:123], v190 offset:57600\n"
    "s_add_u32 s38, s38, 0x4000\n"
    "v_mfma_f32_16x16x32_bf16 v[100:103], a[180:183], v[160:163], v[100:103]\n"
    "ds_read_b128 a[124:127], v191 offset:57600\n"
    "v_mfma_f32_16x16x32_bf16 v[108:111], a[180:183], v[164:167], v[108:111]\n"
    "v_mfma_f32_16x16x32_bf16 v[120:123], a[184:187], v[164:167], v[120:123]\n"
    "v_mfma_f32_16x16x32_bf16 v[112:115], a[184:187], v[160:163], v[112:115]\n"
    "s_waitcnt lgkmcnt(2)\n"
    "v_mfma_f32_16x16x32_bf16 v[116:119], a[188:191], v[160:163], v[116:119]\n"
    "v_mfma_f32_16x16x32_bf16 v[124:127], a[188:191], v[164:167], v[124:127]\n"
    "s_cbranch_vccnz LR2%=\n"
    "LB2%=:\n"
    "s_waitcnt lgkmcnt(0)\n"
    "v_mfma_f32_16x16x32_bf16 v[128:131], a[64:67], a[0:3], v[144:147]\n"
    "s_waitcnt vmcnt(16)\n"
    "s_barrier\n"
    "v_mfma_f32_16x16x32_bf16 v[128:131], a[68:71], a[4:7], v[128:131]\n"
    "ds_read_b64_tr_b16 a[128:129], v192 offset:32768\n"
    "s_mov_b32 m0, s95\n"
    "v_mfma_f32_16x16x32_bf16 v[128:131], a[72:75], a[8:11], v[128:131]\n"
    "buffer_load_dwordx4 v200, s[80:83], s37 offen offset:0 nt lds\n"
    "v_mfma_f32_16x16x32_bf16 v[128:131], a[76:79], a[12:15], v[128:131]\n"
    "ds_read_b64_tr_b16 a[130:131], v192 offset:40960\n"
    "v_mfma_f32_16x16x32_bf16 v[128:131], a[80:83], a[16:19], v[128:131]\n"
    "ds_read_b64_tr_b16 a[132:133], v193 offset:32768\n"
    "v_mfma_f32_16x16x32_bf16 v[128:131], a[84:87], a[20:23], v[128:131]\n"
    "ds_read_b64_tr_b16 a[134:135], v193 offset:40960\n"
    "v_mfma_f32_16x16x32_bf16 v[128:131], a[88:91], a[24:27], v[128:131]\n"
    "ds_read_b64_tr_b16 a[136:137], v194 offset:32768\n"
    "v_mfma_f32_16x16x32_bf16 v[128:131], a[92:95], a[28:31], v[128:131]\n"
    "ds_read_b64_tr_b16 a[138:139], v194 offset:40960\n"
    "v_mfma_f32_16x16x32_bf16 v[132:135], a[64:67], a[32:35], v[148:151]\n"
    "ds_read_b64_tr_b16 a[140:141], v195 offset:32768\n"
    "v_mfma_f32_16x16x32_bf16 v[132:135], a[68:71], a[36:39], v[132:135]\n"
    "ds_read_b64_tr_b16 a[142:143], v195 offset:40960\n"
    "ds_read_b64_tr_b16 a[144:145], v196 offset:32768\n"
    "v_mfma_f32_16x16x32_bf16 v[132:135], a[72:75], a[40:43], v[132:135]\n"
    "buffer_load_dwordx4 v201, s[80:83], s37 offen offset:1024 nt lds\n"
    "v_mfma_f32_16x16x32_bf16 v[132:135], a[76:79], a[44:47], v[132:135]\n"
    "v_exp_f32 v168, v128\n"
    "v_mfma_f32_16x16x32_bf16 v[132:135], a[80:83], a[48:51], v[132:135]\n"
    "ds_read_b64_tr_b16 a[146:147], v196 offset:40960\n"
    "ds_read_b64_tr_b16 a[148:149], v197 offset:32768\n"
    "v_mfma_f32_16x16x32_bf16 v[132:135], a[84:87], a[52:55], v[132:135]\n"
    "ds_read_b64_tr_b16 a[150:151], v197 offset:40960\n"
    "ds_read_b64_tr_b16 a[152:153], v198 offset:32768\n"
    "v_mfma_f32_16x16x32_bf16 v[132:135], a[88:91], a[56:59], v[132:135]\n"
    "v_exp_f32 v169, v129\n"
    "v_mfma_f32_16x16x32_bf16 v[132:135], a[92:95], a[60:63], v[132:135]\n"
    "ds_read_b64_tr_b16 a[154:155], v198 offset:40960\n"
    "ds_read_b64_tr_b16 a[156:157], v199 offset:32768\n"
    "v_mfma_f32_16x16x32_bf16 v[136:139], a[96:99], a[0:3], v[144:147]\n"
    "v_exp_f32 v170, v130\n"
    "v_mfma_f32_16x16x32_bf16 v[136:139], a[100:103], a[4:7], v[136:139]\n"
    "ds_read_b64_tr_b16 a[158:159], v199 offset:40960\n"
    "ds_read_b64_tr_b16 a[160:161], v192 offset:33024\n"
    "v_mfma_f32_16x16x32_bf16 v[136:139], a[104:107], a[8:11], v[136:139]\n"
    "buffer_load_dwordx4 v202, s[80:83], s37 offen offset:2048 nt lds\n"
    "v_mfma_f32_16x16x32_bf16 v[136:139], a[108:111], a[12:15], v[136:139]\n"
    "v_exp_f32 v171, v131\n"
    "v_mfma_f32_16x16x32_bf16 v[136:139], a[112:115], a[16:19], v[136:139]\n"
    "ds_read_b64_tr_b16 a[162:163], v192 offset:41216\n"
    "ds_read_b64_tr_b16 a[164:165], v193 offset:33024\n"
    "v_mfma_f32_16x16x32_bf16 v[136:139], a[116:119], a[20:23], v[136:139]\n"
    "v_exp_f32 v172, v132\n"
    "v_mfma_f32_16x16x32_bf16 v[136:139], a[120:123], a[24:27], v[136:139]\n"
    "ds_read_b128 a[64:67], v188 offset:0\n"
    "v_cvt_pk_bf16_f32 v160, v168, v169\n"
    "v_mfma_f32_16x16x32_bf16 v[136:139], a[124:127], a[28:31], v[136:139]\n"
    "v_exp_f32 v173, v133\n"
    "v_mfma_f32_16x16x32_bf16 v[140:143], a[96:99], a[32:35], v[148:151]\n"
    "ds_read_b128 a[68:71], v189 offset:0\n"
    "v_cvt_pk_bf16_f32 v161, v170, v171\n"
    "v_mfma_f32_16x16x32_bf16 v[140:143], a[100:103], a[36:39], v[140:143]\n"
    "v_exp_f32 v174, v134\n"
    "v_mfma_f32_16x16x32_bf16 v[140:143], a[104:107], a[40:43], v[140:143]\n"
    "buffer_load_dwordx4 v203, s[80:83], s37 offen offset:3072 nt lds\n"
    "v_mfma_f32_16x16x32_bf16 v[140:143], a[108:111], a[44:47], v[140:143]\n"
    "s_add_u32 s37, s37, 0x4000\n"
    "v_exp_f32 v175, v135\n"
    "v_mfma_f32_16x16x32_bf16 v[140:143], a[112:115], a[48:51], v[140:143]\n"
    "ds_read_b64_tr_b16 a[166:167], v193 offset:41216\n"
    "ds_read_b64_tr_b16 a[168:169], v194 offset:33024\n"
    "v_mfma_f32_16x16x32_bf16 v[140:143], a[116:119], a[52:55], v[140:143]\n"
    "ds_read_b128 a[72:75], v190 offset:0\n"
    "v_cvt_pk_bf16_f32 v164, v172, v173\n"
    "v_mfma_f32_16x16x32_bf16 v[140:143], a[120:123], a[56:59], v[140:143]\n"
    "s_mov_b32 m0, s75\n"
    "v_exp_f32 v233, v136\n"
    "v_mfma_f32_16x16x32_bf16 v[140:143], a[124:127], a[60:63], v[140:143]\n"
    "ds_read_b128 a[76:79], v191 offset:0\n"
    "v_cvt_pk_bf16_f32 v165, v174, v175\n"
    "v_mfma_f32_16x16x32_bf16 v[176:179], v[184:187], v[152:155], v[176:179]\n"
    "v_exp_f32 v234, v137\n"
    "v_mfma_f32_16x16x32_bf16 v[180:183], v[184:187], v[156:159], v[180:183]\n"
    "s_waitcnt lgkmcnt(15)\n"
    "ds_read_b64_tr_b16 a[170:171], v194 offset:41216\n"
    "ds_read_b64_tr_b16 a[172:173], v195 offset:33024\n"
    "v_mfma_f32_16x16x32_bf16 v[8:11], a[128:131], v[156:159], v[8:11]\n"
    "buffer_load_dwordx4 v204, s[84:87], s38 offen offset:0 nt lds\n"
    "v_mfma_f32_16x16x32_bf16 v[0:3], a[128:131], v[152:155], v[0:3]\n"
    "v_exp_f32 v235, v138\n"
    "v_mfma_f32_16x16x32_bf16 v[4:7], a[132:135], v[152:155], v[4:7]\n"
    "ds_read_b128 a[80:83], v188 offset:256\n"
    "v_cvt_pk_bf16_f32 v162, v233, v234\n"
    "v_mfma_f32_16x16x32_bf16 v[12:15], a[132:135], v[156:159], v[12:15]\n"
    "ds_read_b128 a[84:87], v189 offset:256\n"
    "v_or3_b32 v208, v160, v161, v164\n"
    "v_mfma_f32_16x16x32_bf16 v[24:27], a[136:139], v[156:159], v[24:27]\n"
    "v_exp_f32 v236, v139\n"
    "v_mfma_f32_16x16x32_bf16 v[16:19], a[136:139], v[152:155], v[16:19]\n"
    "ds_read_b64_tr_b16 a[174:175], v195 offset:41216\n"
    "ds_read_b64_tr_b16 a[176:177], v196 offset:33024\n"
    "v_mfma_f32_16x16x32_bf16 v[20:23], a[140:143], v[152:155], v[20:23]\n"
    "v_exp_f32 v237, v140\n"
    "v_mfma_f32_16x16x32_bf16 v[28:31], a[140:143], v[156:159], v[28:31]\n"
    "s_waitcnt lgkmcnt(15)\n"
    "ds_read_b128 a[88:91], v190 offset:256\n"
    "v_cvt_pk_bf16_f32 v163, v235, v236\n"
    "v_mfma_f32_16x16x32_bf16 v[40:43], a[144:147], v[156:159], v[40:43]\n"
    "buffer_load_dwordx4 v205, s[84:87], s38 offen offset:1024 nt lds\n"
    "v_mfma_f32_16x16x32_bf16 v[32:35], a[144:147], v[152:155], v[32:35]\n"
    "v_exp_f32 v238, v141\n"
    "v_mfma_f32_16x16x32_bf16 v[36:39], a[148:151], v[152:155], v[36:39]\n"
    "ds_read_b128 a[92:95], v191 offset:256\n"
    "v_cvt_pk_bf16_f32 v166, v237, v238\n"
    "v_mfma_f32_16x16x32_bf16 v[44:47], a[148:151], v[156:159], v[44:47]\n"
    "ds_read_b64_tr_b16 a[178:179], v196 offset:41216\n"
    "ds_read_b64_tr_b16 a[180:181], v197 offset:33024\n"
    "v_mfma_f32_16x16x32_bf16 v[56:59], a[152:155], v[156:159], v[56:59]\n"
    "v_exp_f32 v239, v142\n"
    "v_mfma_f32_16x16x32_bf16 v[48:51], a[152:155], v[152:155], v[48:51]\n"
    "ds_read_b128 a[96:99], v188 offset:8192\n"
    "v_or3_b32 v208, v208, v165, v162\n"
    "v_mfma_f32_16x16x32_bf16 v[52:55], a[156:159], v[152:155], v[52:55]\n"
    "v_exp_f32 v240, v143\n"
    "v_mfma_f32_16x16x32_bf16 v[60:63], a[156:159], v[156:159], v[60:63]\n"
    "s_waitcnt lgkmcnt(6)\n"
    "ds_read_b128 a[100:103], v189 offset:8192\n"
    "v_cvt_pk_bf16_f32 v167, v239, v240\n"
    "v_mfma_f32_16x16x32_bf16 v[72:75], a[160:163], v[156:159], v[72:75]\n"
    "buffer_load_dwordx4 v206, s[84:87], s38 offen offset:2048 nt lds\n"
    "v_mfma_f32_16x16x32_bf16 v[64:67], a[160:163], v[152:155], v[64:67]\n"
    "ds_read_b64_tr_b16 a[182:183], v197 offset:41216\n"
    "ds_read_b64_tr_b16 a[184:185], v198 offset:33024\n"
    "v_mfma_f32_16x16x32_bf16 v[68:71], a[164:167], v[152:155], v[68:71]\n"
    "ds_read_b128 a[104:107], v190 offset:8192\n"
    "v_or3_b32 v208, v208, v163, v166\n"
    "v_mfma_f32_16x16x32_bf16 v[76:79], a[164:167], v[156:159], v[76:79]\n"
    "ds_read_b128 a[108:111], v191 offset:8192\n"
    "v_bitop3_b32 v208, v208, v167, s40 bitop3:0xa8\n"
    "v_mfma_f32_16x16x32_bf16 v[88:91], a[168:171], v[156:159], v[88:91]\n"
    "ds_read_b64_tr_b16 a[186:187], v198 offset:41216\n"
    "ds_read_b64_tr_b16 a[188:189], v199 offset:33024\n"
    "v_mfma_f32_16x16x32_bf16 v[80:83], a[168:171], v[152:155], v[80:83]\n"
    "ds_read_b128 a[112:115], v188 offset:8448\n"
    "v_cmp_ne_u32 vcc, 0, v208\n"
    "v_mfma_f32_16x16x32_bf16 v[84:87], a[172:175], v[152:155], v[84:87]\n"
    "ds_read_b128 a[116:119], v189 offset:8448\n"
    "v_mfma_f32_16x16x32_bf16 v[92:95], a[172:175], v[156:159], v[92:95]\n"
    "s_waitcnt lgkmcnt(2)\n"
    "ds_read_b64_tr_b16 a[190:191], v199 offset:41216\n"
    "v_mfma_f32_16x16x32_bf16 v[104:107], a[176:179], v[156:159], v[104:107]\n"
    "buffer_load_dwordx4 v207, s[84:87], s38 offen offset:3072 nt lds\n"
    "v_mfma_f32_16x16x32_bf16 v[96:99], a[176:179], v[152:155], v[96:99]\n"
    "ds_read_b128 a[120:123], v190 offset:8448\n"
    "s_add_u32 s38, s38, 0x4000\n"
    "v_mfma_f32_16x16x32_bf16 v[100:103], a[180:183], v[152:155], v[100:103]\n"
    "ds_read_b128 a[124:127], v191 offset:8448\n"
    "v_mfma_f32_16x16x32_bf16 v[108:111], a[180:183], v[156:159], v[108:111]\n"
    "v_mfma_f32_16x16x32_bf16 v[120:123], a[184:187], v[156:159], v[120:123]\n"
    "v_mfma_f32_16x16x32_bf16 v[112:115], a[184:187], v[152:155], v[112:115]\n"
    "s_waitcnt lgkmcnt(2)\n"
    "v_mfma_f32_16x16x32_bf16 v[116:119], a[188:191], v[152:155], v[116:119]\n"
    "v_mfma_f32_16x16x32_bf16 v[124:127], a[188:191], v[156:159], v[124:127]\n"
    "s_cbranch_vccnz LR3%=\n"
    "LB3%=:\n"
    "s_mov_b32 s39, 5\n"
    "LLOOP%=:\n"
    "s_waitcnt lgkmcnt(0)\n"
    "v_mfma_f32_16x16x32_bf16 v[128:131], a[64:67], a[0:3], v[144:147]\n"
    "s_waitcnt vmcnt(16)\n"
    "s_barrier\n"
    "v_mfma_f32_16x16x32_bf16 v[128:131], a[68:71], a[4:7], v[128:131]\n"
    "ds_read_b64_tr_b16 a[128:129], v192 offset:49152\n"
    "s_mov_b32 m0, s92\n"
    "v_mfma_f32_16x16x32_bf16 v[128:131], a[72:75], a[8:11], v[128:131]\n"
    "buffer_load_dwordx4 v200, s[80:83], s37 offen offset:0 nt lds\n"
    "v_mfma_f32_16x16x32_bf16 v[128:131], a[76:79], a[12:15], v[128:131]\n"
    "ds_read_b64_tr_b16 a[130:131], v192 offset:57344\n"
    "v_mfma_f32_16x16x32_bf16 v[128:131], a[80:83], a[16:19], v[128:131]\n"
    "ds_read_b64_tr_b16 a[132:133], v193 offset:49152\n"
    "v_mfma_f32_16x16x32_bf16 v[128:131], a[84:87], a[20:23], v[128:131]\n"
    "ds_read_b64_tr_b16 a[134:135], v193 offset:57344\n"
    "v_mfma_f32_16x16x32_bf16 v[128:131], a[88:91], a[24:27], v[128:131]\n"
    "ds_read_b64_tr_b16 a[136:137], v194 offset:49152\n"
    "v_mfma_f32_16x16x32_bf16 v[128:131], a[92:95], a[28:31], v[128:131]\n"
    "ds_read_b64_tr_b16 a[138:139], v194 offset:57344\n"
    "v_mfma_f32_16x16x32_bf16 v[132:135], a[64:67], a[32:35], v[148:151]\n"
    "ds_read_b64_tr_b16 a[140:141], v195 offset:49152\n"
    "v_mfma_f32_16x16x32_bf16 v[132:135], a[68:71], a[36:39], v[132:135]\n"
    "ds_read_b64_tr_b16 a[142:143], v195 offset:57344\n"
    "ds_read_b64_tr_b16 a[144:145], v196 offset:49152\n"
    "v_mfma_f32_16x16x32_bf16 v[132:135], a[72:75], a[40:43], v[132:135]\n"
    "buffer_load_dwordx4 v201, s[80:83], s37 offen offset:1024 nt lds\n"
    "v_mfma_f32_16x16x32_bf16 v[132:135], a[76:79], a[44:47], v[132:135]\n"
    "v_exp_f32 v168, v128\n"
    "v_mfma_f32_16x16x32_bf16 v[132:135], a[80:83], a[48:51], v[132:135]\n"
    "ds_read_b64_tr_b16 a[146:147], v196 offset:57344\n"
    "ds_read_b64_tr_b16 a[148:149], v197 offset:49152\n"
    "v_mfma_f32_16x16x32_bf16 v[132:135], a[84:87], a[52:55], v[132:135]\n"
    "ds_read_b64_tr_b16 a[150:151], v197 offset:57344\n"
    "ds_read_b64_tr_b16 a[152:153], v198 offset:49152\n"
    "v_mfma_f32_16x16x32_bf16 v[132:135], a[88:91], a[56:59], v[132:135]\n"
    "v_exp_f32 v169, v129\n"
    "v_mfma_f32_16x16x32_bf16 v[132:135], a[92:95], a[60:63], v[132:135]\n"
    "ds_read_b64_tr_b16 a[154:155], v198 offset:57344\n"
    "ds_read_b64_tr_b16 a[156:157], v199 offset:49152\n"
    "v_mfma_f32_16x16x32_bf16 v[136:139], a[96:99], a[0:3], v[144:147]\n"
    "v_exp_f32 v170, v130\n"
    "v_mfma_f32_16x16x32_bf16 v[136:139], a[100:103], a[4:7], v[136:139]\n"
    "ds_read_b64_tr_b16 a[158:159], v199 offset:57344\n"
    "ds_read_b64_tr_b16 a[160:161], v192 offset:49408\n"
    "v_mfma_f32_16x16x32_bf16 v[136:139], a[104:107], a[8:11], v[136:139]\n"
    "buffer_load_dwordx4 v202, s[80:83], s37 offen offset:2048 nt lds\n"
    "v_mfma_f32_16x16x32_bf16 v[136:139], a[108:111], a[12:15], v[136:139]\n"
    "v_exp_f32 v171, v131\n"
    "v_mfma_f32_16x16x32_bf16 v[136:139], a[112:115], a[16:19], v[136:139]\n"
    "ds_read_b64_tr_b16 a[162:163], v192 offset:57600\n"
    "ds_read_b64_tr_b16 a[164:165], v193 offset:49408\n"
    "v_mfma_f32_16x16x32_bf16 v[136:139], a[116:119], a[20:23], v[136:139]\n"
    "v_exp_f32 v172, v132\n"
    "v_mfma_f32_16x16x32_bf16 v[136:139], a[120:123], a[24:27], v[136:139]\n"
    "ds_read_b128 a[64:67], v188 offset:16384\n"
    "v_cvt_pk_bf16_f32 v152, v168, v169\n"
    "v_mfma_f32_16x16x32_bf16 v[136:139], a[124:127], a[28:31], v[136:139]\n"
    "v_exp_f32 v173, v133\n"
    "v_mfma_f32_16x16x32_bf16 v[140:143], a[96:99], a[32:35], v[148:151]\n"
    "ds_read_b128 a[68:71], v189 offset:16384\n"
    "v_cvt_pk_bf16_f32 v153, v170, v171\n"
    "v_mfma_f32_16x16x32_bf16 v[140:143], a[100:103], a[36:39], v[140:143]\n"
    "v_exp_f32 v174, v134\n"
    "v_mfma_f32_16x16x32_bf16 v[140:143], a[104:107], a[40:43], v[140:143]\n"
    "buffer_load_dwordx4 v203, s[80:83], s37 offen offset:3072 nt lds\n"
    "v_mfma_f32_16x16x32_bf16 v[140:143], a[108:111], a[44:47], v[140:143]\n"
    "s_add_u32 s37, s37, 0x4000\n"
    "v_exp_f32 v175, v135\n"
    "v_mfma_f32_16x16x32_bf16 v[140:143], a[112:115], a[48:51], v[140:143]\n"
    "ds_read_b64_tr_b16 a[166:167], v193 offset:57600\n"
    "ds_read_b64_tr_b16 a[168:169], v194 offset:49408\n"
    "v_mfma_f32_16x16x32_bf16 v[140:143], a[116:119], a[52:55], v[140:143]\n"
    "ds_read_b128 a[72:75], v190 offset:16384\n"
    "v_cvt_pk_bf16_f32 v156, v172, v173\n"
    "v_mfma_f32_16x16x32_bf16 v[140:143], a[120:123], a[56:59], v[140:143]\n"
    "s_mov_b32 m0, s72\n"
    "v_exp_f32 v233, v136\n"
    "v_mfma_f32_16x16x32_bf16 v[140:143], a[124:127], a[60:63], v[140:143]\n"
    "ds_read_b128 a[76:79], v191 offset:16384\n"
    "v_cvt_pk_bf16_f32 v157, v174, v175\n"
    "v_mfma_f32_16x16x32_bf16 v[176:179], v[184:187], v[160:163], v[176:179]\n"
    "v_exp_f32 v234, v137\n"
    "v_mfma_f32_16x16x32_bf16 v[180:183], v[184:187], v[164:167], v[180:183]\n"
    "s_waitcnt lgkmcnt(15)\n"
    "ds_read_b64_tr_b16 a[170:171], v194 offset:57600\n"
    "ds_read_b64_tr_b16 a[172:173], v195 offset:49408\n"
    "v_mfma_f32_16x16x32_bf16 v[8:11], a[128:131], v[164:167], v[8:11]\n"
    "buffer_load_dwordx4 v204, s[84:87], s38 offen offset:0 nt lds\n"
    "v_mfma_f32_16x16x32_bf16 v[0:3], a[128:131], v[160:163], v[0:3]\n"
    "v_exp_f32 v235, v138\n"
    "v_mfma_f32_16x16x32_bf16 v[4:7], a[132:135], v[160:163], v[4:7]\n"
    "ds_read_b128 a[80:83], v188 offset:16640\n"
    "v_cvt_pk_bf16_f32 v154, v233, v234\n"
    "v_mfma_f32_16x16x32_bf16 v[12:15], a[132:135], v[164:167], v[12:15]\n"
    "ds_read_b128 a[84:87], v189 offset:16640\n"
    "v_or3_b32 v208, v152, v153, v156\n"
    "v_mfma_f32_16x16x32_bf16 v[24:27], a[136:139], v[164:167], v[24:27]\n"
    "v_exp_f32 v236, v139\n"
    "v_mfma_f32_16x16x32_bf16 v[16:19], a[136:139], v[160:163], v[16:19]\n"
    "ds_read_b64_tr_b16 a[174:175], v195 offset:57600\n"
    "ds_read_b64_tr_b16 a[176:177], v196 offset:49408\n"
    "v_mfma_f32_16x16x32_bf16 v[20:23], a[140:143], v[160:163], v[20:23]\n"
    "v_exp_f32 v237, v140\n"
    "v_mfma_f32_16x16x32_bf16 v[28:31], a[140:143], v[164:167], v[28:31]\n"
    "s_waitcnt lgkmcnt(15)\n"
    "ds_read_b128 a[88:91], v190 offset:16640\n"
    "v_cvt_pk_bf16_f32 v155, v235, v236\n"
    "v_mfma_f32_16x16x32_bf16 v[40:43], a[144:147], v[164:167], v[40:43]\n"
    "buffer_load_dwordx4 v205, s[84:87], s38 offen offset:1024 nt lds\n"
    "v_mfma_f32_16x16x32_bf16 v[32:35], a[144:147], v[160:163], v[32:35]\n"
    "v_exp_f32 v238, v141\n"
    "v_mfma_f32_16x16x32_bf16 v[36:39], a[148:151], v[160:163], v[36:39]\n"
    "ds_read_b128 a[92:95], v191 offset:16640\n"
    "v_cvt_pk_bf16_f32 v158, v237, v238\n"
    "v_mfma_f32_16x16x32_bf16 v[44:47], a[148:151], v[164:167], v[44:47]\n"
    "ds_read_b64_tr_b16 a[178:179], v196 offset:57600\n"
    "ds_read_b64_tr_b16 a[180:181], v197 offset:49408\n"
    "v_mfma_f32_16x16x32_bf16 v[56:59], a[152:155], v[164:167], v[56:59]\n"
    "v_exp_f32 v239, v142\n"
    "v_mfma_f32_16x16x32_bf16 v[48:51], a[152:155], v[160:163], v[48:51]\n"
    "ds_read_b128 a[96:99], v188 offset:24576\n"
    "v_or3_b32 v208, v208, v157, v154\n"
    "v_mfma_f32_16x16x32_bf16 v[52:55], a[156:159], v[160:163], v[52:55]\n"
    "v_exp_f32 v240, v143\n"
    "v_mfma_f32_16x16x32_bf16 v[60:63], a[156:159], v[164:167], v[60:63]\n"
    "s_waitcnt lgkmcnt(6)\n"
    "ds_read_b128 a[100:103], v189 offset:24576\n"
    "v_cvt_pk_bf16_f32 v159, v239, v240\n"
    "v_mfma_f32_16x16x32_bf16 v[72:75], a[160:163], v[164:167], v[72:75]\n"
    "buffer_load_dwordx4 v206, s[84:87], s38 offen offset:2048 nt lds\n"
    "v_mfma_f32_16x16x32_bf16 v[64:67], a[160:163], v[160:163], v[64:67]\n"
    "ds_read_b64_tr_b16 a[182:183], v197 offset:57600\n"
    "ds_read_b64_tr_b16 a[184:185], v198 offset:49408\n"
    "v_mfma_f32_16x16x32_bf16 v[68:71], a[164:167], v[160:163], v[68:71]\n"
    "ds_read_b128 a[104:107], v190 offset:24576\n"
    "v_or3_b32 v208, v208, v155, v158\n"
    "v_mfma_f32_16x16x32_bf16 v[76:79], a[164:167], v[164:167], v[76:79]\n"
    "ds_read_b128 a[108:111], v191 offset:24576\n"
    "v_bitop3_b32 v208, v208, v159, s40 bitop3:0xa8\n"
    "v_mfma_f32_16x16x32_bf16 v[88:91], a[168:171], v[164:167], v[88:91]\n"
    "ds_read_b64_tr_b16 a[186:187], v198 offset:57600\n"
    "ds_read_b64_tr_b16 a[188:189], v199 offset:49408\n"
    "v_mfma_f32_16x16x32_bf16 v[80:83], a[168:171], v[160:163], v[80:83]\n"
    "ds_read_b128 a[112:115], v188 offset:24832\n"
    "v_cmp_ne_u32 vcc, 0, v208\n"
    "v_mfma_f32_16x16x32_bf16 v[84:87], a[172:175], v[160:163], v[84:87]\n"
    "ds_read_b128 a[116:119], v189 offset:24832\n"
    "v_mfma_f32_16x16x32_bf16 v[92:95], a[172:175], v[164:167], v[92:95]\n"
    "s_waitcnt lgkmcnt(2)\n"
    "ds_read_b64_tr_b16 a[190:191], v199 offset:57600\n"
    "v_mfma_f32_16x16x32_bf16 v[104:107], a[176:179], v[164:167], v[104:107]\n"
    "buffer_load_dwordx4 v207, s[84:87], s38 offen offset:3072 nt lds\n"
    "v_mfma_f32_16x16x32_bf16 v[96:99], a[176:179], v[160:163], v[96:99]\n"
    "ds_read_b128 a[120:123], v190 offset:24832\n"
    "s_add_u32 s38, s38, 0x4000\n"
    "v_mfma_f32_16x16x32_bf16 v[100:103], a[180:183], v[160:163], v[100:103]\n"
    "ds_read_b128 a[124:127], v191 offset:24832\n"
    "v_mfma_f32_16x16x32_bf16 v[108:111], a[180:183], v[164:167], v[108:111]\n"
    "v_mfma_f32_16x16x32_bf16 v[120:123], a[184:187], v[164:167], v[120:123]\n"
    "v_mfma_f32_16x16x32_bf16 v[112:115], a[184:187], v[160:163], v[112:115]\n"
    "s_waitcnt lgkmcnt(2)\n"
    "v_mfma_f32_16x16x32_bf16 v[116:119], a[188:191], v[160:163], v[116:119]\n"
    "v_mfma_f32_16x16x32_bf16 v[124:127], a[188:191], v[164:167], v[124:127]\n"
    "s_cbranch_vccnz LRL0%=\n"
    "LBL0%=:\n"
    "s_waitcnt lgkmcnt(0)\n"
    "v_mfma_f32_16x16x32_bf16 v[128:131], a[64:67], a[0:3], v[144:147]\n"
    "s_waitcnt vmcnt(16)\n"
    "s_barrier\n"
    "v_mfma_f32_16x16x32_bf16 v[128:131], a[68:71], a[4:7], v[128:131]\n"
    "ds_read_b64_tr_b16 a[128:129], v192 offset:0\n"
    "s_mov_b32 m0, s93\n"
    "v_mfma_f32_16x16x32_bf16 v[128:131], a[72:75], a[8:11], v[128:131]\n"
    "buffer_load_dwordx4 v200, s[80:83], s37 offen offset:0 nt lds\n"
    "v_mfma_f32_16x16x32_bf16 v[128:131], a[76:79], a[12:15], v[128:131]\n"
    "ds_read_b64_tr_b16 a[130:131], v192 offset:8192\n"
    "v_mfma_f32_16x16x32_bf16 v[128:131], a[80:83], a[16:19], v[128:131]\n"
    "ds_read_b64_tr_b16 a[132:133], v193 offset:0\n"
    "v_mfma_f32_16x16x32_bf16 v[128:131], a[84:87], a[20:23], v[128:131]\n"
    "ds_read_b64_tr_b16 a[134:135], v193 offset:8192\n"
    "v_mfma_f32_16x16x32_bf16 v[128:131], a[88:91], a[24:27], v[128:131]\n"
    "ds_read_b64_tr_b16 a[136:137], v194 offset:0\n"
    "v_mfma_f32_16x16x32_bf16 v[128:131], a[92:95], a[28:31], v[128:131]\n"
    "ds_read_b64_tr_b16 a[138:139], v194 offset:8192\n"
    "v_mfma_f32_16x16x32_bf16 v[132:135], a[64:67], a[32:35], v[148:151]\n"
    "ds_read_b64_tr_b16 a[140:141], v195 offset:0\n"
    "v_mfma_f32_16x16x32_bf16 v[132:135], a[68:71], a[36:39], v[132:135]\n"
    "ds_read_b64_tr_b16 a[142:143], v195 offset:8192\n"
    "ds_read_b64_tr_b16 a[144:145], v196 offset:0\n"
    "v_mfma_f32_16x16x32_bf16 v[132:135], a[72:75], a[40:43], v[132:135]\n"
    "buffer_load_dwordx4 v201, s[80:83], s37 offen offset:1024 nt lds\n"
    "v_mfma_f32_16x16x32_bf16 v[132:135], a[76:79], a[44:47], v[132:135]\n"
    "v_exp_f32 v168, v128\n"
    "v_mfma_f32_16x16x32_bf16 v[132:135], a[80:83], a[48:51], v[132:135]\n"
    "ds_read_b64_tr_b16 a[146:147], v196 offset:8192\n"
    "ds_read_b64_tr_b16 a[148:149], v197 offset:0\n"
    "v_mfma_f32_16x16x32_bf16 v[132:135], a[84:87], a[52:55], v[132:135]\n"
    "ds_read_b64_tr_b16 a[150:151], v197 offset:8192\n"
    "ds_read_b64_tr_b16 a[152:153], v198 offset:0\n"
    "v_mfma_f32_16x16x32_bf16 v[132:135], a[88:91], a[56:59], v[132:135]\n"
    "v_exp_f32 v169, v129\n"
    "v_mfma_f32_16x16x32_bf16 v[132:135], a[92:95], a[60:63], v[132:135]\n"
    "ds_read_b64_tr_b16 a[154:155], v198 offset:8192\n"
    "ds_read_b64_tr_b16 a[156:157], v199 offset:0\n"
    "v_mfma_f32_16x16x32_bf16 v[136:139], a[96:99], a[0:3], v[144:147]\n"
    "v_exp_f32 v170, v130\n"
    "v_mfma_f32_16x16x32_bf16 v[136:139], a[100:103], a[4:7], v[136:139]\n"
    "ds_read_b64_tr_b16 a[158:159], v199 offset:8192\n"
    "ds_read_b64_tr_b16 a[160:161], v192 offset:256\n"
    "v_mfma_f32_16x16x32_bf16 v[136:139], a[104:107], a[8:11], v[136:139]\n"
    "buffer_load_dwordx4 v202, s[80:83], s37 offen offset:2048 nt lds\n"
    "v_mfma_f32_16x16x32_bf16 v[136:139], a[108:111], a[12:15], v[136:139]\n"
    "v_exp_f32 v171, v131\n"
    "v_mfma_f32_16x16x32_bf16 v[136:139], a[112:115], a[16:19], v[136:139]\n"
    "ds_read_b64_tr_b16 a[162:163], v192 offset:8448\n"
    "ds_read_b64_tr_b16 a[164:165], v193 offset:256\n"
    "v_mfma_f32_16x16x32_bf16 v[136:139], a[116:119], a[20:23], v[136:139]\n"
    "v_exp_f32 v172, v132\n"
    "v_mfma_f32_16x16x32_bf16 v[136:139], a[120:123], a[24:27], v[136:139]\n"
    "ds_read_b128 a[64:67], v188 offset:32768\n"
    "v_cvt_pk_bf16_f32 v160, v168, v169\n"
    "v_mfma_f32_16x16x32_bf16 v[136:139], a[124:127], a[28:31], v[136:139]\n"
    "v_exp_f32 v173, v133\n"
    "v_mfma_f32_16x16x32_bf16 v[140:143], a[96:99], a[32:35], v[148:151]\n"
    "ds_read_b128 a[68:71], v189 offset:32768\n"
    "v_cvt_pk_bf16_f32 v161, v170, v171\n"
    "v_mfma_f32_16x16x32_bf16 v[140:143], a[100:103], a[36:39], v[140:143]\n"
    "v_exp_f32 v174, v134\n"
    "v_mfma_f32_16x16x32_bf16 v[140:143], a[104:107], a[40:43], v[140:143]\n"
    "buffer_load_dwordx4 v203, s[80:83], s37 offen offset:3072 nt lds\n"
    "v_mfma_f32_16x16x32_bf16 v[140:143], a[108:111], a[44:47], v[140:143]\n"
    "s_add_u32 s37, s37, 0x4000\n"
    "v_exp_f32 v175, v135\n"
    "v_mfma_f32_16x16x32_bf16 v[140:143], a[112:115], a[48:51], v[140:143]\n"
    "ds_read_b64_tr_b16 a[166:167], v193 offset:8448\n"
    "ds_read_b64_tr_b16 a[168:169], v194 offset:256\n"
    "v_mfma_f32_16x16x32_bf16 v[140:143], a[116:119], a[52:55], v[140:143]\n"
    "ds_read_b128 a[72:75], v190 offset:32768\n"
    "v_cvt_pk_bf16_f32 v164, v172, v173\n"
    "v_mfma_f32_16x16x32_bf16 v[140:143], a[120:123], a[56:59], v[140:143]\n"
    "s_mov_b32 m0, s73\n"
    "v_exp_f32 v233, v136\n"
    "v_mfma_f32_16x16x32_bf16 v[140:143], a[124:127], a[60:63], v[140:143]\n"
    "ds_read_b128 a[76:79], v191 offset:32768\n"
    "v_cvt_pk_bf16_f32 v165, v174, v175\n"
    "v_mfma_f32_16x16x32_bf16 v[176:179], v[184:187], v[152:155], v[176:179]\n"
    "v_exp_f32 v234, v137\n"
    "v_mfma_f32_16x16x32_bf16 v[180:183], v[184:187], v[156:159], v[180:183]\n"
    "s_waitcnt lgkmcnt(15)\n"
    "ds_read_b64_tr_b16 a[170:171], v194 offset:8448\n"
    "ds_read_b64_tr_b16 a[172:173], v195 offset:256\n"
    "v_mfma_f32_16x16x32_bf16 v[8:11], a[128:131], v[156:159], v[8:11]\n"
    "buffer_load_dwordx4 v204, s[84:87], s38 offen offset:0 nt lds\n"
    "v_mfma_f32_16x16x32_bf16 v[0:3], a[128:131], v[152:155], v[0:3]\n"
    "v_exp_f32 v235, v138\n"
    "v_mfma_f32_16x16x32_bf16 v[4:7], a[132:135], v[152:155], v[4:7]\n"
    "ds_read_b128 a[80:83], v188 offset:33024\n"
    "v_cvt_pk_bf16_f32 v162, v233, v234\n"
    "v_mfma_f32_16x16x32_bf16 v[12:15], a[132:135], v[156:159], v[12:15]\n"
    "ds_read_b128 a[84:87], v189 offset:33024\n"
    "v_or3_b32 v208, v160, v161, v164\n"
    "v_mfma_f32_16x16x32_bf16 v[24:27], a[136:139], v[156:159], v[24:27]\n"
    "v_exp_f32 v236, v139\n"
    "v_mfma_f32_16x16x32_bf16 v[16:19], a[136:139], v[152:155], v[16:19]\n"
    "ds_read_b64_tr_b16 a[174:175], v195 offset:8448\n"
    "ds_read_b64_tr_b16 a[176:177], v196 offset:256\n"
    "v_mfma_f32_16x16x32_bf16 v[20:23], a[140:143], v[152:155], v[20:23]\n"
    "v_exp_f32 v237, v140\n"
    "v_mfma_f32_16x16x32_bf16 v[28:31], a[140:143], v[156:159], v[28:31]\n"
    "s_waitcnt lgkmcnt(15)\n"
    "ds_read_b128 a[88:91], v190 offset:33024\n"
    "v_cvt_pk_bf16_f32 v163, v235, v236\n"
    "v_mfma_f32_16x16x32_bf16 v[40:43], a[144:147], v[156:159], v[40:43]\n"
    "buffer_load_dwordx4 v205, s[84:87], s38 offen offset:1024 nt lds\n"
    "v_mfma_f32_16x16x32_bf16 v[32:35], a[144:147], v[152:155], v[32:35]\n"
    "v_exp_f32 v238, v141\n"
    "v_mfma_f32_16x16x32_bf16 v[36:39], a[148:151], v[152:155], v[36:39]\n"
    "ds_read_b128 a[92:95], v191 offset:33024\n"
    "v_cvt_pk_bf16_f32 v166, v237, v238\n"
    "v_mfma_f32_16x16x32_bf16 v[44:47], a[148:151], v[156:159], v[44:47]\n"
    "ds_read_b64_tr_b16 a[178:179], v196 offset:8448\n"
    "ds_read_b64_tr_b16 a[180:181], v197 offset:256\n"
    "v_mfma_f32_16x16x32_bf16 v[56:59], a[152:155], v[156:159], v[56:59]\n"
    "v_exp_f32 v239, v142\n"
    "v_mfma_f32_16x16x32_bf16 v[48:51], a[152:155], v[152:155], v[48:51]\n"
    "ds_read_b128 a[96:99], v188 offset:40960\n"
    "v_or3_b32 v208, v208, v165, v162\n"
    "v_mfma_f32_16x16x32_bf16 v[52:55], a[156:159], v[152:155], v[52:55]\n"
    "v_exp_f32 v240, v143\n"
    "v_mfma_f32_16x16x32_bf16 v[60:63], a[156:159], v[156:159], v[60:63]\n"
    "s_waitcnt lgkmcnt(6)\n"
    "ds_read_b128 a[100:103], v189 offset:40960\n"
    "v_cvt_pk_bf16_f32 v167, v239, v240\n"
    "v_mfma_f32_16x16x32_bf16 v[72:75], a[160:163], v[156:159], v[72:75]\n"
    "buffer_load_dwordx4 v206, s[84:87], s38 offen offset:2048 nt lds\n"
    "v_mfma_f32_16x16x32_bf16 v[64:67], a[160:163], v[152:155], v[64:67]\n"
    "ds_read_b64_tr_b16 a[182:183], v197 offset:8448\n"
    "ds_read_b64_tr_b16 a[184:185], v198 offset:256\n"
    "v_mfma_f32_16x16x32_bf16 v[68:71], a[164:167], v[152:155], v[68:71]\n"
    "ds_read_b128 a[104:107], v190 offset:40960\n"
    "v_or3_b32 v208, v208, v163, v166\n"
    "v_mfma_f32_16x16x32_bf16 v[76:79], a[164:167], v[156:159], v[76:79]\n"
    "ds_read_b128 a[108:111], v191 offset:40960\n"
    "v_bitop3_b32 v208, v208, v167, s40 bitop3:0xa8\n"
    "v_mfma_f32_16x16x32_bf16 v[88:91], a[168:171], v[156:159], v[88:91]\n"
    "ds_read_b64_tr_b16 a[186:187], v198 offset:8448\n"
    "ds_read_b64_tr_b16 a[188:189], v199 offset:256\n"
    "v_mfma_f32_16x16x32_bf16 v[80:83], a[168:171], v[152:155], v[80:83]\n"
    "ds_read_b128 a[112:115], v188 offset:41216\n"
    "v_cmp_ne_u32 vcc, 0, v208\n"
    "v_mfma_f32_16x16x32_bf16 v[84:87], a[172:175], v[152:155], v[84:87]\n"
    "ds_read_b128 a[116:119], v189 offset:41216\n"
    "v_mfma_f32_16x16x32_bf16 v[92:95], a[172:175], v[156:159], v[92:95]\n"
    "s_waitcnt lgkmcnt(2)\n"
    "ds_read_b64_tr_b16 a[190:191], v199 offset:8448\n"
    "v_mfma_f32_16x16x32_bf16 v[104:107], a[176:179], v[156:159], v[104:107]\n"
    "buffer_load_dwordx4 v207, s[84:87], s38 offen offset:3072 nt lds\n"
    "v_mfma_f32_16x16x32_bf16 v[96:99], a[176:179], v[152:155], v[96:99]\n"
    "ds_read_b128 a[120:123], v190 offset:41216\n"
    "s_add_u32 s38, s38, 0x4000\n"
    "v_mfma_f32_16x16x32_bf16 v[100:103], a[180:183], v[152:155], v[100:103]\n"
    "ds_read_b128 a[124:127], v191 offset:41216\n"
    "v_mfma_f32_16x16x32_bf16 v[108:111], a[180:183], v[156:159], v[108:111]\n"
    "v_mfma_f32_16x16x32_bf16 v[120:123], a[184:187], v[156:159], v[120:123]\n"
    "v_mfma_f32_16x16x32_bf16 v[112:115], a[184:187], v[152:155], v[112:115]\n"
    "s_waitcnt lgkmcnt(2)\n"
    "v_mfma_f32_16x16x32_bf16 v[116:119], a[188:191], v[152:155], v[116:119]\n"
    "v_mfma_f32_16x16x32_bf16 v[124:127], a[188:191], v[156:159], v[124:127]\n"
    "s_cbranch_vccnz LRL1%=\n"
    "LBL1%=:\n"
    "s_waitcnt lgkmcnt(0)\n"
    "v_mfma_f32_16x16x32_bf16 v[128:131], a[64:67], a[0:3], v[144:147]\n"
    "s_waitcnt vmcnt(16)\n"
    "s_barrier\n"
    "v_mfma_f32_16x16x32_bf16 v[128:131], a[68:71], a[4:7], v[128:131]\n"
    "ds_read_b64_tr_b16 a[128:129], v192 offset:16384\n"
    "s_mov_b32 m0, s94\n"
    "v_mfma_f32_16x16x32_bf16 v[128:131], a[72:75], a[8:11], v[128:131]\n"
    "buffer_load_dwordx4 v200, s[80:83], s37 offen offset:0 nt lds\n"
    "v_mfma_f32_16x16x32_bf16 v[128:131], a[76:79], a[12:15], v[128:131]\n"
    "ds_read_b64_tr_b16 a[130:131], v192 offset:24576\n"
    "v_mfma_f32_16x16x32_bf16 v[128:131], a[80:83], a[16:19], v[128:131]\n"
    "ds_read_b64_tr_b16 a[132:133], v193 offset:16384\n"
    "v_mfma_f32_16x16x32_bf16 v[128:131], a[84:87], a[20:23], v[128:131]\n"
    "ds_read_b64_tr_b16 a[134:135], v193 offset:24576\n"
    "v_mfma_f32_16x16x32_bf16 v[128:131], a[88:91], a[24:27], v[128:131]\n"
    "ds_read_b64_tr_b16 a[136:137], v194 offset:16384\n"
    "v_mfma_f32_16x16x32_bf16 v[128:131], a[92:95], a[28:31], v[128:131]\n"
    "ds_read_b64_tr_b16 a[138:139], v194 offset:24576\n"
    "v_mfma_f32_16x16x32_bf16 v[132:135], a[64:67], a[32:35], v[148:151]\n"
    "ds_read_b64_tr_b16 a[140:141], v195 offset:16384\n"
    "v_mfma_f32_16x16x32_bf16 v[132:135], a[68:71], a[36:39], v[132:135]\n"
    "ds_read_b64_tr_b16 a[142:143], v195 offset:24576\n"
    "ds_read_b64_tr_b16 a[144:145], v196 offset:16384\n"
    "v_mfma_f32_16x16x32_bf16 v[132:135], a[72:75], a[40:43], v[132:135]\n"
    "buffer_load_dwordx4 v201, s[80:83], s37 offen offset:1024 nt lds\n"
    "v_mfma_f32_16x16x32_bf16 v[132:135], a[76:79], a[44:47], v[132:135]\n"
    "v_exp_f32 v168, v128\n"
    "v_mfma_f32_16x16x32_bf16 v[132:135], a[80:83], a[48:51], v[132:135]\n"
    "ds_read_b64_tr_b16 a[146:147], v196 offset:24576\n"
    "ds_read_b64_tr_b16 a[148:149], v197 offset:16384\n"
    "v_mfma_f32_16x16x32_bf16 v[132:135], a[84:87], a[52:55], v[132:135]\n"
    "ds_read_b64_tr_b16 a[150:151], v197 offset:24576\n"
    "ds_read_b64_tr_b16 a[152:153], v198 offset:16384\n"
    "v_mfma_f32_16x16x32_bf16 v[132:135], a[88:91], a[56:59], v[132:135]\n"
    "v_exp_f32 v169, v129\n"
    "v_mfma_f32_16x16x32_bf16 v[132:135], a[92:95], a[60:63], v[132:135]\n"
    "ds_read_b64_tr_b16 a[154:155], v198 offset:24576\n"
    "ds_read_b64_tr_b16 a[156:157], v199 offset:16384\n"
    "v_mfma_f32_16x16x32_bf16 v[136:139], a[96:99], a[0:3], v[144:147]\n"
    "v_exp_f32 v170, v130\n"
    "v_mfma_f32_16x16x32_bf16 v[136:139], a[100:103], a[4:7], v[136:139]\n"
    "ds_read_b64_tr_b16 a[158:159], v199 offset:24576\n"
    "ds_read_b64_tr_b16 a[160:161], v192 offset:16640\n"
    "v_mfma_f32_16x16x32_bf16 v[136:139], a[104:107], a[8:11], v[136:139]\n"
    "buffer_load_dwordx4 v202, s[80:83], s37 offen offset:2048 nt lds\n"
    "v_mfma_f32_16x16x32_bf16 v[136:139], a[108:111], a[12:15], v[136:139]\n"
    "v_exp_f32 v171, v131\n"
    "v_mfma_f32_16x16x32_bf16 v[136:139], a[112:115], a[16:19], v[136:139]\n"
    "ds_read_b64_tr_b16 a[162:163], v192 offset:24832\n"
    "ds_read_b64_tr_b16 a[164:165], v193 offset:16640\n"
    "v_mfma_f32_16x16x32_bf16 v[136:139], a[116:119], a[20:23], v[136:139]\n"
    "v_exp_f32 v172, v132\n"
    "v_mfma_f32_16x16x32_bf16 v[136:139], a[120:123], a[24:27], v[136:139]\n"
    "ds_read_b128 a[64:67], v188 offset:49152\n"
    "v_cvt_pk_bf16_f32 v152, v168, v169\n"
    "v_mfma_f32_16x16x32_bf16 v[136:139], a[124:127], a[28:31], v[136:139]\n"
    "v_exp_f32 v173, v133\n"
    "v_mfma_f32_16x16x32_bf16 v[140:143], a[96:99], a[32:35], v[148:151]\n"
    "ds_read_b128 a[68:71], v189 offset:49152\n"
    "v_cvt_pk_bf16_f32 v153, v170, v171\n"
    "v_mfma_f32_16x16x32_bf16 v[140:143], a[100:103], a[36:39], v[140:143]\n"
    "v_exp_f32 v174, v134\n"
    "v_mfma_f32_16x16x32_bf16 v[140:143], a[104:107], a[40:43], v[140:143]\n"
    "buffer_load_dwordx4 v203, s[80:83], s37 offen offset:3072 nt lds\n"
    "v_mfma_f32_16x16x32_bf16 v[140:143], a[108:111], a[44:47], v[140:143]\n"
    "s_add_u32 s37, s37, 0x4000\n"
    "v_exp_f32 v175, v135\n"
    "v_mfma_f32_16x16x32_bf16 v[140:143], a[112:115], a[48:51], v[140:143]\n"
    "ds_read_b64_tr_b16 a[166:167], v193 offset:24832\n"
    "ds_read_b64_tr_b16 a[168:169], v194 offset:16640\n"
    "v_mfma_f32_16x16x32_bf16 v[140:143], a[116:119], a[52:55], v[140:143]\n"
    "ds_read_b128 a[72:75], v190 offset:49152\n"
    "v_cvt_pk_bf16_f32 v156, v172, v173\n"
    "v_mfma_f32_16x16x32_bf16 v[140:143], a[120:123], a[56:59], v[140:143]\n"
    "s_mov_b32 m0, s74\n"
    "v_exp_f32 v233, v136\n"
    "v_mfma_f32_16x16x32_bf16 v[140:143], a[124:127], a[60:63], v[140:143]\n"
    "ds_read_b128 a[76:79], v191 offset:49152\n"
    "v_cvt_pk_bf16_f32 v157, v174, v175\n"
    "v_mfma_f32_16x16x32_bf16 v[176:179], v[184:187], v[160:163], v[176:179]\n"
    "v_exp_f32 v234, v137\n"
    "v_mfma_f32_16x16x32_bf16 v[180:183], v[184:187], v[164:167], v[180:183]\n"
    "s_waitcnt lgkmcnt(15)\n"
    "ds_read_b64_tr_b16 a[170:171], v194 offset:24832\n"
    "ds_read_b64_tr_b16 a[172:173], v195 offset:16640\n"
    "v_mfma_f32_16x16x32_bf16 v[8:11], a[128:131], v[164:167], v[8:11]\n"
    "buffer_load_dwordx4 v204, s[84:87], s38 offen offset:0 nt lds\n"
    "v_mfma_f32_16x16x32_bf16 v[0:3], a[128:131], v[160:163], v[0:3]\n"
    "v_exp_f32 v235, v138\n"
    "v_mfma_f32_16x16x32_bf16 v[4:7], a[132:135], v[160:163], v[4:7]\n"
    "ds_read_b128 a[80:83], v188 offset:49408\n"
    "v_cvt_pk_bf16_f32 v154, v233, v234\n"
    "v_mfma_f32_16x16x32_bf16 v[12:15], a[132:135], v[164:167], v[12:15]\n"
    "ds_read_b128 a[84:87], v189 offset:49408\n"
    "v_or3_b32 v208, v152, v153, v156\n"
    "v_mfma_f32_16x16x32_bf16 v[24:27], a[136:139], v[164:167], v[24:27]\n"
    "v_exp_f32 v236, v139\n"
    "v_mfma_f32_16x16x32_bf16 v[16:19], a[136:139], v[160:163], v[16:19]\n"
    "ds_read_b64_tr_b16 a[174:175], v195 offset:24832\n"
    "ds_read_b64_tr_b16 a[176:177], v196 offset:16640\n"
    "v_mfma_f32_16x16x32_bf16 v[20:23], a[140:143], v[160:163], v[20:23]\n"
    "v_exp_f32 v237, v140\n"
    "v_mfma_f32_16x16x32_bf16 v[28:31], a[140:143], v[164:167], v[28:31]\n"
    "s_waitcnt lgkmcnt(15)\n"
    "ds_read_b128 a[88:91], v190 offset:49408\n"
    "v_cvt_pk_bf16_f32 v155, v235, v236\n"
    "v_mfma_f32_16x16x32_bf16 v[40:43], a[144:147], v[164:167], v[40:43]\n"
    "buffer_load_dwordx4 v205, s[84:87], s38 offen offset:1024 nt lds\n"
    "v_mfma_f32_16x16x32_bf16 v[32:35], a[144:147], v[160:163], v[32:35]\n"
    "v_exp_f32 v238, v141\n"
    "v_mfma_f32_16x16x32_bf16 v[36:39], a[148:151], v[160:163], v[36:39]\n"
    "ds_read_b128 a[92:95], v191 offset:49408\n"
    "v_cvt_pk_bf16_f32 v158, v237, v238\n"
    "v_mfma_f32_16x16x32_bf16 v[44:47], a[148:151], v[164:167], v[44:47]\n"
    "ds_read_b64_tr_b16 a[178:179], v196 offset:24832\n"
    "ds_read_b64_tr_b16 a[180:181], v197 offset:16640\n"
    "v_mfma_f32_16x16x32_bf16 v[56:59], a[152:155], v[164:167], v[56:59]\n"
    "v_exp_f32 v239, v142\n"
    "v_mfma_f32_16x16x32_bf16 v[48:51], a[152:155], v[160:163], v[48:51]\n"
    "ds_read_b128 a[96:99], v188 offset:57344\n"
    "v_or3_b32 v208, v208, v157, v154\n"
    "v_mfma_f32_16x16x32_bf16 v[52:55], a[156:159], v[160:163], v[52:55]\n"
    "v_exp_f32 v240, v143\n"
    "v_mfma_f32_16x16x32_bf16 v[60:63], a[156:159], v[164:167], v[60:63]\n"
    "s_waitcnt lgkmcnt(6)\n"
    "ds_read_b128 a[100:103], v189 offset:57344\n"
    "v_cvt_pk_bf16_f32 v159, v239, v240\n"
    "v_mfma_f32_16x16x32_bf16 v[72:75], a[160:163], v[164:167], v[72:75]\n"
    "buffer_load_dwordx4 v206, s[84:87], s38 offen offset:2048 nt lds\n"
    "v_mfma_f32_16x16x32_bf16 v[64:67], a[160:163], v[160:163], v[64:67]\n"
    "ds_read_b64_tr_b16 a[182:183], v197 offset:24832\n"
    "ds_read_b64_tr_b16 a[184:185], v198 offset:16640\n"
    "v_mfma_f32_16x16x32_bf16 v[68:71], a[164:167], v[160:163], v[68:71]\n"
    "ds_read_b128 a[104:107], v190 offset:57344\n"
    "v_or3_b32 v208, v208, v155, v158\n"
    "v_mfma_f32_16x16x32_bf16 v[76:79], a[164:167], v[164:167], v[76:79]\n"
    "ds_read_b128 a[108:111], v191 offset:57344\n"
    "v_bitop3_b32 v208, v208, v159, s40 bitop3:0xa8\n"
    "v_mfma_f32_16x16x32_bf16 v[88:91], a[168:171], v[164:167], v[88:91]\n"
    "ds_read_b64_tr_b16 a[186:187], v198 offset:24832\n"
    "ds_read_b64_tr_b16 a[188:189], v199 offset:16640\n"
    "v_mfma_f32_16x16x32_bf16 v[80:83], a[168:171], v[160:163], v[80:83]\n"
    "ds_read_b128 a[112:115], v188 offset:57600\n"
    "v_cmp_ne_u32 vcc, 0, v208\n"
    "v_mfma_f32_16x16x32_bf16 v[84:87], a[172:175], v[160:163], v[84:87]\n"
    "ds_read_b128 a[116:119], v189 offset:57600\n"
    "v_mfma_f32_16x16x32_bf16 v[92:95], a[172:175], v[164:167], v[92:95]\n"
    "s_waitcnt lgkmcnt(2)\n"
    "ds_read_b64_tr_b16 a[190:191], v199 offset:24832\n"
    "v_mfma_f32_16x16x32_bf16 v[104:107], a[176:179], v[164:167], v[104:107]\n"
    "buffer_load_dwordx4 v207, s[84:87], s38 offen offset:3072 nt lds\n"
    "v_mfma_f32_16x16x32_bf16 v[96:99], a[176:179], v[160:163], v[96:99]\n"
    "ds_read_b128 a[120:123], v190 offset:57600\n"
    "s_add_u32 s38, s38, 0x4000\n"
    "v_mfma_f32_16x16x32_bf16 v[100:103], a[180:183], v[160:163], v[100:103]\n"
    "ds_read_b128 a[124:127], v191 offset:57600\n"
    "v_mfma_f32_16x16x32_bf16 v[108:111], a[180:183], v[164:167], v[108:111]\n"
    "v_mfma_f32_16x16x32_bf16 v[120:123], a[184:187], v[164:167], v[120:123]\n"
    "v_mfma_f32_16x16x32_bf16 v[112:115], a[184:187], v[160:163], v[112:115]\n"
    "s_waitcnt lgkmcnt(2)\n"
    "v_mfma_f32_16x16x32_bf16 v[116:119], a[188:191], v[160:163], v[116:119]\n"
    "v_mfma_f32_16x16x32_bf16 v[124:127], a[188:191], v[164:167], v[124:127]\n"
    "s_cbranch_vccnz LRL2%=\n"
    "LBL2%=:\n"
    "s_waitcnt lgkmcnt(0)\n"
    "v_mfma_f32_16x16x32_bf16 v[128:131], a[64:67], a[0:3], v[144:147]\n"
    "s_waitcnt vmcnt(16)\n"
    "s_barrier\n"
    "v_mfma_f32_16x16x32_bf16 v[128:131], a[68:71], a[4:7], v[128:131]\n"
    "ds_read_b64_tr_b16 a[128:129], v192 offset:32768\n"
    "s_mov_b32 m0, s95\n"
    "v_mfma_f32_16x16x32_bf16 v[128:131], a[72:75], a[8:11], v[128:131]\n"
    "buffer_load_dwordx4 v200, s[80:83], s37 offen offset:0 nt lds\n"
    "v_mfma_f32_16x16x32_bf16 v[128:131], a[76:79], a[12:15], v[128:131]\n"
    "ds_read_b64_tr_b16 a[130:131], v192 offset:40960\n"
    "v_mfma_f32_16x16x32_bf16 v[128:131], a[80:83], a[16:19], v[128:131]\n"
    "ds_read_b64_tr_b16 a[132:133], v193 offset:32768\n"
    "v_mfma_f32_16x16x32_bf16 v[128:131], a[84:87], a[20:23], v[128:131]\n"
    "ds_read_b64_tr_b16 a[134:135], v193 offset:40960\n"
    "v_mfma_f32_16x16x32_bf16 v[128:131], a[88:91], a[24:27], v[128:131]\n"
    "ds_read_b64_tr_b16 a[136:137], v194 offset:32768\n"
    "v_mfma_f32_16x16x32_bf16 v[128:131], a[92:95], a[28:31], v[128:131]\n"
    "ds_read_b64_tr_b16 a[138:139], v194 offset:40960\n"
    "v_mfma_f32_16x16x32_bf16 v[132:135], a[64:67], a[32:35], v[148:151]\n"
    "ds_read_b64_tr_b16 a[140:141], v195 offset:32768\n"
    "v_mfma_f32_16x16x32_bf16 v[132:135], a[68:71], a[36:39], v[132:135]\n"
    "ds_read_b64_tr_b16 a[142:143], v195 offset:40960\n"
    "ds_read_b64_tr_b16 a[144:145], v196 offset:32768\n"
    "v_mfma_f32_16x16x32_bf16 v[132:135], a[72:75], a[40:43], v[132:135]\n"
    "buffer_load_dwordx4 v201, s[80:83], s37 offen offset:1024 nt lds\n"
    "v_mfma_f32_16x16x32_bf16 v[132:135], a[76:79], a[44:47], v[132:135]\n"
    "v_exp_f32 v168, v128\n"
    "v_mfma_f32_16x16x32_bf16 v[132:135], a[80:83], a[48:51], v[132:135]\n"
    "ds_read_b64_tr_b16 a[146:147], v196 offset:40960\n"
    "ds_read_b64_tr_b16 a[148:149], v197 offset:32768\n"
    "v_mfma_f32_16x16x32_bf16 v[132:135], a[84:87], a[52:55], v[132:135]\n"
    "ds_read_b64_tr_b16 a[150:151], v197 offset:40960\n"
    "ds_read_b64_tr_b16 a[152:153], v198 offset:32768\n"
    "v_mfma_f32_16x16x32_bf16 v[132:135], a[88:91], a[56:59], v[132:135]\n"
    "v_exp_f32 v169, v129\n"
    "v_mfma_f32_16x16x32_bf16 v[132:135], a[92:95], a[60:63], v[132:135]\n"
    "ds_read_b64_tr_b16 a[154:155], v198 offset:40960\n"
    "ds_read_b64_tr_b16 a[156:157], v199 offset:32768\n"
    "v_mfma_f32_16x16x32_bf16 v[136:139], a[96:99], a[0:3], v[144:147]\n"
    "v_exp_f32 v170, v130\n"
    "v_mfma_f32_16x16x32_bf16 v[136:139], a[100:103], a[4:7], v[136:139]\n"
    "ds_read_b64_tr_b16 a[158:159], v199 offset:40960\n"
    "ds_read_b64_tr_b16 a[160:161], v192 offset:33024\n"
    "v_mfma_f32_16x16x32_bf16 v[136:139], a[104:107], a[8:11], v[136:139]\n"
    "buffer_load_dwordx4 v202, s[80:83], s37 offen offset:2048 nt lds\n"
    "v_mfma_f32_16x16x32_bf16 v[136:139], a[108:111], a[12:15], v[136:139]\n"
    "v_exp_f32 v171, v131\n"
    "v_mfma_f32_16x16x32_bf16 v[136:139], a[112:115], a[16:19], v[136:139]\n"
    "ds_read_b64_tr_b16 a[162:163], v192 offset:41216\n"
    "ds_read_b64_tr_b16 a[164:165], v193 offset:33024\n"
    "v_mfma_f32_16x16x32_bf16 v[136:139], a[116:119], a[20:23], v[136:139]\n"
    "v_exp_f32 v172, v132\n"
    "v_mfma_f32_16x16x32_bf16 v[136:139], a[120:123], a[24:27], v[136:139]\n"
    "ds_read_b128 a[64:67], v188 offset:0\n"
    "v_cvt_pk_bf16_f32 v160, v168, v169\n"
    "v_mfma_f32_16x16x32_bf16 v[136:139], a[124:127], a[28:31], v[136:139]\n"
    "v_exp_f32 v173, v133\n"
    "v_mfma_f32_16x16x32_bf16 v[140:143], a[96:99], a[32:35], v[148:151]\n"
    "ds_read_b128 a[68:71], v189 offset:0\n"
    "v_cvt_pk_bf16_f32 v161, v170, v171\n"
    "v_mfma_f32_16x16x32_bf16 v[140:143], a[100:103], a[36:39], v[140:143]\n"
    "v_exp_f32 v174, v134\n"
    "v_mfma_f32_16x16x32_bf16 v[140:143], a[104:107], a[40:43], v[140:143]\n"
    "buffer_load_dwordx4 v203, s[80:83], s37 offen offset:3072 nt lds\n"
    "v_mfma_f32_16x16x32_bf16 v[140:143], a[108:111], a[44:47], v[140:143]\n"
    "s_add_u32 s37, s37, 0x4000\n"
    "v_exp_f32 v175, v135\n"
    "v_mfma_f32_16x16x32_bf16 v[140:143], a[112:115], a[48:51], v[140:143]\n"
    "ds_read_b64_tr_b16 a[166:167], v193 offset:41216\n"
    "ds_read_b64_tr_b16 a[168:169], v194 offset:33024\n"
    "v_mfma_f32_16x16x32_bf16 v[140:143], a[116:119], a[52:55], v[140:143]\n"
    "ds_read_b128 a[72:75], v190 offset:0\n"
    "v_cvt_pk_bf16_f32 v164, v172, v173\n"
    "v_mfma_f32_16x16x32_bf16 v[140:143], a[120:123], a[56:59], v[140:143]\n"
    "s_mov_b32 m0, s75\n"
    "v_exp_f32 v233, v136\n"
    "v_mfma_f32_16x16x32_bf16 v[140:143], a[124:127], a[60:63], v[140:143]\n"
    "ds_read_b128 a[76:79], v191 offset:0\n"
    "v_cvt_pk_bf16_f32 v165, v174, v175\n"
    "v_mfma_f32_16x16x32_bf16 v[176:179], v[184:187], v[152:155], v[176:179]\n"
    "v_exp_f32 v234, v137\n"
    "v_mfma_f32_16x16x32_bf16 v[180:183], v[184:187], v[156:159], v[180:183]\n"
    "s_waitcnt lgkmcnt(15)\n"
    "ds_read_b64_tr_b16 a[170:171], v194 offset:41216\n"
    "ds_read_b64_tr_b16 a[172:173], v195 offset:33024\n"
    "v_mfma_f32_16x16x32_bf16 v[8:11], a[128:131], v[156:159], v[8:11]\n"
    "buffer_load_dwordx4 v204, s[84:87], s38 offen offset:0 nt lds\n"
    "v_mfma_f32_16x16x32_bf16 v[0:3], a[128:131], v[152:155], v[0:3]\n"
    "v_exp_f32 v235, v138\n"
    "v_mfma_f32_16x16x32_bf16 v[4:7], a[132:135], v[152:155], v[4:7]\n"
    "ds_read_b128 a[80:83], v188 offset:256\n"
    "v_cvt_pk_bf16_f32 v162, v233, v234\n"
    "v_mfma_f32_16x16x32_bf16 v[12:15], a[132:135], v[156:159], v[12:15]\n"
    "ds_read_b128 a[84:87], v189 offset:256\n"
    "v_or3_b32 v208, v160, v161, v164\n"
    "v_mfma_f32_16x16x32_bf16 v[24:27], a[136:139], v[156:159], v[24:27]\n"
    "v_exp_f32 v236, v139\n"
    "v_mfma_f32_16x16x32_bf16 v[16:19], a[136:139], v[152:155], v[16:19]\n"
    "ds_read_b64_tr_b16 a[174:175], v195 offset:41216\n"
    "ds_read_b64_tr_b16 a[176:177], v196 offset:33024\n"
    "v_mfma_f32_16x16x32_bf16 v[20:23], a[140:143], v[152:155], v[20:23]\n"
    "v_exp_f32 v237, v140\n"
    "v_mfma_f32_16x16x32_bf16 v[28:31], a[140:143], v[156:159], v[28:31]\n"
    "s_waitcnt lgkmcnt(15)\n"
    "ds_read_b128 a[88:91], v190 offset:256\n"
    "v_cvt_pk_bf16_f32 v163, v235, v236\n"
    "v_mfma_f32_16x16x32_bf16 v[40:43], a[144:147], v[156:159], v[40:43]\n"
    "buffer_load_dwordx4 v205, s[84:87], s38 offen offset:1024 nt lds\n"
    "v_mfma_f32_16x16x32_bf16 v[32:35], a[144:147], v[152:155], v[32:35]\n"
    "v_exp_f32 v238, v141\n"
    "v_mfma_f32_16x16x32_bf16 v[36:39], a[148:151], v[152:155], v[36:39]\n"
    "ds_read_b128 a[92:95], v191 offset:256\n"
    "v_cvt_pk_bf16_f32 v166, v237, v238\n"
    "v_mfma_f32_16x16x32_bf16 v[44:47], a[148:151], v[156:159], v[44:47]\n"
    "ds_read_b64_tr_b16 a[178:179], v196 offset:41216\n"
    "ds_read_b64_tr_b16 a[180:181], v197 offset:33024\n"
    "v_mfma_f32_16x16x32_bf16 v[56:59], a[152:155], v[156:159], v[56:59]\n"
    "v_exp_f32 v239, v142\n"
    "v_mfma_f32_16x16x32_bf16 v[48:51], a[152:155], v[152:155], v[48:51]\n"
    "ds_read_b128 a[96:99], v188 offset:8192\n"
    "v_or3_b32 v208, v208, v165, v162\n"
    "v_mfma_f32_16x16x32_bf16 v[52:55], a[156:159], v[152:155], v[52:55]\n"
    "v_exp_f32 v240, v143\n"
    "v_mfma_f32_16x16x32_bf16 v[60:63], a[156:159], v[156:159], v[60:63]\n"
    "s_waitcnt lgkmcnt(6)\n"
    "ds_read_b128 a[100:103], v189 offset:8192\n"
    "v_cvt_pk_bf16_f32 v167, v239, v240\n"
    "v_mfma_f32_16x16x32_bf16 v[72:75], a[160:163], v[156:159], v[72:75]\n"
    "buffer_load_dwordx4 v206, s[84:87], s38 offen offset:2048 nt lds\n"
    "v_mfma_f32_16x16x32_bf16 v[64:67], a[160:163], v[152:155], v[64:67]\n"
    "ds_read_b64_tr_b16 a[182:183], v197 offset:41216\n"
    "ds_read_b64_tr_b16 a[184:185], v198 offset:33024\n"
    "v_mfma_f32_16x16x32_bf16 v[68:71], a[164:167], v[152:155], v[68:71]\n"
    "ds_read_b128 a[104:107], v190 offset:8192\n"
    "v_or3_b32 v208, v208, v163, v166\n"
    "v_mfma_f32_16x16x32_bf16 v[76:79], a[164:167], v[156:159], v[76:79]\n"
    "ds_read_b128 a[108:111], v191 offset:8192\n"
    "v_bitop3_b32 v208, v208, v167, s40 bitop3:0xa8\n"
    "v_mfma_f32_16x16x32_bf16 v[88:91], a[168:171], v[156:159], v[88:91]\n"
    "ds_read_b64_tr_b16 a[186:187], v198 offset:41216\n"
    "ds_read_b64_tr_b16 a[188:189], v199 offset:33024\n"
    "v_mfma_f32_16x16x32_bf16 v[80:83], a[168:171], v[152:155], v[80:83]\n"
    "ds_read_b128 a[112:115], v188 offset:8448\n"
    "v_cmp_ne_u32 vcc, 0, v208\n"
    "v_mfma_f32_16x16x32_bf16 v[84:87], a[172:175], v[152:155], v[84:87]\n"
    "ds_read_b128 a[116:119], v189 offset:8448\n"
    "v_mfma_f32_16x16x32_bf16 v[92:95], a[172:175], v[156:159], v[92:95]\n"
    "s_waitcnt lgkmcnt(2)\n"
    "ds_read_b64_tr_b16 a[190:191], v199 offset:41216\n"
    "v_mfma_f32_16x16x32_bf16 v[104:107], a[176:179], v[156:159], v[104:107]\n"
    "buffer_load_dwordx4 v207, s[84:87], s38 offen offset:3072 nt lds\n"
    "v_mfma_f32_16x16x32_bf16 v[96:99], a[176:179], v[152:155], v[96:99]\n"
    "ds_read_b128 a[120:123], v190 offset:8448\n"
    "s_add_u32 s38, s38, 0x4000\n"
    "v_mfma_f32_16x16x32_bf16 v[100:103], a[180:183], v[152:155], v[100:103]\n"
    "ds_read_b128 a[124:127], v191 offset:8448\n"
    "s_sub_u32 s39, s39, 1\n"
    "v_mfma_f32_16x16x32_bf16 v[108:111], a[180:183], v[156:159], v[108:111]\n"
    "v_mfma_f32_16x16x32_bf16 v[120:123], a[184:187], v[156:159], v[120:123]\n"
    "v_mfma_f32_16x16x32_bf16 v[112:115], a[184:187], v[152:155], v[112:115]\n"
    "s_waitcnt lgkmcnt(2)\n"
    "v_mfma_f32_16x16x32_bf16 v[116:119], a[188:191], v[152:155], v[116:119]\n"
    "v_mfma_f32_16x16x32_bf16 v[124:127], a[188:191], v[156:159], v[124:127]\n"
    "s_cbranch_vccnz LRL3%=\n"
    "LBL3%=:\n"
    "s_cbranch_scc0 LLOOP%=\n"
    "s_waitcnt lgkmcnt(0)\n"
    "v_mfma_f32_16x16x32_bf16 v[128:131], a[64:67], a[0:3], v[144:147]\n"
    "s_waitcnt vmcnt(16)\n"
    "s_barrier\n"
    "v_mfma_f32_16x16x32_bf16 v[128:131], a[68:71], a[4:7], v[128:131]\n"
    "ds_read_b64_tr_b16 a[128:129], v192 offset:49152\n"
    "s_mov_b32 s41, 0x78000\n"
    "v_mfma_f32_16x16x32_bf16 v[128:131], a[72:75], a[8:11], v[128:131]\n"
    "ds_read_b64_tr_b16 a[130:131], v192 offset:57344\n"
    "s_add_u32 m0, s36, 0x18000\n"
    "v_mfma_f32_16x16x32_bf16 v[128:131], a[76:79], a[12:15], v[128:131]\n"
    "buffer_load_dwordx4 v204, s[84:87], s41 offen offset:0 nt lds\n"
    "v_mfma_f32_16x16x32_bf16 v[128:131], a[80:83], a[16:19], v[128:131]\n"
    "ds_read_b64_tr_b16 a[132:133], v193 offset:49152\n"
    "v_mfma_f32_16x16x32_bf16 v[128:131], a[84:87], a[20:23], v[128:131]\n"
    "ds_read_b64_tr_b16 a[134:135], v193 offset:57344\n"
    "v_mfma_f32_16x16x32_bf16 v[128:131], a[88:91], a[24:27], v[128:131]\n"
    "ds_read_b64_tr_b16 a[136:137], v194 offset:49152\n"
    "v_mfma_f32_16x16x32_bf16 v[128:131], a[92:95], a[28:31], v[128:131]\n"
    "ds_read_b64_tr_b16 a[138:139], v194 offset:57344\n"
    "v_mfma_f32_16x16x32_bf16 v[132:135], a[64:67], a[32:35], v[148:151]\n"
    "ds_read_b64_tr_b16 a[140:141], v195 offset:49152\n"
    "v_mfma_f32_16x16x32_bf16 v[132:135], a[68:71], a[36:39], v[132:135]\n"
    "ds_read_b64_tr_b16 a[142:143], v195 offset:57344\n"
    "ds_read_b64_tr_b16 a[144:145], v196 offset:49152\n"
    "v_mfma_f32_16x16x32_bf16 v[132:135], a[72:75], a[40:43], v[132:135]\n"
    "buffer_load_dwordx4 v205, s[84:87], s41 offen offset:1024 nt lds\n"
    "v_mfma_f32_16x16x32_bf16 v[132:135], a[76:79], a[44:47], v[132:135]\n"
    "v_exp_f32 v168, v128\n"
    "v_mfma_f32_16x16x32_bf16 v[132:135], a[80:83], a[48:51], v[132:135]\n"
    "ds_read_b64_tr_b16 a[146:147], v196 offset:57344\n"
    "ds_read_b64_tr_b16 a[148:149], v197 offset:49152\n"
    "v_mfma_f32_16x16x32_bf16 v[132:135], a[84:87], a[52:55], v[132:135]\n"
    "ds_read_b64_tr_b16 a[150:151], v197 offset:57344\n"
    "ds_read_b64_tr_b16 a[152:153], v198 offset:49152\n"
    "v_mfma_f32_16x16x32_bf16 v[132:135], a[88:91], a[56:59], v[132:135]\n"
    "v_exp_f32 v169, v129\n"
    "v_mfma_f32_16x16x32_bf16 v[132:135], a[92:95], a[60:63], v[132:135]\n"
    "ds_read_b64_tr_b16 a[154:155], v198 offset:57344\n"
    "ds_read_b64_tr_b16 a[156:157], v199 offset:49152\n"
    "v_mfma_f32_16x16x32_bf16 v[136:139], a[96:99], a[0:3], v[144:147]\n"
    "v_exp_f32 v170, v130\n"
    "v_mfma_f32_16x16x32_bf16 v[136:139], a[100:103], a[4:7], v[136:139]\n"
    "ds_read_b64_tr_b16 a[158:159], v199 offset:57344\n"
    "ds_read_b64_tr_b16 a[160:161], v192 offset:49408\n"
    "v_mfma_f32_16x16x32_bf16 v[136:139], a[104:107], a[8:11], v[136:139]\n"
    "buffer_load_dwordx4 v206, s[84:87], s41 offen offset:2048 nt lds\n"
    "v_mfma_f32_16x16x32_bf16 v[136:139], a[108:111], a[12:15], v[136:139]\n"
    "v_exp_f32 v171, v131\n"
    "v_mfma_f32_16x16x32_bf16 v[136:139], a[112:115], a[16:19], v[136:139]\n"
    "ds_read_b64_tr_b16 a[162:163], v192 offset:57600\n"
    "ds_read_b64_tr_b16 a[164:165], v193 offset:49408\n"
    "v_mfma_f32_16x16x32_bf16 v[136:139], a[116:119], a[20:23], v[136:139]\n"
    "v_exp_f32 v172, v132\n"
    "v_mfma_f32_16x16x32_bf16 v[136:139], a[120:123], a[24:27], v[136:139]\n"
    "ds_read_b128 a[64:67], v188 offset:16384\n"
    "v_cvt_pk_bf16_f32 v152, v168, v169\n"
    "v_mfma_f32_16x16x32_bf16 v[136:139], a[124:127], a[28:31], v[136:139]\n"
    "v_exp_f32 v173, v133\n"
    "v_mfma_f32_16x16x32_bf16 v[140:143], a[96:99], a[32:35], v[148:151]\n"
    "ds_read_b128 a[68:71], v189 offset:16384\n"
    "v_cvt_pk_bf16_f32 v153, v170, v171\n"
    "v_mfma_f32_16x16x32_bf16 v[140:143], a[100:103], a[36:39], v[140:143]\n"
    "v_exp_f32 v174, v134\n"
    "v_mfma_f32_16x16x32_bf16 v[140:143], a[104:107], a[40:43], v[140:143]\n"
    "buffer_load_dwordx4 v207, s[84:87], s41 offen offset:3072 nt lds\n"
    "v_mfma_f32_16x16x32_bf16 v[140:143], a[108:111], a[44:47], v[140:143]\n"
    "s_add_u32 s41, s65, 0x0\n"
    "v_exp_f32 v175, v135\n"
    "v_mfma_f32_16x16x32_bf16 v[140:143], a[112:115], a[48:51], v[140:143]\n"
    "ds_read_b64_tr_b16 a[166:167], v193 offset:57600\n"
    "ds_read_b64_tr_b16 a[168:169], v194 offset:49408\n"
    "v_mfma_f32_16x16x32_bf16 v[140:143], a[116:119], a[52:55], v[140:143]\n"
    "ds_read_b128 a[72:75], v190 offset:16384\n"
    "v_cvt_pk_bf16_f32 v156, v172, v173\n"
    "v_mfma_f32_16x16x32_bf16 v[140:143], a[120:123], a[56:59], v[140:143]\n"
    "s_add_u32 m0, s36, 0x20000\n"
    "v_exp_f32 v233, v136\n"
    "v_mfma_f32_16x16x32_bf16 v[140:143], a[124:127], a[60:63], v[140:143]\n"
    "ds_read_b128 a[76:79], v191 offset:16384\n"
    "v_cvt_pk_bf16_f32 v157, v174, v175\n"
    "v_mfma_f32_16x16x32_bf16 v[176:179], v[184:187], v[160:163], v[176:179]\n"
    "v_exp_f32 v234, v137\n"
    "v_mfma_f32_16x16x32_bf16 v[180:183], v[184:187], v[164:167], v[180:183]\n"
    "s_waitcnt lgkmcnt(15)\n"
    "ds_read_b64_tr_b16 a[170:171], v194 offset:57600\n"
    "ds_read_b64_tr_b16 a[172:173], v195 offset:49408\n"
    "v_mfma_f32_16x16x32_bf16 v[8:11], a[128:131], v[164:167], v[8:11]\n"
    "buffer_load_dwordx4 v241, s[12:15], s41 offen offset:0 lds\n"
    "v_mfma_f32_16x16x32_bf16 v[0:3], a[128:131], v[160:163], v[0:3]\n"
    "v_exp_f32 v235, v138\n"
    "v_mfma_f32_16x16x32_bf16 v[4:7], a[132:135], v[160:163], v[4:7]\n"
    "ds_read_b128 a[80:83], v188 offset:16640\n"
    "v_cvt_pk_bf16_f32 v154, v233, v234\n"
    "v_mfma_f32_16x16x32_bf16 v[12:15], a[132:135], v[164:167], v[12:15]\n"
    "ds_read_b128 a[84:87], v189 offset:16640\n"
    "v_or3_b32 v208, v152, v153, v156\n"
    "v_mfma_f32_16x16x32_bf16 v[24:27], a[136:139], v[164:167], v[24:27]\n"
    "v_exp_f32 v236, v139\n"
    "v_mfma_f32_16x16x32_bf16 v[16:19], a[136:139], v[160:163], v[16:19]\n"
    "ds_read_b64_tr_b16 a[174:175], v195 offset:57600\n"
    "ds_read_b64_tr_b16 a[176:177], v196 offset:49408\n"
    "v_mfma_f32_16x16x32_bf16 v[20:23], a[140:143], v[160:163], v[20:23]\n"
    "v_exp_f32 v237, v140\n"
    "v_mfma_f32_16x16x32_bf16 v[28:31], a[140:143], v[164:167], v[28:31]\n"
    "s_waitcnt lgkmcnt(15)\n"
    "ds_read_b128 a[88:91], v190 offset:16640\n"
    "v_cvt_pk_bf16_f32 v155, v235, v236\n"
    "v_mfma_f32_16x16x32_bf16 v[40:43], a[144:147], v[164:167], v[40:43]\n"
    "buffer_load_dwordx4 v242, s[12:15], s41 offen offset:1024 lds\n"
    "v_mfma_f32_16x16x32_bf16 v[32:35], a[144:147], v[160:163], v[32:35]\n"
    "v_exp_f32 v238, v141\n"
    "v_mfma_f32_16x16x32_bf16 v[36:39], a[148:151], v[160:163], v[36:39]\n"
    "ds_read_b128 a[92:95], v191 offset:16640\n"
    "v_cvt_pk_bf16_f32 v158, v237, v238\n"
    "v_mfma_f32_16x16x32_bf16 v[44:47], a[148:151], v[164:167], v[44:47]\n"
    "ds_read_b64_tr_b16 a[178:179], v196 offset:57600\n"
    "ds_read_b64_tr_b16 a[180:181], v197 offset:49408\n"
    "v_mfma_f32_16x16x32_bf16 v[56:59], a[152:155], v[164:167], v[56:59]\n"
    "v_exp_f32 v239, v142\n"
    "v_mfma_f32_16x16x32_bf16 v[48:51], a[152:155], v[160:163], v[48:51]\n"
    "ds_read_b128 a[96:99], v188 offset:24576\n"
    "v_or3_b32 v208, v208, v157, v154\n"
    "v_mfma_f32_16x16x32_bf16 v[52:55], a[156:159], v[160:163], v[52:55]\n"
    "v_exp_f32 v240, v143\n"
    "v_mfma_f32_16x16x32_bf16 v[60:63], a[156:159], v[164:167], v[60:63]\n"
    "s_waitcnt lgkmcnt(6)\n"
    "ds_read_b128 a[100:103], v189 offset:24576\n"
    "v_cvt_pk_bf16_f32 v159, v239, v240\n"
    "v_mfma_f32_16x16x32_bf16 v[72:75], a[160:163], v[164:167], v[72:75]\n"
    "buffer_load_dwordx4 v243, s[12:15], s41 offen offset:2048 lds\n"
    "v_mfma_f32_16x16x32_bf16 v[64:67], a[160:163], v[160:163], v[64:67]\n"
    "ds_read_b64_tr_b16 a[182:183], v197 offset:57600\n"
    "ds_read_b64_tr_b16 a[184:185], v198 offset:49408\n"
    "v_mfma_f32_16x16x32_bf16 v[68:71], a[164:167], v[160:163], v[68:71]\n"
    "ds_read_b128 a[104:107], v190 offset:24576\n"
    "v_or3_b32 v208, v208, v155, v158\n"
    "v_mfma_f32_16x16x32_bf16 v[76:79], a[164:167], v[164:167], v[76:79]\n"
    "ds_read_b128 a[108:111], v191 offset:24576\n"
    "v_bitop3_b32 v208, v208, v159, s40 bitop3:0xa8\n"
    "v_mfma_f32_16x16x32_bf16 v[88:91], a[168:171], v[164:167], v[88:91]\n"
    "ds_read_b64_tr_b16 a[186:187], v198 offset:57600\n"
    "ds_read_b64_tr_b16 a[188:189], v199 offset:49408\n"
    "v_mfma_f32_16x16x32_bf16 v[80:83], a[168:171], v[160:163], v[80:83]\n"
    "ds_read_b128 a[112:115], v188 offset:24832\n"
    "v_cmp_ne_u32 vcc, 0, v208\n"
    "v_mfma_f32_16x16x32_bf16 v[84:87], a[172:175], v[160:163], v[84:87]\n"
    "ds_read_b128 a[116:119], v189 offset:24832\n"
    "v_mfma_f32_16x16x32_bf16 v[92:95], a[172:175], v[164:167], v[92:95]\n"
    "s_waitcnt lgkmcnt(2)\n"
    "ds_read_b64_tr_b16 a[190:191], v199 offset:57600\n"
    "v_mfma_f32_16x16x32_bf16 v[104:107], a[176:179], v[164:167], v[104:107]\n"
    "buffer_load_dwordx4 v244, s[12:15], s41 offen offset:3072 lds\n"
    "v_mfma_f32_16x16x32_bf16 v[96:99], a[176:179], v[160:163], v[96:99]\n"
    "ds_read_b128 a[120:123], v190 offset:24832\n"
    "v_mfma_f32_16x16x32_bf16 v[100:103], a[180:183], v[160:163], v[100:103]\n"
    "ds_read_b128 a[124:127], v191 offset:24832\n"
    "v_mfma_f32_16x16x32_bf16 v[108:111], a[180:183], v[164:167], v[108:111]\n"
    "v_mfma_f32_16x16x32_bf16 v[120:123], a[184:187], v[164:167], v[120:123]\n"
    "v_mfma_f32_16x16x32_bf16 v[112:115], a[184:187], v[160:163], v[112:115]\n"
    "s_waitcnt lgkmcnt(2)\n"
    "v_mfma_f32_16x16x32_bf16 v[116:119], a[188:191], v[160:163], v[116:119]\n"
    "buffer_load_dword a248, v231, s[12:15], 0 offen offset:0\n"
    "v_mfma_f32_16x16x32_bf16 v[124:127], a[188:191], v[164:167], v[124:127]\n"
    "s_cbranch_vccnz LR28%=\n"
    "LB28%=:\n"
    "s_waitcnt lgkmcnt(0)\n"
    "v_mfma_f32_16x16x32_bf16 v[128:131], a[64:67], a[0:3], v[144:147]\n"
    "s_waitcnt vmcnt(17)\n"
    "s_barrier\n"
    "v_mfma_f32_16x16x32_bf16 v[128:131], a[68:71], a[4:7], v[128:131]\n"
    "ds_read_b64_tr_b16 a[128:129], v192 offset:0\n"
    "s_mov_b32 s41, 0x7c000\n"
    "v_mfma_f32_16x16x32_bf16 v[128:131], a[72:75], a[8:11], v[128:131]\n"
    "ds_read_b64_tr_b16 a[130:131], v192 offset:8192\n"
    "s_add_u32 m0, s36, 0x1c000\n"
    "v_mfma_f32_16x16x32_bf16 v[128:131], a[76:79], a[12:15], v[128:131]\n"
    "buffer_load_dwordx4 v204, s[84:87], s41 offen offset:0 nt lds\n"
    "v_mfma_f32_16x16x32_bf16 v[128:131], a[80:83], a[16:19], v[128:131]\n"
    "ds_read_b64_tr_b16 a[132:133], v193 offset:0\n"
    "v_mfma_f32_16x16x32_bf16 v[128:131], a[84:87], a[20:23], v[128:131]\n"
    "ds_read_b64_tr_b16 a[134:135], v193 offset:8192\n"
    "v_mfma_f32_16x16x32_bf16 v[128:131], a[88:91], a[24:27], v[128:131]\n"
    "ds_read_b64_tr_b16 a[136:137], v194 offset:0\n"
    "v_mfma_f32_16x16x32_bf16 v[128:131], a[92:95], a[28:31], v[128:131]\n"
    "ds_read_b64_tr_b16 a[138:139], v194 offset:8192\n"
    "v_mfma_f32_16x16x32_bf16 v[132:135], a[64:67], a[32:35], v[148:151]\n"
    "ds_read_b64_tr_b16 a[140:141], v195 offset:0\n"
    "v_mfma_f32_16x16x32_bf16 v[132:135], a[68:71], a[36:39], v[132:135]\n"
    "ds_read_b64_tr_b16 a[142:143], v195 offset:8192\n"
    "ds_read_b64_tr_b16 a[144:145], v196 offset:0\n"
    "v_mfma_f32_16x16x32_bf16 v[132:135], a[72:75], a[40:43], v[132:135]\n"
    "buffer_load_dwordx4 v205, s[84:87], s41 offen offset:1024 nt lds\n"
    "v_mfma_f32_16x16x32_bf16 v[132:135], a[76:79], a[44:47], v[132:135]\n"
    "v_exp_f32 v168, v128\n"
    "v_mfma_f32_16x16x32_bf16 v[132:135], a[80:83], a[48:51], v[132:135]\n"
    "ds_read_b64_tr_b16 a[146:147], v196 offset:8192\n"
    "ds_read_b64_tr_b16 a[148:149], v197 offset:0\n"
    "v_mfma_f32_16x16x32_bf16 v[132:135], a[84:87], a[52:55], v[132:135]\n"
    "ds_read_b64_tr_b16 a[150:151], v197 offset:8192\n"
    "ds_read_b64_tr_b16 a[152:153], v198 offset:0\n"
    "v_mfma_f32_16x16x32_bf16 v[132:135], a[88:91], a[56:59], v[132:135]\n"
    "v_exp_f32 v169, v129\n"
    "v_mfma_f32_16x16x32_bf16 v[132:135], a[92:95], a[60:63], v[132:135]\n"
    "ds_read_b64_tr_b16 a[154:155], v198 offset:8192\n"
    "ds_read_b64_tr_b16 a[156:157], v199 offset:0\n"
    "v_mfma_f32_16x16x32_bf16 v[136:139], a[96:99], a[0:3], v[144:147]\n"
    "v_exp_f32 v170, v130\n"
    "v_mfma_f32_16x16x32_bf16 v[136:139], a[100:103], a[4:7], v[136:139]\n"
    "ds_read_b64_tr_b16 a[158:159], v199 offset:8192\n"
    "ds_read_b64_tr_b16 a[160:161], v192 offset:256\n"
    "v_mfma_f32_16x16x32_bf16 v[136:139], a[104:107], a[8:11], v[136:139]\n"
    "buffer_load_dwordx4 v206, s[84:87], s41 offen offset:2048 nt lds\n"
    "v_mfma_f32_16x16x32_bf16 v[136:139], a[108:111], a[12:15], v[136:139]\n"
    "v_exp_f32 v171, v131\n"
    "v_mfma_f32_16x16x32_bf16 v[136:139], a[112:115], a[16:19], v[136:139]\n"
    "ds_read_b64_tr_b16 a[162:163], v192 offset:8448\n"
    "ds_read_b64_tr_b16 a[164:165], v193 offset:256\n"
    "v_mfma_f32_16x16x32_bf16 v[136:139], a[116:119], a[20:23], v[136:139]\n"
    "v_exp_f32 v172, v132\n"
    "v_mfma_f32_16x16x32_bf16 v[136:139], a[120:123], a[24:27], v[136:139]\n"
    "ds_read_b128 a[64:67], v188 offset:32768\n"
    "v_cvt_pk_bf16_f32 v160, v168, v169\n"
    "v_mfma_f32_16x16x32_bf16 v[136:139], a[124:127], a[28:31], v[136:139]\n"
    "v_exp_f32 v173, v133\n"
    "v_mfma_f32_16x16x32_bf16 v[140:143], a[96:99], a[32:35], v[148:151]\n"
    "ds_read_b128 a[68:71], v189 offset:32768\n"
    "v_cvt_pk_bf16_f32 v161, v170, v171\n"
    "v_mfma_f32_16x16x32_bf16 v[140:143], a[100:103], a[36:39], v[140:143]\n"
    "v_exp_f32 v174, v134\n"
    "v_mfma_f32_16x16x32_bf16 v[140:143], a[104:107], a[40:43], v[140:143]\n"
    "buffer_load_dwordx4 v207, s[84:87], s41 offen offset:3072 nt lds\n"
    "v_mfma_f32_16x16x32_bf16 v[140:143], a[108:111], a[44:47], v[140:143]\n"
    "s_add_u32 s41, s65, 0x1c000\n"
    "v_exp_f32 v175, v135\n"
    "v_mfma_f32_16x16x32_bf16 v[140:143], a[112:115], a[48:51], v[140:143]\n"
    "ds_read_b64_tr_b16 a[166:167], v193 offset:8448\n"
    "ds_read_b64_tr_b16 a[168:169], v194 offset:256\n"
    "v_mfma_f32_16x16x32_bf16 v[140:143], a[116:119], a[52:55], v[140:143]\n"
    "ds_read_b128 a[72:75], v190 offset:32768\n"
    "v_cvt_pk_bf16_f32 v164, v172, v173\n"
    "v_mfma_f32_16x16x32_bf16 v[140:143], a[120:123], a[56:59], v[140:143]\n"
    "s_add_u32 m0, s36, 0x24000\n"
    "v_exp_f32 v233, v136\n"
    "v_mfma_f32_16x16x32_bf16 v[140:143], a[124:127], a[60:63], v[140:143]\n"
    "ds_read_b128 a[76:79], v191 offset:32768\n"
    "v_cvt_pk_bf16_f32 v165, v174, v175\n"
    "v_mfma_f32_16x16x32_bf16 v[176:179], v[184:187], v[152:155], v[176:179]\n"
    "v_exp_f32 v234, v137\n"
    "v_mfma_f32_16x16x32_bf16 v[180:183], v[184:187], v[156:159], v[180:183]\n"
    "s_waitcnt lgkmcnt(15)\n"
    "ds_read_b64_tr_b16 a[170:171], v194 offset:8448\n"
    "ds_read_b64_tr_b16 a[172:173], v195 offset:256\n"
    "v_mfma_f32_16x16x32_bf16 v[8:11], a[128:131], v[156:159], v[8:11]\n"
    "buffer_load_dwordx4 v241, s[12:15], s41 offen offset:0 lds\n"
    "v_mfma_f32_16x16x32_bf16 v[0:3], a[128:131], v[152:155], v[0:3]\n"
    "v_exp_f32 v235, v138\n"
    "v_mfma_f32_16x16x32_bf16 v[4:7], a[132:135], v[152:155], v[4:7]\n"
    "ds_read_b128 a[80:83], v188 offset:33024\n"
    "v_cvt_pk_bf16_f32 v162, v233, v234\n"
    "v_mfma_f32_16x16x32_bf16 v[12:15], a[132:135], v[156:159], v[12:15]\n"
    "ds_read_b128 a[84:87], v189 offset:33024\n"
    "v_or3_b32 v208, v160, v161, v164\n"
    "v_mfma_f32_16x16x32_bf16 v[24:27], a[136:139], v[156:159], v[24:27]\n"
    "v_exp_f32 v236, v139\n"
    "v_mfma_f32_16x16x32_bf16 v[16:19], a[136:139], v[152:155], v[16:19]\n"
    "ds_read_b64_tr_b16 a[174:175], v195 offset:8448\n"
    "ds_read_b64_tr_b16 a[176:177], v196 offset:256\n"
    "v_mfma_f32_16x16x32_bf16 v[20:23], a[140:143], v[152:155], v[20:23]\n"
    "s_and_b32 s43, s68, 1\n"
    "v_exp_f32 v237, v140\n"
    "v_mfma_f32_16x16x32_bf16 v[28:31], a[140:143], v[156:159], v[28:31]\n"
    "s_waitcnt lgkmcnt(15)\n"
    "ds_read_b128 a[88:91], v190 offset:33024\n"
    "v_cvt_pk_bf16_f32 v163, v235, v236\n"
    "v_mfma_f32_16x16x32_bf16 v[40:43], a[144:147], v[156:159], v[40:43]\n"
    "buffer_load_dwordx4 v242, s[12:15], s41 offen offset:1024 lds\n"
    "v_mfma_f32_16x16x32_bf16 v[32:35], a[144:147], v[152:155], v[32:35]\n"
    "s_lshl_b32 s43, s43, 7\n"
    "v_exp_f32 v238, v141\n"
    "v_mfma_f32_16x16x32_bf16 v[36:39], a[148:151], v[152:155], v[36:39]\n"
    "ds_read_b128 a[92:95], v191 offset:33024\n"
    "v_cvt_pk_bf16_f32 v166, v237, v238\n"
    "v_mfma_f32_16x16x32_bf16 v[44:47], a[148:151], v[156:159], v[44:47]\n"
    "ds_read_b64_tr_b16 a[178:179], v196 offset:8448\n"
    "ds_read_b64_tr_b16 a[180:181], v197 offset:256\n"
    "v_mfma_f32_16x16x32_bf16 v[56:59], a[152:155], v[156:159], v[56:59]\n"
    "v_exp_f32 v239, v142\n"
    "v_mfma_f32_16x16x32_bf16 v[48:51], a[152:155], v[152:155], v[48:51]\n"
    "ds_read_b128 a[96:99], v188 offset:40960\n"
    "v_or3_b32 v208, v208, v165, v162\n"
    "v_mfma_f32_16x16x32_bf16 v[52:55], a[156:159], v[152:155], v[52:55]\n"
    "v_exp_f32 v240, v143\n"
    "v_mfma_f32_16x16x32_bf16 v[60:63], a[156:159], v[156:159], v[60:63]\n"
    "s_waitcnt lgkmcnt(6)\n"
    "ds_read_b128 a[100:103], v189 offset:40960\n"
    "v_cvt_pk_bf16_f32 v167, v239, v240\n"
    "v_mfma_f32_16x16x32_bf16 v[72:75], a[160:163], v[156:159], v[72:75]\n"
    "buffer_load_dwordx4 v243, s[12:15], s41 offen offset:2048 lds\n"
    "v_mfma_f32_16x16x32_bf16 v[64:67], a[160:163], v[152:155], v[64:67]\n"
    "ds_read_b64_tr_b16 a[182:183], v197 offset:8448\n"
    "ds_read_b64_tr_b16 a[184:185], v198 offset:256\n"
    "v_mfma_f32_16x16x32_bf16 v[68:71], a[164:167], v[152:155], v[68:71]\n"
    "ds_read_b128 a[104:107], v190 offset:40960\n"
    "v_or3_b32 v208, v208, v163, v166\n"
    "v_mfma_f32_16x16x32_bf16 v[76:79], a[164:167], v[156:159], v[76:79]\n"
    "ds_read_b128 a[108:111], v191 offset:40960\n"
    "v_bitop3_b32 v208, v208, v167, s40 bitop3:0xa8\n"
    "v_mfma_f32_16x16x32_bf16 v[88:91], a[168:171], v[156:159], v[88:91]\n"
    "ds_read_b64_tr_b16 a[186:187], v198 offset:8448\n"
    "ds_read_b64_tr_b16 a[188:189], v199 offset:256\n"
    "v_mfma_f32_16x16x32_bf16 v[80:83], a[168:171], v[152:155], v[80:83]\n"
    "ds_read_b128 a[112:115], v188 offset:41216\n"
    "v_cmp_ne_u32 vcc, 0, v208\n"
    "v_mfma_f32_16x16x32_bf16 v[84:87], a[172:175], v[152:155], v[84:87]\n"
    "ds_read_b128 a[116:119], v189 offset:41216\n"
    "v_lshlrev_b32 v254, 2, v247\n"
    "v_mfma_f32_16x16x32_bf16 v[92:95], a[172:175], v[156:159], v[92:95]\n"
    "s_waitcnt lgkmcnt(2)\n"
    "ds_read_b64_tr_b16 a[190:191], v199 offset:8448\n"
    "v_add_u32 v254, s43, v254\n"
    "v_mfma_f32_16x16x32_bf16 v[104:107], a[176:179], v[156:159], v[104:107]\n"
    "buffer_load_dwordx4 v244, s[12:15], s41 offen offset:3072 lds\n"
    "v_mfma_f32_16x16x32_bf16 v[96:99], a[176:179], v[152:155], v[96:99]\n"
    "ds_read_b128 a[120:123], v190 offset:41216\n"
    "v_add_u32 v200, 57344, v254\n"
    "v_mfma_f32_16x16x32_bf16 v[100:103], a[180:183], v[152:155], v[100:103]\n"
    "ds_read_b128 a[124:127], v191 offset:41216\n"
    "v_add_u32 v254, s43, v254\n"
    "v_mfma_f32_16x16x32_bf16 v[108:111], a[180:183], v[156:159], v[108:111]\n"
    "s_and_b32 s43, s57, 1\n"
    "s_lshl_b32 s43, s43, 6\n"
    "v_mfma_f32_16x16x32_bf16 v[120:123], a[184:187], v[156:159], v[120:123]\n"
    "s_add_u32 s43, s43, 0x8000\n"
    "v_add_u32 v255, s43, v254\n"
    "v_mfma_f32_16x16x32_bf16 v[112:115], a[184:187], v[152:155], v[112:115]\n"
    "s_waitcnt lgkmcnt(2)\n"
    "v_mfma_f32_16x16x32_bf16 v[116:119], a[188:191], v[152:155], v[116:119]\n"
    "v_mfma_f32_16x16x32_bf16 v[124:127], a[188:191], v[156:159], v[124:127]\n"
    "s_cbranch_vccnz LR29%=\n"
    "LB29%=:\n"
    "s_waitcnt lgkmcnt(0)\n"
    "v_mfma_f32_16x16x32_bf16 v[128:131], a[64:67], a[0:3], v[144:147]\n"
    "s_waitcnt vmcnt(17)\n"
    "s_barrier\n"
    "v_mfma_f32_16x16x32_bf16 v[128:131], a[68:71], a[4:7], v[128:131]\n"
    "ds_read_b64_tr_b16 a[128:129], v192 offset:16384\n"
    "s_add_u32 s41, s65, 0x80\n"
    "v_mfma_f32_16x16x32_bf16 v[128:131], a[72:75], a[8:11], v[128:131]\n"
    "ds_read_b64_tr_b16 a[130:131], v192 offset:24576\n"
    "s_add_u32 m0, s36, 0x0\n"
    "v_mfma_f32_16x16x32_bf16 v[128:131], a[76:79], a[12:15], v[128:131]\n"
    "buffer_load_dwordx4 v241, s[12:15], s41 offen offset:0 lds\n"
    "v_mfma_f32_16x16x32_bf16 v[128:131], a[80:83], a[16:19], v[128:131]\n"
    "ds_read_b64_tr_b16 a[132:133], v193 offset:16384\n"
    "v_mfma_f32_16x16x32_bf16 v[128:131], a[84:87], a[20:23], v[128:131]\n"
    "ds_read_b64_tr_b16 a[134:135], v193 offset:24576\n"
    "v_mfma_f32_16x16x32_bf16 v[128:131], a[88:91], a[24:27], v[128:131]\n"
    "ds_read_b64_tr_b16 a[136:137], v194 offset:16384\n"
    "v_mfma_f32_16x16x32_bf16 v[128:131], a[92:95], a[28:31], v[128:131]\n"
    "ds_read_b64_tr_b16 a[138:139], v194 offset:24576\n"
    "v_mfma_f32_16x16x32_bf16 v[132:135], a[64:67], a[32:35], v[148:151]\n"
    "ds_read_b64_tr_b16 a[140:141], v195 offset:16384\n"
    "s_add_u32 s88, s30, 0x1ffff\n"
    "s_addc_u32 s89, s31, 0\n"
    "s_and_b32 s88, s88, 0xfffe0000\n"
    "v_mfma_f32_16x16x32_bf16 v[132:135], a[68:71], a[36:39], v[132:135]\n"
    "ds_read_b64_tr_b16 a[142:143], v195 offset:24576\n"
    "ds_read_b64_tr_b16 a[144:145], v196 offset:16384\n"
    "v_mfma_f32_16x16x32_bf16 v[132:135], a[72:75], a[40:43], v[132:135]\n"
    "buffer_load_dwordx4 v242, s[12:15], s41 offen offset:1024 lds\n"
    "v_mfma_f32_16x16x32_bf16 v[132:135], a[76:79], a[44:47], v[132:135]\n"
    "s_lshl_b32 s43, s16, 4\n"
    "v_exp_f32 v168, v128\n"
    "v_mfma_f32_16x16x32_bf16 v[132:135], a[80:83], a[48:51], v[132:135]\n"
    "ds_read_b64_tr_b16 a[146:147], v196 offset:24576\n"
    "ds_read_b64_tr_b16 a[148:149], v197 offset:16384\n"
    "v_mfma_f32_16x16x32_bf16 v[132:135], a[84:87], a[52:55], v[132:135]\n"
    "ds_read_b64_tr_b16 a[150:151], v197 offset:24576\n"
    "ds_read_b64_tr_b16 a[152:153], v198 offset:16384\n"
    "v_mfma_f32_16x16x32_bf16 v[132:135], a[88:91], a[56:59], v[132:135]\n"
    "s_add_u32 s43, s43, s57\n"
    "v_exp_f32 v169, v129\n"
    "v_mfma_f32_16x16x32_bf16 v[132:135], a[92:95], a[60:63], v[132:135]\n"
    "ds_read_b64_tr_b16 a[154:155], v198 offset:24576\n"
    "ds_read_b64_tr_b16 a[156:157], v199 offset:16384\n"
    "v_mfma_f32_16x16x32_bf16 v[136:139], a[96:99], a[0:3], v[144:147]\n"
    "s_lshl_b32 s43, s43, 16\n"
    "v_exp_f32 v170, v130\n"
    "v_mfma_f32_16x16x32_bf16 v[136:139], a[100:103], a[4:7], v[136:139]\n"
    "ds_read_b64_tr_b16 a[158:159], v199 offset:24576\n"
    "ds_read_b64_tr_b16 a[160:161], v192 offset:16640\n"
    "v_mfma_f32_16x16x32_bf16 v[136:139], a[104:107], a[8:11], v[136:139]\n"
    "buffer_load_dwordx4 v243, s[12:15], s41 offen offset:2048 lds\n"
    "v_mfma_f32_16x16x32_bf16 v[136:139], a[108:111], a[12:15], v[136:139]\n"
    "s_add_u32 s43, s43, 0xf00000\n"
    "v_exp_f32 v171, v131\n"
    "v_mfma_f32_16x16x32_bf16 v[136:139], a[112:115], a[16:19], v[136:139]\n"
    "ds_read_b64_tr_b16 a[162:163], v192 offset:24832\n"
    "ds_read_b64_tr_b16 a[164:165], v193 offset:16640\n"
    "v_mfma_f32_16x16x32_bf16 v[136:139], a[116:119], a[20:23], v[136:139]\n"
    "s_add_u32 s88, s88, s43\n"
    "s_addc_u32 s89, s89, 0\n"
    "s_and_b32 s89, s89, 0xffff\n"
    "v_exp_f32 v172, v132\n"
    "v_mfma_f32_16x16x32_bf16 v[136:139], a[120:123], a[24:27], v[136:139]\n"
    "ds_read_b128 a[64:67], v188 offset:49152\n"
    "v_cvt_pk_bf16_f32 v152, v168, v169\n"
    "v_mfma_f32_16x16x32_bf16 v[136:139], a[124:127], a[28:31], v[136:139]\n"
    "s_mov_b32 s90, 0x8200\n"
    "v_exp_f32 v173, v133\n"
    "v_mfma_f32_16x16x32_bf16 v[140:143], a[96:99], a[32:35], v[148:151]\n"
    "ds_read_b128 a[68:71], v189 offset:49152\n"
    "v_cvt_pk_bf16_f32 v153, v170, v171\n"
    "v_mfma_f32_16x16x32_bf16 v[140:143], a[100:103], a[36:39], v[140:143]\n"
    "s_mov_b32 s91, 0x20000\n"
    "v_exp_f32 v174, v134\n"
    "v_mfma_f32_16x16x32_bf16 v[140:143], a[104:107], a[40:43], v[140:143]\n"
    "buffer_load_dwordx4 v244, s[12:15], s41 offen offset:3072 lds\n"
    "v_mfma_f32_16x16x32_bf16 v[140:143], a[108:111], a[44:47], v[140:143]\n"
    "s_add_u32 s41, s65, 0x1c080\n"
    "v_exp_f32 v175, v135\n"
    "v_mfma_f32_16x16x32_bf16 v[140:143], a[112:115], a[48:51], v[140:143]\n"
    "ds_read_b64_tr_b16 a[166:167], v193 offset:24832\n"
    "ds_read_b64_tr_b16 a[168:169], v194 offset:16640\n"
    "v_mfma_f32_16x16x32_bf16 v[140:143], a[116:119], a[52:55], v[140:143]\n"
    "ds_read_b128 a[72:75], v190 offset:49152\n"
    "v_cvt_pk_bf16_f32 v156, v172, v173\n"
    "v_mfma_f32_16x16x32_bf16 v[140:143], a[120:123], a[56:59], v[140:143]\n"
    "s_add_u32 m0, s36, 0x4000\n"
    "v_exp_f32 v233, v136\n"
    "v_mfma_f32_16x16x32_bf16 v[140:143], a[124:127], a[60:63], v[140:143]\n"
    "ds_read_b128 a[76:79], v191 offset:49152\n"
    "v_cvt_pk_bf16_f32 v157, v174, v175\n"
    "v_mfma_f32_16x16x32_bf16 v[176:179], v[184:187], v[160:163], v[176:179]\n"
    "s_xor_b32 s44, s88, 0x10000\n"
    "v_exp_f32 v234, v137\n"
    "v_mfma_f32_16x16x32_bf16 v[180:183], v[184:187], v[164:167], v[180:183]\n"
    "s_waitcnt lgkmcnt(15)\n"
    "ds_read_b64_tr_b16 a[170:171], v194 offset:24832\n"
    "ds_read_b64_tr_b16 a[172:173], v195 offset:16640\n"
    "v_mfma_f32_16x16x32_bf16 v[8:11], a[128:131], v[164:167], v[8:11]\n"
    "buffer_load_dwordx4 v241, s[12:15], s41 offen offset:0 lds\n"
    "v_mfma_f32_16x16x32_bf16 v[0:3], a[128:131], v[160:163], v[0:3]\n"
    "s_mov_b32 s45, s89\n"
    "v_exp_f32 v235, v138\n"
    "v_mfma_f32_16x16x32_bf16 v[4:7], a[132:135], v[160:163], v[4:7]\n"
    "ds_read_b128 a[80:83], v188 offset:49408\n"
    "v_cvt_pk_bf16_f32 v154, v233, v234\n"
    "v_mfma_f32_16x16x32_bf16 v[12:15], a[132:135], v[164:167], v[12:15]\n"
    "ds_read_b128 a[84:87], v189 offset:49408\n"
    "v_or3_b32 v208, v152, v153, v156\n"
    "v_mfma_f32_16x16x32_bf16 v[24:27], a[136:139], v[164:167], v[24:27]\n"
    "s_mov_b32 s46, 0x8200\n"
    "v_exp_f32 v236, v139\n"
    "v_mfma_f32_16x16x32_bf16 v[16:19], a[136:139], v[160:163], v[16:19]\n"
    "ds_read_b64_tr_b16 a[174:175], v195 offset:24832\n"
    "ds_read_b64_tr_b16 a[176:177], v196 offset:16640\n"
    "v_mfma_f32_16x16x32_bf16 v[20:23], a[140:143], v[160:163], v[20:23]\n"
    "s_and_b32 s60, s57, 1\n"
    "v_exp_f32 v237, v140\n"
    "v_mfma_f32_16x16x32_bf16 v[28:31], a[140:143], v[164:167], v[28:31]\n"
    "s_waitcnt lgkmcnt(15)\n"
    "ds_read_b128 a[88:91], v190 offset:49408\n"
    "v_cvt_pk_bf16_f32 v155, v235, v236\n"
    "v_mfma_f32_16x16x32_bf16 v[40:43], a[144:147], v[164:167], v[40:43]\n"
    "buffer_load_dwordx4 v242, s[12:15], s41 offen offset:1024 lds\n"
    "v_mfma_f32_16x16x32_bf16 v[32:35], a[144:147], v[160:163], v[32:35]\n"
    "s_lshl_b32 s43, s16, 1\n"
    "v_exp_f32 v238, v141\n"
    "v_mfma_f32_16x16x32_bf16 v[36:39], a[148:151], v[160:163], v[36:39]\n"
    "ds_read_b128 a[92:95], v191 offset:49408\n"
    "v_cvt_pk_bf16_f32 v158, v237, v238\n"
    "v_mfma_f32_16x16x32_bf16 v[44:47], a[148:151], v[164:167], v[44:47]\n"
    "ds_read_b64_tr_b16 a[178:179], v196 offset:24832\n"
    "ds_read_b64_tr_b16 a[180:181], v197 offset:16640\n"
    "v_mfma_f32_16x16x32_bf16 v[56:59], a[152:155], v[164:167], v[56:59]\n"
    "s_add_u32 s60, s60, s43\n"
    "v_exp_f32 v239, v142\n"
    "v_mfma_f32_16x16x32_bf16 v[48:51], a[152:155], v[160:163], v[48:51]\n"
    "ds_read_b128 a[96:99], v188 offset:57344\n"
    "v_or3_b32 v208, v208, v157, v154\n"
    "v_mfma_f32_16x16x32_bf16 v[52:55], a[156:159], v[160:163], v[52:55]\n"
    "s_mul_i32 s43, s60, 460800\n"
    "v_exp_f32 v240, v143\n"
    "v_mfma_f32_16x16x32_bf16 v[60:63], a[156:159], v[164:167], v[60:63]\n"
    "s_waitcnt lgkmcnt(6)\n"
    "ds_read_b128 a[100:103], v189 offset:57344\n"
    "v_cvt_pk_bf16_f32 v159, v239, v240\n"
    "v_mfma_f32_16x16x32_bf16 v[72:75], a[160:163], v[164:167], v[72:75]\n"
    "buffer_load_dwordx4 v243, s[12:15], s41 offen offset:2048 lds\n"
    "v_mfma_f32_16x16x32_bf16 v[64:67], a[160:163], v[160:163], v[64:67]\n"
    "ds_read_b64_tr_b16 a[182:183], v197 offset:24832\n"
    "ds_read_b64_tr_b16 a[184:185], v198 offset:16640\n"
    "v_mfma_f32_16x16x32_bf16 v[68:71], a[164:167], v[160:163], v[68:71]\n"
    "ds_read_b128 a[104:107], v190 offset:57344\n"
    "v_or3_b32 v208, v208, v155, v158\n"
    "v_mfma_f32_16x16x32_bf16 v[76:79], a[164:167], v[164:167], v[76:79]\n"
    "ds_read_b128 a[108:111], v191 offset:57344\n"
    "v_bitop3_b32 v208, v208, v159, s40 bitop3:0xa8\n"
    "v_mfma_f32_16x16x32_bf16 v[88:91], a[168:171], v[164:167], v[88:91]\n"
    "ds_read_b64_tr_b16 a[186:187], v198 offset:24832\n"
    "ds_read_b64_tr_b16 a[188:189], v199 offset:16640\n"
    "v_mfma_f32_16x16x32_bf16 v[80:83], a[168:171], v[160:163], v[80:83]\n"
    "ds_read_b128 a[112:115], v188 offset:57600\n"
    "v_cmp_ne_u32 vcc, 0, v208\n"
    "v_mfma_f32_16x16x32_bf16 v[84:87], a[172:175], v[160:163], v[84:87]\n"
    "ds_read_b128 a[116:119], v189 offset:57600\n"
    "s_add_u32 s84, s30, s43\n"
    "s_addc_u32 s85, s31, 0\n"
    "s_and_b32 s85, s85, 0xffff\n"
    "v_mfma_f32_16x16x32_bf16 v[92:95], a[172:175], v[164:167], v[92:95]\n"
    "s_waitcnt lgkmcnt(2)\n"
    "ds_read_b64_tr_b16 a[190:191], v199 offset:24832\n"
    "s_mov_b32 s86, 0x70800\n"
    "v_mfma_f32_16x16x32_bf16 v[104:107], a[176:179], v[164:167], v[104:107]\n"
    "buffer_load_dwordx4 v244, s[12:15], s41 offen offset:3072 lds\n"
    "v_mfma_f32_16x16x32_bf16 v[96:99], a[176:179], v[160:163], v[96:99]\n"
    "ds_read_b128 a[120:123], v190 offset:57600\n"
    "s_lshr_b32 s61, s57, 1\n"
    "v_mfma_f32_16x16x32_bf16 v[100:103], a[180:183], v[160:163], v[100:103]\n"
    "ds_read_b128 a[124:127], v191 offset:57600\n"
    "s_mul_i32 s43, s61, 57600\n"
    "v_mfma_f32_16x16x32_bf16 v[108:111], a[180:183], v[164:167], v[108:111]\n"
    "s_add_u32 s80, s84, s43\n"
    "s_addc_u32 s81, s85, 0\n"
    "s_and_b32 s81, s81, 0xffff\n"
    "s_mov_b32 s82, 57600\n"
    "v_mfma_f32_16x16x32_bf16 v[120:123], a[184:187], v[164:167], v[120:123]\n"
    "s_lshl_b32 s43, s16, 4\n"
    "s_and_b32 s60, s57, 12\n"
    "v_mfma_f32_16x16x32_bf16 v[112:115], a[184:187], v[160:163], v[112:115]\n"
    "s_add_u32 s43, s43, s60\n"
    "s_and_b32 s60, s57, 1\n"
    "s_waitcnt lgkmcnt(2)\n"
    "v_mfma_f32_16x16x32_bf16 v[116:119], a[188:191], v[160:163], v[116:119]\n"
    "buffer_load_dword v226, v219, s[52:55], 0 offen offset:0 sc1\n"
    "v_mfma_f32_16x16x32_bf16 v[124:127], a[188:191], v[164:167], v[124:127]\n"
    "s_lshl_b32 s60, s60, 1\n"
    "s_add_u32 s43, s43, s60\n"
    "s_and_b32 s61, s61, 1\n"
    "s_add_u32 s43, s43, s61\n"
    "s_mul_i32 s43, s43, 7168\n"
    "s_add_u32 s72, s34, s43\n"
    "s_addc_u32 s73, s35, 0\n"
    "s_and_b32 s73, s73, 0xffff\n"
    "s_mov_b32 s74, 7168\n"
    "s_mov_b32 s75, 0x20000\n"
    "s_mov_b32 s92, 0x1000\n"
    "s_mov_b32 s93, 0x2000\n"
    "s_mov_b32 s94, 0x3000\n"
    "s_cbranch_vccnz LR30%=\n"
    "LB30%=:\n"
    "s_waitcnt lgkmcnt(0)\n"
    "v_mfma_f32_16x16x32_bf16 v[128:131], a[64:67], a[0:3], v[144:147]\n"
    "s_waitcnt vmcnt(9)\n"
    "s_barrier\n"
    "v_mfma_f32_16x16x32_bf16 v[128:131], a[68:71], a[4:7], v[128:131]\n"
    "ds_read_b64_tr_b16 a[128:129], v192 offset:32768\n"
    "s_add_u32 s41, s65, 0x100\n"
    "v_mfma_f32_16x16x32_bf16 v[128:131], a[72:75], a[8:11], v[128:131]\n"
    "ds_read_b64_tr_b16 a[130:131], v192 offset:40960\n"
    "s_add_u32 m0, s36, 0x8000\n"
    "v_mfma_f32_16x16x32_bf16 v[128:131], a[76:79], a[12:15], v[128:131]\n"
    "buffer_load_dwordx4 v241, s[12:15], s41 offen offset:0 lds\n"
    "v_mfma_f32_16x16x32_bf16 v[128:131], a[80:83], a[16:19], v[128:131]\n"
    "ds_read_b64_tr_b16 a[132:133], v193 offset:32768\n"
    "ds_read_b64_tr_b16 a[134:135], v193 offset:40960\n"
    "v_mfma_f32_16x16x32_bf16 v[128:131], a[84:87], a[20:23], v[128:131]\n"
    "ds_read_b64_tr_b16 a[136:137], v194 offset:32768\n"
    "v_mfma_f32_16x16x32_bf16 v[128:131], a[88:91], a[24:27], v[128:131]\n"
    "ds_read_b64_tr_b16 a[140:141], v195 offset:32768\n"
    "v_mfma_f32_16x16x32_bf16 v[128:131], a[92:95], a[28:31], v[128:131]\n"
    "ds_read_b64_tr_b16 a[144:145], v196 offset:32768\n"
    "v_mfma_f32_16x16x32_bf16 v[132:135], a[64:67], a[32:35], v[148:151]\n"
    "ds_read_b64_tr_b16 a[148:149], v197 offset:32768\n"
    "v_mfma_f32_16x16x32_bf16 v[132:135], a[68:71], a[36:39], v[132:135]\n"
    "ds_read_b64_tr_b16 a[152:153], v198 offset:32768\n"
    "ds_read_b64_tr_b16 a[156:157], v199 offset:32768\n"
    "v_mfma_f32_16x16x32_bf16 v[132:135], a[72:75], a[40:43], v[132:135]\n"
    "buffer_load_dwordx4 v242, s[12:15], s41 offen offset:1024 lds\n"
    "v_mfma_f32_16x16x32_bf16 v[132:135], a[76:79], a[44:47], v[132:135]\n"
    "v_exp_f32 v168, v128\n"
    "v_mfma_f32_16x16x32_bf16 v[132:135], a[80:83], a[48:51], v[132:135]\n"
    "ds_read_b64_tr_b16 a[160:161], v192 offset:33024\n"
    "ds_read_b64_tr_b16 a[164:165], v193 offset:33024\n"
    "v_mfma_f32_16x16x32_bf16 v[132:135], a[84:87], a[52:55], v[132:135]\n"
    "ds_read_b64_tr_b16 a[168:169], v194 offset:33024\n"
    "ds_read_b64_tr_b16 a[172:173], v195 offset:33024\n"
    "v_mfma_f32_16x16x32_bf16 v[132:135], a[88:91], a[56:59], v[132:135]\n"
    "v_exp_f32 v169, v129\n"
    "v_mfma_f32_16x16x32_bf16 v[132:135], a[92:95], a[60:63], v[132:135]\n"
    "ds_read_b64_tr_b16 a[176:177], v196 offset:33024\n"
    "ds_read_b64_tr_b16 a[180:181], v197 offset:33024\n"
    "v_mfma_f32_16x16x32_bf16 v[136:139], a[96:99], a[0:3], v[144:147]\n"
    "v_exp_f32 v170, v130\n"
    "v_mfma_f32_16x16x32_bf16 v[136:139], a[100:103], a[4:7], v[136:139]\n"
    "ds_read_b64_tr_b16 a[184:185], v198 offset:33024\n"
    "ds_read_b64_tr_b16 a[188:189], v199 offset:33024\n"
    "v_mfma_f32_16x16x32_bf16 v[136:139], a[104:107], a[8:11], v[136:139]\n"
    "buffer_load_dwordx4 v243, s[12:15], s41 offen offset:2048 lds\n"
    "v_mfma_f32_16x16x32_bf16 v[136:139], a[108:111], a[12:15], v[136:139]\n"
    "v_exp_f32 v171, v131\n"
    "v_mfma_f32_16x16x32_bf16 v[136:139], a[112:115], a[16:19], v[136:139]\n"
    "ds_read_b64_tr_b16 a[138:139], v194 offset:40960\n"
    "ds_read_b64_tr_b16 a[142:143], v195 offset:40960\n"
    "v_mfma_f32_16x16x32_bf16 v[136:139], a[116:119], a[20:23], v[136:139]\n"
    "v_exp_f32 v172, v132\n"
    "v_mfma_f32_16x16x32_bf16 v[136:139], a[120:123], a[24:27], v[136:139]\n"
    "ds_read_b64_tr_b16 a[146:147], v196 offset:40960\n"
    "ds_read_b64_tr_b16 a[150:151], v197 offset:40960\n"
    "v_mfma_f32_16x16x32_bf16 v[136:139], a[124:127], a[28:31], v[136:139]\n"
    "v_exp_f32 v173, v133\n"
    "v_mfma_f32_16x16x32_bf16 v[140:143], a[96:99], a[32:35], v[148:151]\n"
    "ds_read_b64_tr_b16 a[154:155], v198 offset:40960\n"
    "ds_read_b64_tr_b16 a[158:159], v199 offset:40960\n"
    "v_mfma_f32_16x16x32_bf16 v[140:143], a[100:103], a[36:39], v[140:143]\n"
    "v_exp_f32 v174, v134\n"
    "v_mfma_f32_16x16x32_bf16 v[140:143], a[104:107], a[40:43], v[140:143]\n"
    "buffer_load_dwordx4 v244, s[12:15], s41 offen offset:3072 lds\n"
    "v_mfma_f32_16x16x32_bf16 v[140:143], a[108:111], a[44:47], v[140:143]\n"
    "s_add_u32 s41, s65, 0x1c100\n"
    "v_exp_f32 v175, v135\n"
    "v_mfma_f32_16x16x32_bf16 v[140:143], a[112:115], a[48:51], v[140:143]\n"
    "ds_read_b64_tr_b16 a[162:163], v192 offset:41216\n"
    "ds_read_b64_tr_b16 a[166:167], v193 offset:41216\n"
    "v_mfma_f32_16x16x32_bf16 v[140:143], a[116:119], a[52:55], v[140:143]\n"
    "ds_read_b64_tr_b16 a[170:171], v194 offset:41216\n"
    "ds_read_b64_tr_b16 a[174:175], v195 offset:41216\n"
    "v_mfma_f32_16x16x32_bf16 v[140:143], a[120:123], a[56:59], v[140:143]\n"
    "s_add_u32 m0, s36, 0xc000\n"
    "v_exp_f32 v233, v136\n"
    "v_mfma_f32_16x16x32_bf16 v[140:143], a[124:127], a[60:63], v[140:143]\n"
    "ds_read_b64_tr_b16 a[178:179], v196 offset:41216\n"
    "ds_read_b64_tr_b16 a[182:183], v197 offset:41216\n"
    "v_mfma_f32_16x16x32_bf16 v[176:179], v[184:187], v[152:155], v[176:179]\n"
    "v_exp_f32 v234, v137\n"
    "v_mfma_f32_16x16x32_bf16 v[180:183], v[184:187], v[156:159], v[180:183]\n"
    "s_waitcnt lgkmcnt(10)\n"
    "ds_read_b64_tr_b16 a[186:187], v198 offset:41216\n"
    "ds_read_b64_tr_b16 a[190:191], v199 offset:41216\n"
    "v_mfma_f32_16x16x32_bf16 v[8:11], a[128:131], v[156:159], v[8:11]\n"
    "buffer_load_dwordx4 v241, s[12:15], s41 offen offset:0 lds\n"
    "v_mfma_f32_16x16x32_bf16 v[0:3], a[128:131], v[152:155], v[0:3]\n"
    "v_exp_f32 v235, v138\n"
    "v_mfma_f32_16x16x32_bf16 v[4:7], a[132:135], v[152:155], v[4:7]\n"
    "ds_read_b64_tr_b16 a[128:129], v192 offset:49152\n"
    "ds_read_b64_tr_b16 a[130:131], v192 offset:57344\n"
    "v_mfma_f32_16x16x32_bf16 v[12:15], a[132:135], v[156:159], v[12:15]\n"
    "ds_read_b64_tr_b16 a[132:133], v193 offset:49152\n"
    "ds_read_b64_tr_b16 a[134:135], v193 offset:57344\n"
    "v_mfma_f32_16x16x32_bf16 v[24:27], a[136:139], v[156:159], v[24:27]\n"
    "v_exp_f32 v236, v139\n"
    "v_mfma_f32_16x16x32_bf16 v[16:19], a[136:139], v[152:155], v[16:19]\n"
    "ds_read_b64_tr_b16 a[136:137], v194 offset:49152\n"
    "ds_read_b64_tr_b16 a[138:139], v194 offset:57344\n"
    "v_mfma_f32_16x16x32_bf16 v[20:23], a[140:143], v[152:155], v[20:23]\n"
    "v_exp_f32 v237, v140\n"
    "v_mfma_f32_16x16x32_bf16 v[28:31], a[140:143], v[156:159], v[28:31]\n"
    "s_waitcnt lgkmcnt(14)\n"
    "ds_read_b64_tr_b16 a[140:141], v195 offset:49152\n"
    "ds_read_b64_tr_b16 a[142:143], v195 offset:57344\n"
    "v_mfma_f32_16x16x32_bf16 v[40:43], a[144:147], v[156:159], v[40:43]\n"
    "buffer_load_dwordx4 v242, s[12:15], s41 offen offset:1024 lds\n"
    "v_mfma_f32_16x16x32_bf16 v[32:35], a[144:147], v[152:155], v[32:35]\n"
    "v_exp_f32 v238, v141\n"
    "v_mfma_f32_16x16x32_bf16 v[36:39], a[148:151], v[152:155], v[36:39]\n"
    "ds_read_b64_tr_b16 a[144:145], v196 offset:49152\n"
    "ds_read_b64_tr_b16 a[146:147], v196 offset:57344\n"
    "v_mfma_f32_16x16x32_bf16 v[44:47], a[148:151], v[156:159], v[44:47]\n"
    "ds_read_b64_tr_b16 a[148:149], v197 offset:49152\n"
    "ds_read_b64_tr_b16 a[150:151], v197 offset:57344\n"
    "v_mfma_f32_16x16x32_bf16 v[56:59], a[152:155], v[156:159], v[56:59]\n"
    "v_exp_f32 v239, v142\n"
    "v_mfma_f32_16x16x32_bf16 v[48:51], a[152:155], v[152:155], v[48:51]\n"
    "ds_read_b64_tr_b16 a[152:153], v198 offset:49152\n"
    "ds_read_b64_tr_b16 a[154:155], v198 offset:57344\n"
    "v_mfma_f32_16x16x32_bf16 v[52:55], a[156:159], v[152:155], v[52:55]\n"
    "v_exp_f32 v240, v143\n"
    "v_mfma_f32_16x16x32_bf16 v[60:63], a[156:159], v[156:159], v[60:63]\n"
    "s_waitcnt lgkmcnt(15)\n"
    "ds_read_b64_tr_b16 a[156:157], v199 offset:49152\n"
    "ds_read_b64_tr_b16 a[158:159], v199 offset:57344\n"
    "v_mfma_f32_16x16x32_bf16 v[72:75], a[160:163], v[156:159], v[72:75]\n"
    "buffer_load_dwordx4 v243, s[12:15], s41 offen offset:2048 lds\n"
    "v_mfma_f32_16x16x32_bf16 v[64:67], a[160:163], v[152:155], v[64:67]\n"
    "ds_read_b64_tr_b16 a[160:161], v192 offset:49408\n"
    "ds_read_b64_tr_b16 a[162:163], v192 offset:57600\n"
    "v_mfma_f32_16x16x32_bf16 v[68:71], a[164:167], v[152:155], v[68:71]\n"
    "v_cvt_pk_bf16_f32 v160, v168, v169\n"
    "v_cvt_pk_bf16_f32 v161, v170, v171\n"
    "v_mfma_f32_16x16x32_bf16 v[76:79], a[164:167], v[156:159], v[76:79]\n"
    "ds_read_b64_tr_b16 a[164:165], v193 offset:49408\n"
    "v_cvt_pk_bf16_f32 v164, v172, v173\n"
    "v_mfma_f32_16x16x32_bf16 v[88:91], a[168:171], v[156:159], v[88:91]\n"
    "ds_read_b64_tr_b16 a[166:167], v193 offset:57600\n"
    "v_cvt_pk_bf16_f32 v165, v174, v175\n"
    "v_mfma_f32_16x16x32_bf16 v[80:83], a[168:171], v[152:155], v[80:83]\n"
    "ds_read_b64_tr_b16 a[168:169], v194 offset:49408\n"
    "v_cvt_pk_bf16_f32 v162, v233, v234\n"
    "v_mfma_f32_16x16x32_bf16 v[84:87], a[172:175], v[152:155], v[84:87]\n"
    "ds_read_b64_tr_b16 a[170:171], v194 offset:57600\n"
    "v_cvt_pk_bf16_f32 v163, v235, v236\n"
    "v_mfma_f32_16x16x32_bf16 v[92:95], a[172:175], v[156:159], v[92:95]\n"
    "s_waitcnt lgkmcnt(15)\n"
    "ds_read_b64_tr_b16 a[172:173], v195 offset:49408\n"
    "ds_read_b64_tr_b16 a[174:175], v195 offset:57600\n"
    "v_mfma_f32_16x16x32_bf16 v[104:107], a[176:179], v[156:159], v[104:107]\n"
    "buffer_load_dwordx4 v244, s[12:15], s41 offen offset:3072 lds\n"
    "v_mfma_f32_16x16x32_bf16 v[96:99], a[176:179], v[152:155], v[96:99]\n"
    "ds_read_b64_tr_b16 a[176:177], v196 offset:49408\n"
    "ds_read_b64_tr_b16 a[178:179], v196 offset:57600\n"
    "v_mfma_f32_16x16x32_bf16 v[100:103], a[180:183], v[152:155], v[100:103]\n"
    "v_cvt_pk_bf16_f32 v166, v237, v238\n"
    "v_cvt_pk_bf16_f32 v167, v239, v240\n"
    "v_mfma_f32_16x16x32_bf16 v[108:111], a[180:183], v[156:159], v[108:111]\n"
    "ds_read_b64_tr_b16 a[180:181], v197 offset:49408\n"
    "ds_read_b64_tr_b16 a[182:183], v197 offset:57600\n"
    "v_mfma_f32_16x16x32_bf16 v[120:123], a[184:187], v[156:159], v[120:123]\n"
    "v_or3_b32 v208, v160, v161, v164\n"
    "v_mfma_f32_16x16x32_bf16 v[112:115], a[184:187], v[152:155], v[112:115]\n"
    "ds_read_b64_tr_b16 a[184:185], v198 offset:49408\n"
    "ds_read_b64_tr_b16 a[186:187], v198 offset:57600\n"
    "v_mfma_f32_16x16x32_bf16 v[116:119], a[188:191], v[152:155], v[116:119]\n"
    "v_or3_b32 v208, v208, v165, v162\n"
    "v_mfma_f32_16x16x32_bf16 v[124:127], a[188:191], v[156:159], v[124:127]\n"
    "ds_read_b64_tr_b16 a[188:189], v199 offset:49408\n"
    "ds_read_b64_tr_b16 a[190:191], v199 offset:57600\n"
    "v_or3_b32 v208, v208, v163, v166\n"
    "v_bitop3_b32 v208, v208, v167, s40 bitop3:0xa8\n"
    "v_cmp_ne_u32 vcc, 0, v208\n"
    "s_nop 0\n"
    "s_cbranch_vccnz LR31%=\n"
    "LB31%=:\n"
    "s_cmp_eq_u32 s48, 1\n"
    "s_cbranch_scc1 LDF%=\n"
    "v_mfma_f32_16x16x32_bf16 v[176:179], v[184:187], v[160:163], v[176:179]\n"
    "s_add_u32 s41, s65, 0x180\n"
    "s_add_u32 m0, s36, 0x10000\n"
    "v_mfma_f32_16x16x32_bf16 v[180:183], v[184:187], v[164:167], v[180:183]\n"
    "s_waitcnt lgkmcnt(15)\n"
    "buffer_load_dwordx4 v241, s[12:15], s41 offen offset:0 lds\n"
    "v_mfma_f32_16x16x32_bf16 v[0:3], a[128:131], v[160:163], v[0:3]\n"
    "v_mfma_f32_16x16x32_bf16 v[8:11], a[128:131], v[164:167], v[8:11]\n"
    "ds_read_b64_tr_b16 a[128:129], v213 offset:0\n"
    "ds_read_b64_tr_b16 a[130:131], v213 offset:2048\n"
    "v_mfma_f32_16x16x32_bf16 v[4:7], a[132:135], v[160:163], v[4:7]\n"
    "buffer_load_dwordx4 v242, s[12:15], s41 offen offset:1024 lds\n"
    "v_mfma_f32_16x16x32_bf16 v[12:15], a[132:135], v[164:167], v[12:15]\n"
    "ds_read_b64_tr_b16 a[132:133], v213 offset:4096\n"
    "ds_read_b64_tr_b16 a[134:135], v213 offset:6144\n"
    "v_mfma_f32_16x16x32_bf16 v[16:19], a[136:139], v[160:163], v[16:19]\n"
    "v_mfma_f32_16x16x32_bf16 v[24:27], a[136:139], v[164:167], v[24:27]\n"
    "buffer_store_dword v176, v230, s[88:91], 0 offen offset:0 sc1\n"
    "v_mfma_f32_16x16x32_bf16 v[20:23], a[140:143], v[160:163], v[20:23]\n"
    "ds_read_b64_tr_b16 a[136:137], v213 offset:8192\n"
    "ds_read_b64_tr_b16 a[138:139], v213 offset:10240\n"
    "v_mfma_f32_16x16x32_bf16 v[28:31], a[140:143], v[164:167], v[28:31]\n"
    "ds_read_b64_tr_b16 a[140:141], v213 offset:12288\n"
    "ds_read_b64_tr_b16 a[142:143], v213 offset:14336\n"
    "v_mfma_f32_16x16x32_bf16 v[32:35], a[144:147], v[160:163], v[32:35]\n"
    "buffer_store_dword v180, v230, s[88:91], 0 offen offset:64 sc1\n"
    "v_mfma_f32_16x16x32_bf16 v[40:43], a[144:147], v[164:167], v[40:43]\n"
    "ds_read_b64_tr_b16 a[144:145], v213 offset:16384\n"
    "ds_read_b64_tr_b16 a[146:147], v213 offset:18432\n"
    "v_mfma_f32_16x16x32_bf16 v[36:39], a[148:151], v[160:163], v[36:39]\n"
    "v_cvt_pk_bf16_f32 v0, v0, v1\n"
    "v_cvt_pk_bf16_f32 v1, v2, v3\n"
    "v_mfma_f32_16x16x32_bf16 v[44:47], a[148:151], v[164:167], v[44:47]\n"
    "buffer_load_dwordx4 v243, s[12:15], s41 offen offset:2048 lds\n"
    "v_mfma_f32_16x16x32_bf16 v[48:51], a[152:155], v[160:163], v[48:51]\n"
    "ds_read_b64_tr_b16 a[148:149], v213 offset:20480\n"
    "ds_read_b64_tr_b16 a[150:151], v213 offset:22528\n"
    "v_mfma_f32_16x16x32_bf16 v[56:59], a[152:155], v[164:167], v[56:59]\n"
    "ds_read_b64_tr_b16 a[152:153], v213 offset:24576\n"
    "v_cvt_pk_bf16_f32 v2, v4, v5\n"
    "v_mfma_f32_16x16x32_bf16 v[52:55], a[156:159], v[160:163], v[52:55]\n"
    "buffer_load_dwordx4 v244, s[12:15], s41 offen offset:3072 lds\n"
    "v_mfma_f32_16x16x32_bf16 v[60:63], a[156:159], v[164:167], v[60:63]\n"
    "s_waitcnt lgkmcnt(15)\n"
    "ds_read_b64_tr_b16 a[154:155], v213 offset:26624\n"
    "s_add_u32 s41, s65, 0x1c180\n"
    "v_mfma_f32_16x16x32_bf16 v[64:67], a[160:163], v[160:163], v[64:67]\n"
    "ds_read_b64_tr_b16 a[156:157], v213 offset:28672\n"
    "s_add_u32 m0, s36, 0x14000\n"
    "v_mfma_f32_16x16x32_bf16 v[72:75], a[160:163], v[164:167], v[72:75]\n"
    "buffer_load_dwordx4 v241, s[12:15], s41 offen offset:0 lds\n"
    "v_mfma_f32_16x16x32_bf16 v[68:71], a[164:167], v[160:163], v[68:71]\n"
    "ds_read_b64_tr_b16 a[158:159], v213 offset:30720\n"
    "v_cvt_pk_bf16_f32 v3, v6, v7\n"
    "v_mfma_f32_16x16x32_bf16 v[76:79], a[164:167], v[164:167], v[76:79]\n"
    "v_cvt_pk_bf16_f32 v4, v8, v9\n"
    "v_cvt_pk_bf16_f32 v5, v10, v11\n"
    "v_mfma_f32_16x16x32_bf16 v[80:83], a[168:171], v[160:163], v[80:83]\n"
    "buffer_load_dwordx4 v242, s[12:15], s41 offen offset:1024 lds\n"
    "v_mfma_f32_16x16x32_bf16 v[88:91], a[168:171], v[164:167], v[88:91]\n"
    "v_cvt_pk_bf16_f32 v6, v12, v13\n"
    "v_cvt_pk_bf16_f32 v7, v14, v15\n"
    "v_mfma_f32_16x16x32_bf16 v[84:87], a[172:175], v[160:163], v[84:87]\n"
    "v_cvt_pk_bf16_f32 v12, v24, v25\n"
    "v_cvt_pk_bf16_f32 v13, v26, v27\n"
    "v_mfma_f32_16x16x32_bf16 v[92:95], a[172:175], v[164:167], v[92:95]\n"
    "s_waitcnt lgkmcnt(15)\n"
    "buffer_load_dwordx4 v243, s[12:15], s41 offen offset:2048 lds\n"
    "v_mfma_f32_16x16x32_bf16 v[96:99], a[176:179], v[160:163], v[96:99]\n"
    "s_barrier\n"
    "v_mfma_f32_16x16x32_bf16 v[104:107], a[176:179], v[164:167], v[104:107]\n"
    "ds_write_b128 v224, v[0:3] offset:0\n"
    "v_cvt_pk_bf16_f32 v14, v28, v29\n"
    "v_mfma_f32_16x16x32_bf16 v[100:103], a[180:183], v[160:163], v[100:103]\n"
    "buffer_load_dwordx4 v244, s[12:15], s41 offen offset:3072 lds\n"
    "v_mfma_f32_16x16x32_bf16 v[108:111], a[180:183], v[164:167], v[108:111]\n"
    "v_cvt_pk_bf16_f32 v15, v30, v31\n"
    "v_cvt_pk_bf16_f32 v8, v16, v17\n"
    "v_mfma_f32_16x16x32_bf16 v[112:115], a[184:187], v[160:163], v[112:115]\n"
    "v_cvt_pk_bf16_f32 v9, v18, v19\n"
    "v_cvt_pk_bf16_f32 v10, v20, v21\n"
    "v_mfma_f32_16x16x32_bf16 v[120:123], a[184:187], v[164:167], v[120:123]\n"
    "buffer_store_dwordx4 v[4:7], v222, s[88:91], 0 offen offset:0 sc1\n"
    "v_mfma_f32_16x16x32_bf16 v[116:119], a[188:191], v[160:163], v[116:119]\n"
    "v_cvt_pk_bf16_f32 v11, v22, v23\n"
    "v_cvt_pk_bf16_f32 v20, v40, v41\n"
    "v_mfma_f32_16x16x32_bf16 v[124:127], a[188:191], v[164:167], v[124:127]\n"
    "ds_write_b128 v224, v[8:11] offset:1024\n"
    "v_cvt_pk_bf16_f32 v21, v42, v43\n"
    "buffer_store_dwordx4 v[12:15], v222, s[88:91], 0 offen offset:1024 sc1\n"
    "v_cvt_pk_bf16_f32 v22, v44, v45\n"
    "v_cvt_pk_bf16_f32 v23, v46, v47\n"
    "v_cvt_pk_bf16_f32 v16, v32, v33\n"
    "buffer_store_dwordx4 v[20:23], v222, s[88:91], 0 offen offset:2048 sc1\n"
    "v_cvt_pk_bf16_f32 v17, v34, v35\n"
    "v_cvt_pk_bf16_f32 v18, v36, v37\n"
    "v_cvt_pk_bf16_f32 v19, v38, v39\n"
    "ds_write_b128 v224, v[16:19] offset:2048\n"
    "v_cvt_pk_bf16_f32 v28, v56, v57\n"
    "v_cvt_pk_bf16_f32 v29, v58, v59\n"
    "v_cvt_pk_bf16_f32 v30, v60, v61\n"
    "v_cvt_pk_bf16_f32 v31, v62, v63\n"
    "v_cvt_pk_bf16_f32 v24, v48, v49\n"
    "v_cvt_pk_bf16_f32 v25, v50, v51\n"
    "buffer_store_dwordx4 v[28:31], v222, s[88:91], 0 offen offset:3072 sc1\n"
    "v_cvt_pk_bf16_f32 v26, v52, v53\n"
    "v_cvt_pk_bf16_f32 v27, v54, v55\n"
    "v_cvt_pk_bf16_f32 v36, v72, v73\n"
    "ds_write_b128 v224, v[24:27] offset:3072\n"
    "v_cvt_pk_bf16_f32 v37, v74, v75\n"
    "v_cvt_pk_bf16_f32 v38, v76, v77\n"
    "v_cvt_pk_bf16_f32 v39, v78, v79\n"
    "buffer_store_dwordx4 v[36:39], v222, s[88:91], s92 offen offset:0 sc1\n"
    "v_cvt_pk_bf16_f32 v32, v64, v65\n"
    "v_cvt_pk_bf16_f32 v33, v66, v67\n"
    "v_cvt_pk_bf16_f32 v34, v68, v69\n"
    "v_cvt_pk_bf16_f32 v35, v70, v71\n"
    "v_cvt_pk_bf16_f32 v44, v88, v89\n"
    "v_cvt_pk_bf16_f32 v45, v90, v91\n"
    "ds_write_b128 v224, v[32:35] offset:4096\n"
    "v_cvt_pk_bf16_f32 v46, v92, v93\n"
    "v_cvt_pk_bf16_f32 v47, v94, v95\n"
    "v_cvt_pk_bf16_f32 v40, v80, v81\n"
    "buffer_store_dwordx4 v[44:47], v222, s[88:91], s92 offen offset:1024 sc1\n"
    "v_cvt_pk_bf16_f32 v41, v82, v83\n"
    "v_cvt_pk_bf16_f32 v42, v84, v85\n"
    "v_cvt_pk_bf16_f32 v43, v86, v87\n"
    "ds_write_b128 v224, v[40:43] offset:5120\n"
    "v_cvt_pk_bf16_f32 v52, v104, v105\n"
    "v_cvt_pk_bf16_f32 v53, v106, v107\n"
    "v_cvt_pk_bf16_f32 v54, v108, v109\n"
    "v_cvt_pk_bf16_f32 v55, v110, v111\n"
    "v_cvt_pk_bf16_f32 v48, v96, v97\n"
    "v_cvt_pk_bf16_f32 v49, v98, v99\n"
    "buffer_store_dwordx4 v[52:55], v222, s[88:91], s92 offen offset:2048 sc1\n"
    "v_cvt_pk_bf16_f32 v50, v100, v101\n"
    "v_cvt_pk_bf16_f32 v51, v102, v103\n"
    "v_cvt_pk_bf16_f32 v60, v120, v121\n"
    "ds_write_b128 v224, v[48:51] offset:6144\n"
    "v_cvt_pk_bf16_f32 v61, v122, v123\n"
    "v_cvt_pk_bf16_f32 v62, v124, v125\n"
    "v_cvt_pk_bf16_f32 v63, v126, v127\n"
    "buffer_store_dwordx4 v[60:63], v222, s[88:91], s92 offen offset:3072 sc1\n"
    "v_cvt_pk_bf16_f32 v56, v112, v113\n"
    "v_cvt_pk_bf16_f32 v57, v114, v115\n"
    "v_cvt_pk_bf16_f32 v58, v116, v117\n"
    "v_cvt_pk_bf16_f32 v59, v118, v119\n"
    "ds_write_b128 v224, v[56:59] offset:7168\n"
    "LDJ%=:\n"
    "s_waitcnt lgkmcnt(0)\n"
    "s_waitcnt lgkmcnt(0)\n"
    "s_barrier\n"
    "ds_read_b128 a[64:67], v225 offset:0\n"
    "ds_read_b128 a[72:75], v225 offset:1024\n"
    "ds_read_b128 a[80:83], v225 offset:2048\n"
    "ds_read_b128 a[88:91], v225 offset:3072\n"
    "ds_read_b128 a[96:99], v225 offset:4096\n"
    "ds_read_b128 a[104:107], v225 offset:5120\n"
    "ds_read_b128 a[112:115], v225 offset:6144\n"
    "ds_read_b128 a[120:123], v225 offset:7168\n"
    "ds_read_b128 a[68:71], v225 offset:8192\n"
    "ds_read_b128 a[76:79], v225 offset:9216\n"
    "ds_read_b128 a[84:87], v225 offset:10240\n"
    "ds_read_b128 a[92:95], v225 offset:11264\n"
    "ds_read_b128 a[100:103], v225 offset:12288\n"
    "ds_read_b128 a[108:111], v225 offset:13312\n"
    "ds_read_b128 a[116:119], v225 offset:14336\n"
    "ds_read_b128 a[124:127], v225 offset:15360\n"
    "s_waitcnt lgkmcnt(8)\n"
    "v_mfma_f32_16x16x32_bf16 v[64:67], a[128:131], a[64:67], 0\n"
    "ds_read_b64_tr_b16 a[160:161], v214 offset:0\n"
    "v_mfma_f32_16x16x32_bf16 v[64:67], a[132:135], a[72:75], v[64:67]\n"
    "ds_read_b64_tr_b16 a[162:163], v214 offset:2048\n"
    "v_mfma_f32_16x16x32_bf16 v[64:67], a[136:139], a[80:83], v[64:67]\n"
    "ds_read_b64_tr_b16 a[164:165], v214 offset:4096\n"
    "v_mfma_f32_16x16x32_bf16 v[64:67], a[140:143], a[88:91], v[64:67]\n"
    "ds_read_b64_tr_b16 a[166:167], v214 offset:6144\n"
    "v_mfma_f32_16x16x32_bf16 v[64:67], a[144:147], a[96:99], v[64:67]\n"
    "ds_read_b64_tr_b16 a[168:169], v214 offset:8192\n"
    "v_mfma_f32_16x16x32_bf16 v[64:67], a[148:151], a[104:107], v[64:67]\n"
    "ds_read_b64_tr_b16 a[170:171], v214 offset:10240\n"
    "v_mfma_f32_16x16x32_bf16 v[64:67], a[152:155], a[112:115], v[64:67]\n"
    "ds_read_b64_tr_b16 a[172:173], v214 offset:12288\n"
    "v_mfma_f32_16x16x32_bf16 v[64:67], a[156:159], a[120:123], v[64:67]\n"
    "s_waitcnt lgkmcnt(7)\n"
    "ds_read_b64_tr_b16 a[174:175], v214 offset:14336\n"
    "v_mfma_f32_16x16x32_bf16 v[72:75], a[128:131], a[68:71], 0\n"
    "ds_read_b64_tr_b16 a[176:177], v214 offset:16384\n"
    "v_mfma_f32_16x16x32_bf16 v[72:75], a[132:135], a[76:79], v[72:75]\n"
    "s_waitcnt vmcnt(27)\n"
    "s_barrier\n"
    "v_mfma_f32_16x16x32_bf16 v[72:75], a[136:139], a[84:87], v[72:75]\n"
    "ds_read_b64_tr_b16 a[178:179], v214 offset:18432\n"
    "s_add_u32 s41, s65, 0x200\n"
    "v_mfma_f32_16x16x32_bf16 v[72:75], a[140:143], a[92:95], v[72:75]\n"
    "ds_read_b64_tr_b16 a[180:181], v214 offset:20480\n"
    "s_add_u32 m0, s36, 0x18000\n"
    "v_mfma_f32_16x16x32_bf16 v[72:75], a[144:147], a[100:103], v[72:75]\n"
    "buffer_load_dwordx4 v241, s[12:15], s41 offen offset:0 lds\n"
    "v_mfma_f32_16x16x32_bf16 v[72:75], a[148:151], a[108:111], v[72:75]\n"
    "ds_read_b64_tr_b16 a[182:183], v214 offset:22528\n"
    "ds_read_b64_tr_b16 a[184:185], v214 offset:24576\n"
    "v_mfma_f32_16x16x32_bf16 v[72:75], a[152:155], a[116:119], v[72:75]\n"
    "ds_read_b64_tr_b16 a[186:187], v214 offset:26624\n"
    "ds_read_b64_tr_b16 a[188:189], v214 offset:28672\n"
    "v_mfma_f32_16x16x32_bf16 v[72:75], a[156:159], a[124:127], v[72:75]\n"
    "s_waitcnt lgkmcnt(7)\n"
    "buffer_load_dwordx4 v242, s[12:15], s41 offen offset:1024 lds\n"
    "v_mfma_f32_16x16x32_bf16 v[68:71], a[160:163], a[64:67], 0\n"
    "ds_read_b64_tr_b16 a[190:191], v214 offset:30720\n"
    "ds_read_b64_tr_b16 a[128:129], v209 offset:0\n"
    "v_mfma_f32_16x16x32_bf16 v[68:71], a[164:167], a[72:75], v[68:71]\n"
    "ds_read_b64_tr_b16 a[130:131], v209 offset:2048\n"
    "ds_read_b64_tr_b16 a[132:133], v209 offset:4096\n"
    "v_mfma_f32_16x16x32_bf16 v[68:71], a[168:171], a[80:83], v[68:71]\n"
    "buffer_load_dwordx4 v243, s[12:15], s41 offen offset:2048 lds\n"
    "v_mfma_f32_16x16x32_bf16 v[68:71], a[172:175], a[88:91], v[68:71]\n"
    "s_waitcnt lgkmcnt(3)\n"
    "ds_read_b64_tr_b16 a[134:135], v209 offset:6144\n"
    "ds_read_b64_tr_b16 a[136:137], v209 offset:8192\n"
    "v_mfma_f32_16x16x32_bf16 v[68:71], a[176:179], a[96:99], v[68:71]\n"
    "ds_read_b64_tr_b16 a[138:139], v209 offset:10240\n"
    "ds_read_b64_tr_b16 a[140:141], v209 offset:12288\n"
    "v_mfma_f32_16x16x32_bf16 v[68:71], a[180:183], a[104:107], v[68:71]\n"
    "buffer_load_dwordx4 v244, s[12:15], s41 offen offset:3072 lds\n"
    "v_mfma_f32_16x16x32_bf16 v[68:71], a[184:187], a[112:115], v[68:71]\n"
    "ds_read_b64_tr_b16 a[142:143], v209 offset:14336\n"
    "s_add_u32 s41, s65, 0x1c200\n"
    "v_mfma_f32_16x16x32_bf16 v[68:71], a[188:191], a[120:123], v[68:71]\n"
    "ds_read_b64_tr_b16 a[144:145], v209 offset:16384\n"
    "s_add_u32 m0, s36, 0x1c000\n"
    "v_mfma_f32_16x16x32_bf16 v[76:79], a[160:163], a[68:71], 0\n"
    "buffer_load_dwordx4 v241, s[12:15], s41 offen offset:0 lds\n"
    "v_mfma_f32_16x16x32_bf16 v[76:79], a[164:167], a[76:79], v[76:79]\n"
    "ds_read_b64_tr_b16 a[146:147], v209 offset:18432\n"
    "ds_read_b64_tr_b16 a[148:149], v209 offset:20480\n"
    "v_mfma_f32_16x16x32_bf16 v[76:79], a[168:171], a[84:87], v[76:79]\n"
    "ds_read_b64_tr_b16 a[150:151], v209 offset:22528\n"
    "ds_read_b64_tr_b16 a[152:153], v209 offset:24576\n"
    "v_mfma_f32_16x16x32_bf16 v[76:79], a[172:175], a[92:95], v[76:79]\n"
    "buffer_load_dwordx4 v242, s[12:15], s41 offen offset:1024 lds\n"
    "v_mfma_f32_16x16x32_bf16 v[76:79], a[176:179], a[100:103], v[76:79]\n"
    "ds_read_b64_tr_b16 a[154:155], v209 offset:26624\n"
    "ds_read_b64_tr_b16 a[156:157], v209 offset:28672\n"
    "v_mfma_f32_16x16x32_bf16 v[76:79], a[180:183], a[108:111], v[76:79]\n"
    "ds_read_b64_tr_b16 a[158:159], v209 offset:30720\n"
    "ds_read_b64_tr_b16 a[160:161], v210 offset:0\n"
    "v_mfma_f32_16x16x32_bf16 v[76:79], a[184:187], a[116:119], v[76:79]\n"
    "buffer_load_dwordx4 v243, s[12:15], s41 offen offset:2048 lds\n"
    "v_mfma_f32_16x16x32_bf16 v[76:79], a[188:191], a[124:127], v[76:79]\n"
    "s_waitcnt lgkmcnt(9)\n"
    "ds_read_b64_tr_b16 a[162:163], v210 offset:2048\n"
    "ds_read_b64_tr_b16 a[164:165], v210 offset:4096\n"
    "v_mfma_f32_16x16x32_bf16 v[80:83], a[128:131], a[64:67], 0\n"
    "ds_read_b64_tr_b16 a[166:167], v210 offset:6144\n"
    "ds_read_b64_tr_b16 a[168:169], v210 offset:8192\n"
    "v_mfma_f32_16x16x32_bf16 v[80:83], a[132:135], a[72:75], v[80:83]\n"
    "buffer_load_dwordx4 v244, s[12:15], s41 offen offset:3072 lds\n"
    "v_mfma_f32_16x16x32_bf16 v[80:83], a[136:139], a[80:83], v[80:83]\n"
    "ds_read_b64_tr_b16 a[170:171], v210 offset:10240\n"
    "ds_read_b64_tr_b16 a[172:173], v210 offset:12288\n"
    "v_mfma_f32_16x16x32_bf16 v[80:83], a[140:143], a[88:91], v[80:83]\n"
    "s_waitcnt lgkmcnt(7)\n"
    "ds_read_b64_tr_b16 a[174:175], v210 offset:14336\n"
    "s_lshr_b32 s62, s57, 1\n"
    "v_mfma_f32_16x16x32_bf16 v[80:83], a[144:147], a[96:99], v[80:83]\n"
    "ds_read_b64_tr_b16 a[176:177], v210 offset:16384\n"
    "s_lshl_b32 s43, s62, 5\n"
    "v_mfma_f32_16x16x32_bf16 v[80:83], a[148:151], a[104:107], v[80:83]\n"
    "ds_read_b64_tr_b16 a[178:179], v210 offset:18432\n"
    "s_add_u32 s43, s43, 57344\n"
    "v_mfma_f32_16x16x32_bf16 v[80:83], a[152:155], a[112:115], v[80:83]\n"
    "ds_read_b64_tr_b16 a[180:181], v210 offset:20480\n"
    "v_and_b32 v233, 7, v245\n"
    "v_mfma_f32_16x16x32_bf16 v[80:83], a[156:159], a[120:123], v[80:83]\n"
    "ds_read_b64_tr_b16 a[182:183], v210 offset:22528\n"
    "v_lshlrev_b32 v233, 2, v233\n"
    "v_mfma_f32_16x16x32_bf16 v[88:91], a[128:131], a[68:71], 0\n"
    "ds_read_b64_tr_b16 a[184:185], v210 offset:24576\n"
    "v_add_u32 v233, s43, v233\n"
    "v_mfma_f32_16x16x32_bf16 v[88:91], a[132:135], a[76:79], v[88:91]\n"
    "s_waitcnt vmcnt(26)\n"
    "s_barrier\n"
    "v_mfma_f32_16x16x32_bf16 v[88:91], a[136:139], a[84:87], v[88:91]\n"
    "ds_read_b64_tr_b16 a[186:187], v210 offset:26624\n"
    "s_add_u32 s41, s65, 0x280\n"
    "v_mfma_f32_16x16x32_bf16 v[88:91], a[140:143], a[92:95], v[88:91]\n"
    "ds_read_b64_tr_b16 a[188:189], v210 offset:28672\n"
    "s_add_u32 m0, s36, 0x20000\n"
    "v_mfma_f32_16x16x32_bf16 v[88:91], a[144:147], a[100:103], v[88:91]\n"
    "buffer_load_dwordx4 v241, s[12:15], s41 offen offset:0 lds\n"
    "v_mfma_f32_16x16x32_bf16 v[88:91], a[148:151], a[108:111], v[88:91]\n"
    "ds_read_b64_tr_b16 a[190:191], v210 offset:30720\n"
    "ds_read_b64_tr_b16 a[128:129], v209 offset:32768\n"
    "v_mfma_f32_16x16x32_bf16 v[88:91], a[152:155], a[116:119], v[88:91]\n"
    "ds_read_b64_tr_b16 a[130:131], v209 offset:34816\n"
    "ds_read_b64_tr_b16 a[132:133], v209 offset:36864\n"
    "v_mfma_f32_16x16x32_bf16 v[88:91], a[156:159], a[124:127], v[88:91]\n"
    "s_waitcnt lgkmcnt(11)\n"
    "buffer_load_dwordx4 v242, s[12:15], s41 offen offset:1024 lds\n"
    "v_mfma_f32_16x16x32_bf16 v[84:87], a[160:163], a[64:67], 0\n"
    "ds_read_b64_tr_b16 a[134:135], v209 offset:38912\n"
    "ds_read_b64_tr_b16 a[136:137], v209 offset:40960\n"
    "v_mfma_f32_16x16x32_bf16 v[84:87], a[164:167], a[72:75], v[84:87]\n"
    "ds_read_b64_tr_b16 a[138:139], v209 offset:43008\n"
    "ds_read_b64_tr_b16 a[140:141], v209 offset:45056\n"
    "v_mfma_f32_16x16x32_bf16 v[84:87], a[168:171], a[80:83], v[84:87]\n"
    "buffer_load_dwordx4 v243, s[12:15], s41 offen offset:2048 lds\n"
    "v_mfma_f32_16x16x32_bf16 v[84:87], a[172:175], a[88:91], v[84:87]\n"
    "s_waitcnt lgkmcnt(7)\n"
    "ds_read_b64_tr_b16 a[142:143], v209 offset:47104\n"
    "ds_read_b64_tr_b16 a[144:145], v209 offset:49152\n"
    "v_mfma_f32_16x16x32_bf16 v[84:87], a[176:179], a[96:99], v[84:87]\n"
    "ds_read_b64_tr_b16 a[146:147], v209 offset:51200\n"
    "ds_read_b64_tr_b16 a[148:149], v209 offset:53248\n"
    "v_mfma_f32_16x16x32_bf16 v[84:87], a[180:183], a[104:107], v[84:87]\n"
    "buffer_load_dwordx4 v244, s[12:15], s41 offen offset:3072 lds\n"
    "v_mfma_f32_16x16x32_bf16 v[84:87], a[184:187], a[112:115], v[84:87]\n"
    "ds_read_b64_tr_b16 a[150:151], v209 offset:55296\n"
    "s_add_u32 s41, s65, 0x1c280\n"
    "v_mfma_f32_16x16x32_bf16 v[84:87], a[188:191], a[120:123], v[84:87]\n"
    "ds_read_b64_tr_b16 a[152:153], v209 offset:57344\n"
    "s_add_u32 m0, s36, 0x24000\n"
    "v_mfma_f32_16x16x32_bf16 v[92:95], a[160:163], a[68:71], 0\n"
    "buffer_load_dwordx4 v241, s[12:15], s41 offen offset:0 lds\n"
    "v_mfma_f32_16x16x32_bf16 v[92:95], a[164:167], a[76:79], v[92:95]\n"
    "ds_read_b64_tr_b16 a[154:155], v209 offset:59392\n"
    "ds_read_b64_tr_b16 a[156:157], v209 offset:61440\n"
    "v_mfma_f32_16x16x32_bf16 v[92:95], a[168:171], a[84:87], v[92:95]\n"
    "ds_read_b64_tr_b16 a[158:159], v209 offset:63488\n"
    "ds_read_b64_tr_b16 a[160:161], v210 offset:32768\n"
    "v_mfma_f32_16x16x32_bf16 v[92:95], a[172:175], a[92:95], v[92:95]\n"
    "buffer_load_dwordx4 v242, s[12:15], s41 offen offset:1024 lds\n"
    "v_mfma_f32_16x16x32_bf16 v[92:95], a[176:179], a[100:103], v[92:95]\n"
    "ds_read_b64_tr_b16 a[162:163], v210 offset:34816\n"
    "ds_read_b64_tr_b16 a[164:165], v210 offset:36864\n"
    "v_mfma_f32_16x16x32_bf16 v[92:95], a[180:183], a[108:111], v[92:95]\n"
    "ds_read_b64_tr_b16 a[166:167], v210 offset:38912\n"
    "ds_read_b64_tr_b16 a[168:169], v210 offset:40960\n"
    "v_mfma_f32_16x16x32_bf16 v[92:95], a[184:187], a[116:119], v[92:95]\n"
    "buffer_load_dwordx4 v243, s[12:15], s41 offen offset:2048 lds\n"
    "v_mfma_f32_16x16x32_bf16 v[92:95], a[188:191], a[124:127], v[92:95]\n"
    "s_waitcnt lgkmcnt(13)\n"
    "ds_read_b64_tr_b16 a[170:171], v210 offset:43008\n"
    "ds_read_b64_tr_b16 a[172:173], v210 offset:45056\n"
    "v_mfma_f32_16x16x32_bf16 v[96:99], a[128:131], a[64:67], 0\n"
    "ds_read_b64_tr_b16 a[174:175], v210 offset:47104\n"
    "ds_read_b64_tr_b16 a[176:177], v210 offset:49152\n"
    "v_mfma_f32_16x16x32_bf16 v[96:99], a[132:135], a[72:75], v[96:99]\n"
    "buffer_load_dwordx4 v244, s[12:15], s41 offen offset:3072 lds\n"
    "v_mfma_f32_16x16x32_bf16 v[96:99], a[136:139], a[80:83], v[96:99]\n"
    "ds_read_b64_tr_b16 a[178:179], v210 offset:51200\n"
    "s_add_u32 s43, s62, 0\n"
    "v_mfma_f32_16x16x32_bf16 v[96:99], a[140:143], a[88:91], v[96:99]\n"
    "s_waitcnt lgkmcnt(10)\n"
    "ds_read_b64_tr_b16 a[180:181], v210 offset:53248\n"
    "v_add_u32 v254, s43, v246\n"
    "v_mfma_f32_16x16x32_bf16 v[96:99], a[144:147], a[96:99], v[96:99]\n"
    "ds_read_b64_tr_b16 a[182:183], v210 offset:55296\n"
    "v_and_b32 v254, 7, v254\n"
    "v_mfma_f32_16x16x32_bf16 v[96:99], a[148:151], a[104:107], v[96:99]\n"
    "ds_read_b64_tr_b16 a[184:185], v210 offset:57344\n"
    "v_mul_u32_u24 v254, 57600, v254\n"
    "v_mfma_f32_16x16x32_bf16 v[96:99], a[152:155], a[112:115], v[96:99]\n"
    "ds_read_b64_tr_b16 a[186:187], v210 offset:59392\n"
    "v_add_u32 v203, v254, v233\n"
    "v_mfma_f32_16x16x32_bf16 v[96:99], a[156:159], a[120:123], v[96:99]\n"
    "ds_read_b64_tr_b16 a[188:189], v210 offset:61440\n"
    "v_add_u32 v201, v254, v220\n"
    "v_mfma_f32_16x16x32_bf16 v[104:107], a[128:131], a[68:71], 0\n"
    "ds_read_b64_tr_b16 a[190:191], v210 offset:63488\n"
    "s_add_u32 s43, s62, 4\n"
    "v_mfma_f32_16x16x32_bf16 v[104:107], a[132:135], a[76:79], v[104:107]\n"
    "s_waitcnt vmcnt(16)\n"
    "s_barrier\n"
    "v_mfma_f32_16x16x32_bf16 v[104:107], a[136:139], a[84:87], v[104:107]\n"
    "buffer_store_dword v226, v216, s[52:55], 0 offen offset:0 sc1\n"
    "v_mfma_f32_16x16x32_bf16 v[104:107], a[140:143], a[92:95], v[104:107]\n"
    "ds_read_b64_tr_b16 a[128:129], v211 offset:0\n"
    "s_add_u32 s41, s65, 0x300\n"
    "v_mfma_f32_16x16x32_bf16 v[104:107], a[144:147], a[100:103], v[104:107]\n"
    "ds_read_b64_tr_b16 a[130:131], v211 offset:2048\n"
    "s_add_u32 m0, s36, 0x0\n"
    "v_mfma_f32_16x16x32_bf16 v[104:107], a[148:151], a[108:111], v[104:107]\n"
    "buffer_load_dwordx4 v241, s[12:15], s41 offen offset:0 lds\n"
    "v_mfma_f32_16x16x32_bf16 v[104:107], a[152:155], a[116:119], v[104:107]\n"
    "ds_read_b64_tr_b16 a[132:133], v211 offset:4096\n"
    "ds_read_b64_tr_b16 a[134:135], v211 offset:6144\n"
    "v_mfma_f32_16x16x32_bf16 v[104:107], a[156:159], a[124:127], v[104:107]\n"
    "s_waitcnt lgkmcnt(12)\n"
    "ds_read_b64_tr_b16 a[136:137], v211 offset:8192\n"
    "ds_read_b64_tr_b16 a[138:139], v211 offset:10240\n"
    "v_mfma_f32_16x16x32_bf16 v[100:103], a[160:163], a[64:67], 0\n"
    "buffer_load_dwordx4 v242, s[12:15], s41 offen offset:1024 lds\n"
    "v_mfma_f32_16x16x32_bf16 v[100:103], a[164:167], a[72:75], v[100:103]\n"
    "ds_read_b64_tr_b16 a[140:141], v211 offset:12288\n"
    "ds_read_b64_tr_b16 a[142:143], v211 offset:14336\n"
    "v_mfma_f32_16x16x32_bf16 v[100:103], a[168:171], a[80:83], v[100:103]\n"
    "ds_read_b64_tr_b16 a[144:145], v211 offset:16384\n"
    "ds_read_b64_tr_b16 a[146:147], v211 offset:18432\n"
    "v_mfma_f32_16x16x32_bf16 v[100:103], a[172:175], a[88:91], v[100:103]\n"
    "s_waitcnt lgkmcnt(10)\n"
    "buffer_load_dwordx4 v243, s[12:15], s41 offen offset:2048 lds\n"
    "v_mfma_f32_16x16x32_bf16 v[100:103], a[176:179], a[96:99], v[100:103]\n"
    "ds_read_b64_tr_b16 a[148:149], v211 offset:20480\n"
    "v_add_u32 v254, s43, v246\n"
    "v_mfma_f32_16x16x32_bf16 v[100:103], a[180:183], a[104:107], v[100:103]\n"
    "ds_read_b64_tr_b16 a[150:151], v211 offset:22528\n"
    "ds_read_b64_tr_b16 a[152:153], v211 offset:24576\n"
    "v_mfma_f32_16x16x32_bf16 v[100:103], a[184:187], a[112:115], v[100:103]\n"
    "buffer_load_dwordx4 v244, s[12:15], s41 offen offset:3072 lds\n"
    "v_mfma_f32_16x16x32_bf16 v[100:103], a[188:191], a[120:123], v[100:103]\n"
    "ds_read_b64_tr_b16 a[154:155], v211 offset:26624\n"
    "s_add_u32 s41, s65, 0x1c300\n"
    "v_mfma_f32_16x16x32_bf16 v[108:111], a[160:163], a[68:71], 0\n"
    "ds_read_b64_tr_b16 a[156:157], v211 offset:28672\n"
    "s_add_u32 m0, s36, 0x4000\n"
    "v_mfma_f32_16x16x32_bf16 v[108:111], a[164:167], a[76:79], v[108:111]\n"
    "buffer_load_dwordx4 v241, s[12:15], s41 offen offset:0 lds\n"
    "v_mfma_f32_16x16x32_bf16 v[108:111], a[168:171], a[84:87], v[108:111]\n"
    "ds_read_b64_tr_b16 a[158:159], v211 offset:30720\n"
    "ds_read_b64_tr_b16 a[160:161], v212 offset:0\n"
    "v_mfma_f32_16x16x32_bf16 v[108:111], a[172:175], a[92:95], v[108:111]\n"
    "ds_read_b64_tr_b16 a[162:163], v212 offset:2048\n"
    "ds_read_b64_tr_b16 a[164:165], v212 offset:4096\n"
    "v_mfma_f32_16x16x32_bf16 v[108:111], a[176:179], a[100:103], v[108:111]\n"
    "buffer_load_dwordx4 v242, s[12:15], s41 offen offset:1024 lds\n"
    "v_mfma_f32_16x16x32_bf16 v[108:111], a[180:183], a[108:111], v[108:111]\n"
    "ds_read_b64_tr_b16 a[166:167], v212 offset:6144\n"
    "ds_read_b64_tr_b16 a[168:169], v212 offset:8192\n"
    "v_mfma_f32_16x16x32_bf16 v[108:111], a[184:187], a[116:119], v[108:111]\n"
    "ds_read_b64_tr_b16 a[170:171], v212 offset:10240\n"
    "ds_read_b64_tr_b16 a[172:173], v212 offset:12288\n"
    "v_mfma_f32_16x16x32_bf16 v[108:111], a[188:191], a[124:127], v[108:111]\n"
    "s_waitcnt lgkmcnt(15)\n"
    "buffer_load_dwordx4 v243, s[12:15], s41 offen offset:2048 lds\n"
    "v_mfma_f32_16x16x32_bf16 v[112:115], a[128:131], a[64:67], 0\n"
    "ds_read_b64_tr_b16 a[174:175], v212 offset:14336\n"
    "ds_read_b64_tr_b16 a[176:177], v212 offset:16384\n"
    "v_mfma_f32_16x16x32_bf16 v[112:115], a[132:135], a[72:75], v[112:115]\n"
    "ds_read_b64_tr_b16 a[178:179], v212 offset:18432\n"
    "v_and_b32 v254, 7, v254\n"
    "v_mfma_f32_16x16x32_bf16 v[112:115], a[136:139], a[80:83], v[112:115]\n"
    "buffer_load_dwordx4 v244, s[12:15], s41 offen offset:3072 lds\n"
    "v_mfma_f32_16x16x32_bf16 v[112:115], a[140:143], a[88:91], v[112:115]\n"
    "s_waitcnt lgkmcnt(10)\n"
    "ds_read_b64_tr_b16 a[180:181], v212 offset:20480\n"
    "v_mul_u32_u24 v254, 57600, v254\n"
    "v_mfma_f32_16x16x32_bf16 v[112:115], a[144:147], a[96:99], v[112:115]\n"
    "ds_read_b64_tr_b16 a[182:183], v212 offset:22528\n"
    "v_add_u32 v204, v254, v233\n"
    "v_mfma_f32_16x16x32_bf16 v[112:115], a[148:151], a[104:107], v[112:115]\n"
    "ds_read_b64_tr_b16 a[184:185], v212 offset:24576\n"
    "v_add_u32 v202, v254, v220\n"
    "v_mfma_f32_16x16x32_bf16 v[112:115], a[152:155], a[112:115], v[112:115]\n"
    "ds_read_b64_tr_b16 a[186:187], v212 offset:26624\n"
    "v_min_u32 v254, 1, v246\n"
    "v_mfma_f32_16x16x32_bf16 v[112:115], a[156:159], a[120:123], v[112:115]\n"
    "ds_read_b64_tr_b16 a[188:189], v212 offset:28672\n"
    "v_xor_b32 v233, 1, v254\n"
    "v_mfma_f32_16x16x32_bf16 v[120:123], a[128:131], a[68:71], 0\n"
    "ds_read_b64_tr_b16 a[190:191], v212 offset:30720\n"
    "v_lshl_add_u32 v201, v233, 30, v201\n"
    "v_mfma_f32_16x16x32_bf16 v[120:123], a[132:135], a[76:79], v[120:123]\n"
    "s_waitcnt vmcnt(17)\n"
    "s_barrier\n"
    "v_mfma_f32_16x16x32_bf16 v[120:123], a[136:139], a[84:87], v[120:123]\n"
    "ds_read_b64_tr_b16 a[128:129], v211 offset:32768\n"
    "s_add_u32 s41, s66, 0x0\n"
    "v_mfma_f32_16x16x32_bf16 v[120:123], a[140:143], a[92:95], v[120:123]\n"
    "ds_read_b64_tr_b16 a[130:131], v211 offset:34816\n"
    "s_add_u32 m0, s36, 0x8000\n"
    "v_mfma_f32_16x16x32_bf16 v[120:123], a[144:147], a[100:103], v[120:123]\n"
    "buffer_load_dwordx4 v241, s[12:15], s41 offen offset:0 lds\n"
    "v_mfma_f32_16x16x32_bf16 v[120:123], a[148:151], a[108:111], v[120:123]\n"
    "ds_read_b64_tr_b16 a[132:133], v211 offset:36864\n"
    "ds_read_b64_tr_b16 a[134:135], v211 offset:38912\n"
    "v_mfma_f32_16x16x32_bf16 v[120:123], a[152:155], a[116:119], v[120:123]\n"
    "ds_read_b64_tr_b16 a[136:137], v211 offset:40960\n"
    "ds_read_b64_tr_b16 a[138:139], v211 offset:43008\n"
    "v_mfma_f32_16x16x32_bf16 v[120:123], a[156:159], a[124:127], v[120:123]\n"
    "s_waitcnt lgkmcnt(14)\n"
    "buffer_load_dwordx4 v242, s[12:15], s41 offen offset:1024 lds\n"
    "v_mfma_f32_16x16x32_bf16 v[116:119], a[160:163], a[64:67], 0\n"
    "ds_read_b64_tr_b16 a[140:141], v211 offset:45056\n"
    "ds_read_b64_tr_b16 a[142:143], v211 offset:47104\n"
    "v_mfma_f32_16x16x32_bf16 v[116:119], a[164:167], a[72:75], v[116:119]\n"
    "ds_read_b64_tr_b16 a[144:145], v211 offset:49152\n"
    "ds_read_b64_tr_b16 a[146:147], v211 offset:51200\n"
    "v_mfma_f32_16x16x32_bf16 v[116:119], a[168:171], a[80:83], v[116:119]\n"
    "buffer_load_dwordx4 v243, s[12:15], s41 offen offset:2048 lds\n"
    "v_mfma_f32_16x16x32_bf16 v[116:119], a[172:175], a[88:91], v[116:119]\n"
    "s_waitcnt lgkmcnt(10)\n"
    "ds_read_b64_tr_b16 a[148:149], v211 offset:53248\n"
    "v_and_b32 v233, 0x7ff, v220\n"
    "v_mfma_f32_16x16x32_bf16 v[116:119], a[176:179], a[96:99], v[116:119]\n"
    "ds_read_b64_tr_b16 a[150:151], v211 offset:55296\n"
    "v_add_u32 v234, -1, v254\n"
    "v_mfma_f32_16x16x32_bf16 v[116:119], a[180:183], a[104:107], v[116:119]\n"
    "buffer_load_dwordx4 v244, s[12:15], s41 offen offset:3072 lds\n"
    "v_mfma_f32_16x16x32_bf16 v[116:119], a[184:187], a[112:115], v[116:119]\n"
    "ds_read_b64_tr_b16 a[152:153], v211 offset:57344\n"
    "ds_read_b64_tr_b16 a[154:155], v211 offset:59392\n"
    "v_mfma_f32_16x16x32_bf16 v[116:119], a[188:191], a[120:123], v[116:119]\n"
    "ds_read_b64_tr_b16 a[156:157], v211 offset:61440\n"
    "ds_read_b64_tr_b16 a[158:159], v211 offset:63488\n"
    "v_mfma_f32_16x16x32_bf16 v[124:127], a[160:163], a[68:71], 0\n"
    "buffer_load_dword v227, v218, s[52:55], 0 offen offset:96 sc1\n"
    "v_mfma_f32_16x16x32_bf16 v[124:127], a[164:167], a[76:79], v[124:127]\n"
    "ds_read_b64_tr_b16 a[160:161], v212 offset:32768\n"
    "s_add_u32 s41, s66, 0x1c000\n"
    "v_mfma_f32_16x16x32_bf16 v[124:127], a[168:171], a[84:87], v[124:127]\n"
    "ds_read_b64_tr_b16 a[162:163], v212 offset:34816\n"
    "s_add_u32 m0, s36, 0xc000\n"
    "v_mfma_f32_16x16x32_bf16 v[124:127], a[172:175], a[92:95], v[124:127]\n"
    "buffer_load_dwordx4 v241, s[12:15], s41 offen offset:0 lds\n"
    "v_mfma_f32_16x16x32_bf16 v[124:127], a[176:179], a[100:103], v[124:127]\n"
    "ds_read_b64_tr_b16 a[164:165], v212 offset:36864\n"
    "ds_read_b64_tr_b16 a[166:167], v212 offset:38912\n"
    "v_mfma_f32_16x16x32_bf16 v[124:127], a[180:183], a[108:111], v[124:127]\n"
    "ds_read_b64_tr_b16 a[168:169], v212 offset:40960\n"
    "ds_read_b64_tr_b16 a[170:171], v212 offset:43008\n"
    "v_mfma_f32_16x16x32_bf16 v[124:127], a[184:187], a[116:119], v[124:127]\n"
    "buffer_load_dwordx4 v242, s[12:15], s41 offen offset:1024 lds\n"
    "v_mfma_f32_16x16x32_bf16 v[124:127], a[188:191], a[124:127], v[124:127]\n"
    "s_waitcnt lgkmcnt(14)\n"
    "ds_read_b64_tr_b16 a[172:173], v212 offset:45056\n"
    "ds_read_b64_tr_b16 a[174:175], v212 offset:47104\n"
    "v_mfma_f32_16x16x32_bf16 v[128:131], a[128:131], a[64:67], 0\n"
    "ds_read_b64_tr_b16 a[176:177], v212 offset:49152\n"
    "ds_read_b64_tr_b16 a[178:179], v212 offset:51200\n"
    "v_mfma_f32_16x16x32_bf16 v[128:131], a[132:135], a[72:75], v[128:131]\n"
    "buffer_load_dwordx4 v243, s[12:15], s41 offen offset:2048 lds\n"
    "v_mfma_f32_16x16x32_bf16 v[128:131], a[136:139], a[80:83], v[128:131]\n"
    "ds_read_b64_tr_b16 a[180:181], v212 offset:53248\n"
    "v_add_u32 v233, 0x18000, v233\n"
    "v_mfma_f32_16x16x32_bf16 v[128:131], a[140:143], a[88:91], v[128:131]\n"
    "s_waitcnt lgkmcnt(11)\n"
    "ds_read_b64_tr_b16 a[182:183], v212 offset:55296\n"
    "s_lshr_b32 s43, s57, 3\n"
    "v_mfma_f32_16x16x32_bf16 v[128:131], a[144:147], a[96:99], v[128:131]\n"
    "buffer_load_dwordx4 v244, s[12:15], s41 offen offset:3072 lds\n"
    "v_mfma_f32_16x16x32_bf16 v[128:131], a[148:151], a[104:107], v[128:131]\n"
    "ds_read_b64_tr_b16 a[184:185], v212 offset:57344\n"
    "ds_read_b64_tr_b16 a[186:187], v212 offset:59392\n"
    "v_mfma_f32_16x16x32_bf16 v[128:131], a[152:155], a[112:115], v[128:131]\n"
    "ds_read_b64_tr_b16 a[188:189], v212 offset:61440\n"
    "s_and_b32 s19, s68, 1\n"
    "v_mfma_f32_16x16x32_bf16 v[128:131], a[156:159], a[120:123], v[128:131]\n"
    "ds_read_b64_tr_b16 a[190:191], v212 offset:63488\n"
    "s_cmp_eq_u32 s43, s19\n"
    "s_cselect_b32 s19, 1, 0\n"
    "v_mfma_f32_16x16x32_bf16 v[136:139], a[128:131], a[68:71], 0\n"
    "s_bfe_u32 s43, s57, 0x10002\n"
    "s_lshl_b32 s19, s19, s43\n"
    "v_mfma_f32_16x16x32_bf16 v[136:139], a[132:135], a[76:79], v[136:139]\n"
    "s_waitcnt vmcnt(18)\n"
    "s_barrier\n"
    "v_mfma_f32_16x16x32_bf16 v[136:139], a[136:139], a[84:87], v[136:139]\n"
    "ds_read_b64_tr_b16 a[128:129], v213 offset:0\n"
    "s_add_u32 s41, s66, 0x80\n"
    "v_mfma_f32_16x16x32_bf16 v[136:139], a[140:143], a[92:95], v[136:139]\n"
    "ds_read_b64_tr_b16 a[130:131], v213 offset:2048\n"
    "s_add_u32 m0, s36, 0x10000\n"
    "v_mfma_f32_16x16x32_bf16 v[136:139], a[144:147], a[100:103], v[136:139]\n"
    "buffer_load_dwordx4 v241, s[12:15], s41 offen offset:0 lds\n"
    "v_mfma_f32_16x16x32_bf16 v[136:139], a[148:151], a[108:111], v[136:139]\n"
    "ds_read_b64_tr_b16 a[132:133], v213 offset:4096\n"
    "ds_read_b64_tr_b16 a[134:135], v213 offset:6144\n"
    "v_mfma_f32_16x16x32_bf16 v[136:139], a[152:155], a[116:119], v[136:139]\n"
    "ds_read_b64_tr_b16 a[136:137], v213 offset:8192\n"
    "ds_read_b64_tr_b16 a[138:139], v213 offset:10240\n"
    "v_mfma_f32_16x16x32_bf16 v[136:139], a[156:159], a[124:127], v[136:139]\n"
    "s_waitcnt lgkmcnt(14)\n"
    "buffer_load_dwordx4 v242, s[12:15], s41 offen offset:1024 lds\n"
    "v_mfma_f32_16x16x32_bf16 v[132:135], a[160:163], a[64:67], 0\n"
    "ds_read_b64_tr_b16 a[140:141], v213 offset:12288\n"
    "ds_read_b64_tr_b16 a[142:143], v213 offset:14336\n"
    "v_mfma_f32_16x16x32_bf16 v[132:135], a[164:167], a[72:75], v[132:135]\n"
    "ds_read_b64_tr_b16 a[144:145], v213 offset:16384\n"
    "ds_read_b64_tr_b16 a[146:147], v213 offset:18432\n"
    "v_mfma_f32_16x16x32_bf16 v[132:135], a[168:171], a[80:83], v[132:135]\n"
    "buffer_load_dwordx4 v243, s[12:15], s41 offen offset:2048 lds\n"
    "v_mfma_f32_16x16x32_bf16 v[132:135], a[172:175], a[88:91], v[132:135]\n"
    "s_waitcnt lgkmcnt(10)\n"
    "ds_read_b64_tr_b16 a[148:149], v213 offset:20480\n"
    "ds_read_b64_tr_b16 a[150:151], v213 offset:22528\n"
    "v_mfma_f32_16x16x32_bf16 v[132:135], a[176:179], a[96:99], v[132:135]\n"
    "ds_read_b64_tr_b16 a[152:153], v213 offset:24576\n"
    "s_bfe_u32 s43, s57, 0x10001\n"
    "v_mfma_f32_16x16x32_bf16 v[132:135], a[180:183], a[104:107], v[132:135]\n"
    "buffer_load_dwordx4 v244, s[12:15], s41 offen offset:3072 lds\n"
    "v_mfma_f32_16x16x32_bf16 v[132:135], a[184:187], a[112:115], v[132:135]\n"
    "ds_read_b64_tr_b16 a[154:155], v213 offset:26624\n"
    "s_add_u32 s41, s66, 0x1c080\n"
    "v_mfma_f32_16x16x32_bf16 v[132:135], a[188:191], a[120:123], v[132:135]\n"
    "ds_read_b64_tr_b16 a[156:157], v213 offset:28672\n"
    "s_add_u32 m0, s36, 0x14000\n"
    "v_mfma_f32_16x16x32_bf16 v[140:143], a[160:163], a[68:71], 0\n"
    "buffer_load_dwordx4 v241, s[12:15], s41 offen offset:0 lds\n"
    "v_mfma_f32_16x16x32_bf16 v[140:143], a[164:167], a[76:79], v[140:143]\n"
    "ds_read_b64_tr_b16 a[158:159], v213 offset:30720\n"
    "ds_read_b64_tr_b16 a[160:161], v214 offset:0\n"
    "v_mfma_f32_16x16x32_bf16 v[140:143], a[168:171], a[84:87], v[140:143]\n"
    "ds_read_b64_tr_b16 a[162:163], v214 offset:2048\n"
    "ds_read_b64_tr_b16 a[164:165], v214 offset:4096\n"
    "v_mfma_f32_16x16x32_bf16 v[140:143], a[172:175], a[92:95], v[140:143]\n"
    "buffer_load_dwordx4 v242, s[12:15], s41 offen offset:1024 lds\n"
    "v_mfma_f32_16x16x32_bf16 v[140:143], a[176:179], a[100:103], v[140:143]\n"
    "ds_read_b64_tr_b16 a[166:167], v214 offset:6144\n"
    "ds_read_b64_tr_b16 a[168:169], v214 offset:8192\n"
    "v_mfma_f32_16x16x32_bf16 v[140:143], a[180:183], a[108:111], v[140:143]\n"
    "ds_read_b64_tr_b16 a[170:171], v214 offset:10240\n"
    "ds_read_b64_tr_b16 a[172:173], v214 offset:12288\n"
    "v_mfma_f32_16x16x32_bf16 v[140:143], a[184:187], a[116:119], v[140:143]\n"
    "s_waitcnt vmcnt(10)\n"
    "buffer_load_dwordx4 v243, s[12:15], s41 offen offset:2048 lds\n"
    "v_mfma_f32_16x16x32_bf16 v[140:143], a[188:191], a[124:127], v[140:143]\n"
    "s_waitcnt lgkmcnt(15)\n"
    "ds_read_b64_tr_b16 a[174:175], v214 offset:14336\n"
    "ds_read_b64_tr_b16 a[176:177], v214 offset:16384\n"
    "v_mfma_f32_16x16x32_bf16 v[144:147], a[128:131], a[64:67], 0\n"
    "ds_read_b64_tr_b16 a[178:179], v214 offset:18432\n"
    "ds_read_b64_tr_b16 a[180:181], v214 offset:20480\n"
    "v_mfma_f32_16x16x32_bf16 v[144:147], a[132:135], a[72:75], v[144:147]\n"
    "ds_read_b64_tr_b16 a[182:183], v214 offset:22528\n"
    "v_cmp_ne_u32 vcc, s56, v227\n"
    "v_mfma_f32_16x16x32_bf16 v[144:147], a[136:139], a[80:83], v[144:147]\n"
    "ds_read_b64_tr_b16 a[184:185], v214 offset:24576\n"
    "v_lshrrev_b32 v254, 3, v247\n"
    "v_mfma_f32_16x16x32_bf16 v[144:147], a[140:143], a[88:91], v[144:147]\n"
    "s_waitcnt lgkmcnt(13)\n"
    "ds_read_b64_tr_b16 a[186:187], v214 offset:26624\n"
    "s_cbranch_vccnz LSPINX%=\n"
    "LGOX%=:\n"
    "v_mfma_f32_16x16x32_bf16 v[144:147], a[144:147], a[96:99], v[144:147]\n"
    "buffer_load_dword v176, v255, s[88:91], 0 offen offset:0 sc1\n"
    "v_mfma_f32_16x16x32_bf16 v[144:147], a[148:151], a[104:107], v[144:147]\n"
    "buffer_load_dword v178, v255, s[44:47], 0 offen offset:0 sc1\n"
    "v_mfma_f32_16x16x32_bf16 v[144:147], a[152:155], a[112:115], v[144:147]\n"
    "ds_read_b64_tr_b16 a[188:189], v214 offset:28672\n"
    "ds_read_b64_tr_b16 a[190:191], v214 offset:30720\n"
    "v_mfma_f32_16x16x32_bf16 v[144:147], a[156:159], a[120:123], v[144:147]\n"
    "buffer_load_dword v177, v255, s[88:91], 0 offen offset:128 sc1\n"
    "v_mfma_f32_16x16x32_bf16 v[152:155], a[128:131], a[68:71], 0\n"
    "v_xor_b32 v254, s43, v254\n"
    "v_xor_b32 v254, 1, v254\n"
    "v_mfma_f32_16x16x32_bf16 v[152:155], a[132:135], a[76:79], v[152:155]\n"
    "s_barrier\n"
    "v_mfma_f32_16x16x32_bf16 v[152:155], a[136:139], a[84:87], v[152:155]\n"
    "buffer_load_dword v179, v255, s[44:47], 0 offen offset:128 sc1\n"
    "v_mfma_f32_16x16x32_bf16 v[152:155], a[140:143], a[92:95], v[152:155]\n"
    "ds_read_b64_tr_b16 a[128:129], v209 offset:0\n"
    "s_and_b32 s43, s19, 1\n"
    "v_mfma_f32_16x16x32_bf16 v[152:155], a[144:147], a[100:103], v[152:155]\n"
    "ds_read_b64_tr_b16 a[130:131], v209 offset:2048\n"
    "ds_read_b64_tr_b16 a[132:133], v209 offset:4096\n"
    "v_mfma_f32_16x16x32_bf16 v[152:155], a[148:151], a[108:111], v[152:155]\n"
    "buffer_load_dwordx4 a[0:3], v223, s[44:47], 0 offen offset:0 sc1\n"
    "v_mfma_f32_16x16x32_bf16 v[152:155], a[152:155], a[116:119], v[152:155]\n"
    "ds_read_b64_tr_b16 a[134:135], v209 offset:6144\n"
    "ds_read_b64_tr_b16 a[136:137], v209 offset:8192\n"
    "v_mfma_f32_16x16x32_bf16 v[152:155], a[156:159], a[124:127], v[152:155]\n"
    "s_waitcnt lgkmcnt(13)\n"
    "ds_read_b64_tr_b16 a[138:139], v209 offset:10240\n"
    "ds_read_b64_tr_b16 a[140:141], v209 offset:12288\n"
    "v_mfma_f32_16x16x32_bf16 v[148:151], a[160:163], a[64:67], 0\n"
    "buffer_load_dwordx4 a[8:11], v223, s[44:47], 0 offen offset:1024 sc1\n"
    "v_mfma_f32_16x16x32_bf16 v[148:151], a[164:167], a[72:75], v[148:151]\n"
    "ds_read_b64_tr_b16 a[142:143], v209 offset:14336\n"
    "ds_read_b64_tr_b16 a[144:145], v209 offset:16384\n"
    "v_mfma_f32_16x16x32_bf16 v[148:151], a[168:171], a[80:83], v[148:151]\n"
    "ds_read_b64_tr_b16 a[146:147], v209 offset:18432\n"
    "ds_read_b64_tr_b16 a[148:149], v209 offset:20480\n"
    "v_mfma_f32_16x16x32_bf16 v[148:151], a[172:175], a[88:91], v[148:151]\n"
    "s_waitcnt lgkmcnt(11)\n"
    "buffer_load_dwordx4 a[16:19], v223, s[44:47], 0 offen offset:2048 sc1\n"
    "v_mfma_f32_16x16x32_bf16 v[148:151], a[176:179], a[96:99], v[148:151]\n"
    "ds_read_b64_tr_b16 a[150:151], v209 offset:22528\n"
    "ds_read_b64_tr_b16 a[152:153], v209 offset:24576\n"
    "v_mfma_f32_16x16x32_bf16 v[148:151], a[180:183], a[104:107], v[148:151]\n"
    "ds_read_b64_tr_b16 a[154:155], v209 offset:26624\n"
    "ds_read_b64_tr_b16 a[156:157], v209 offset:28672\n"
    "v_mfma_f32_16x16x32_bf16 v[148:151], a[184:187], a[112:115], v[148:151]\n"
    "buffer_load_dwordx4 a[24:27], v223, s[44:47], 0 offen offset:3072 sc1\n"
    "v_mfma_f32_16x16x32_bf16 v[148:151], a[188:191], a[120:123], v[148:151]\n"
    "ds_read_b64_tr_b16 a[158:159], v209 offset:30720\n"
    "v_and_b32 v207, s43, v254\n"
    "v_mfma_f32_16x16x32_bf16 v[156:159], a[160:163], a[68:71], 0\n"
    "ds_read_b64_tr_b16 a[160:161], v210 offset:0\n"
    "ds_read_b64_tr_b16 a[162:163], v210 offset:2048\n"
    "v_mfma_f32_16x16x32_bf16 v[156:159], a[164:167], a[76:79], v[156:159]\n"
    "buffer_load_dwordx4 a[32:35], v223, s[44:47], s92 offen offset:0 sc1\n"
    "v_mfma_f32_16x16x32_bf16 v[156:159], a[168:171], a[84:87], v[156:159]\n"
    "ds_read_b64_tr_b16 a[164:165], v210 offset:4096\n"
    "ds_read_b64_tr_b16 a[166:167], v210 offset:6144\n"
    "v_mfma_f32_16x16x32_bf16 v[156:159], a[172:175], a[92:95], v[156:159]\n"
    "ds_read_b64_tr_b16 a[168:169], v210 offset:8192\n"
    "ds_read_b64_tr_b16 a[170:171], v210 offset:10240\n"
    "v_mfma_f32_16x16x32_bf16 v[156:159], a[176:179], a[100:103], v[156:159]\n"
    "buffer_load_dwordx4 a[40:43], v223, s[44:47], s92 offen offset:1024 sc1\n"
    "v_mfma_f32_16x16x32_bf16 v[156:159], a[180:183], a[108:111], v[156:159]\n"
    "ds_read_b64_tr_b16 a[172:173], v210 offset:12288\n"
    "ds_read_b64_tr_b16 a[174:175], v210 offset:14336\n"
    "v_mfma_f32_16x16x32_bf16 v[156:159], a[184:187], a[116:119], v[156:159]\n"
    "ds_read_b64_tr_b16 a[176:177], v210 offset:16384\n"
    "ds_read_b64_tr_b16 a[178:179], v210 offset:18432\n"
    "v_mfma_f32_16x16x32_bf16 v[156:159], a[188:191], a[124:127], v[156:159]\n"
    "s_waitcnt lgkmcnt(15)\n"
    "buffer_load_dwordx4 a[48:51], v223, s[44:47], s92 offen offset:2048 sc1\n"
    "v_mfma_f32_16x16x32_bf16 v[160:163], a[128:131], a[64:67], 0\n"
    "ds_read_b64_tr_b16 a[180:181], v210 offset:20480\n"
    "ds_read_b64_tr_b16 a[182:183], v210 offset:22528\n"
    "v_mfma_f32_16x16x32_bf16 v[160:163], a[132:135], a[72:75], v[160:163]\n"
    "ds_read_b64_tr_b16 a[184:185], v210 offset:24576\n"
    "v_lshl_add_u32 v205, v207, 30, v229\n"
    "v_mfma_f32_16x16x32_bf16 v[160:163], a[136:139], a[80:83], v[160:163]\n"
    "buffer_load_dwordx4 a[56:59], v223, s[44:47], s92 offen offset:3072 sc1\n"
    "v_mfma_f32_16x16x32_bf16 v[160:163], a[140:143], a[88:91], v[160:163]\n"
    "s_waitcnt lgkmcnt(13)\n"
    "ds_read_b64_tr_b16 a[186:187], v210 offset:26624\n"
    "s_lshr_b32 s43, s19, 1\n"
    "v_mfma_f32_16x16x32_bf16 v[160:163], a[144:147], a[96:99], v[160:163]\n"
    "ds_read_b64_tr_b16 a[188:189], v210 offset:28672\n"
    "v_and_b32 v207, s43, v254\n"
    "v_mfma_f32_16x16x32_bf16 v[160:163], a[148:151], a[104:107], v[160:163]\n"
    "buffer_load_dwordx4 a[4:7], v223, s[44:47], s93 offen offset:0 sc1\n"
    "v_mfma_f32_16x16x32_bf16 v[160:163], a[152:155], a[112:115], v[160:163]\n"
    "ds_read_b64_tr_b16 a[190:191], v210 offset:30720\n"
    "v_lshl_add_u32 v206, v207, 30, v229\n"
    "v_mfma_f32_16x16x32_bf16 v[160:163], a[156:159], a[120:123], v[160:163]\n"
    "s_lshr_b32 s43, s68, 1\n"
    "s_lshl_b32 s43, s43, 10\n"
    "v_mfma_f32_16x16x32_bf16 v[168:171], a[128:131], a[68:71], 0\n"
    "buffer_load_dwordx4 a[12:15], v223, s[44:47], s93 offen offset:1024 sc1\n"
    "v_mfma_f32_16x16x32_bf16 v[168:171], a[132:135], a[76:79], v[168:171]\n"
    "s_waitcnt vmcnt(21)\n"
    "s_barrier\n"
    "v_mfma_f32_16x16x32_bf16 v[168:171], a[136:139], a[84:87], v[168:171]\n"
    "ds_read_b64_tr_b16 a[128:129], v209 offset:32768\n"
    "s_add_u32 s43, s43, 0x18000\n"
    "v_mfma_f32_16x16x32_bf16 v[168:171], a[140:143], a[92:95], v[168:171]\n"
    "buffer_load_dwordx4 a[20:23], v223, s[44:47], s93 offen offset:2048 sc1\n"
    "v_mfma_f32_16x16x32_bf16 v[168:171], a[144:147], a[100:103], v[168:171]\n"
    "ds_read_b64_tr_b16 a[130:131], v209 offset:34816\n"
    "ds_read_b64_tr_b16 a[132:133], v209 offset:36864\n"
    "v_mfma_f32_16x16x32_bf16 v[168:171], a[148:151], a[108:111], v[168:171]\n"
    "ds_read_b64_tr_b16 a[134:135], v209 offset:38912\n"
    "ds_read_b64_tr_b16 a[136:137], v209 offset:40960\n"
    "v_mfma_f32_16x16x32_bf16 v[168:171], a[152:155], a[116:119], v[168:171]\n"
    "buffer_load_dwordx4 a[28:31], v223, s[44:47], s93 offen offset:3072 sc1\n"
    "v_mfma_f32_16x16x32_bf16 v[168:171], a[156:159], a[124:127], v[168:171]\n"
    "s_waitcnt lgkmcnt(13)\n"
    "ds_read_b64_tr_b16 a[138:139], v209 offset:43008\n"
    "ds_read_b64_tr_b16 a[140:141], v209 offset:45056\n"
    "v_mfma_f32_16x16x32_bf16 v[164:167], a[160:163], a[64:67], 0\n"
    "ds_read_b64_tr_b16 a[142:143], v209 offset:47104\n"
    "ds_read_b64_tr_b16 a[144:145], v209 offset:49152\n"
    "v_mfma_f32_16x16x32_bf16 v[164:167], a[164:167], a[72:75], v[164:167]\n"
    "buffer_load_dwordx4 a[36:39], v223, s[44:47], s94 offen offset:0 sc1\n"
    "v_mfma_f32_16x16x32_bf16 v[164:167], a[168:171], a[80:83], v[164:167]\n"
    "ds_read_b64_tr_b16 a[146:147], v209 offset:51200\n"
    "ds_read_b64_tr_b16 a[148:149], v209 offset:53248\n"
    "v_mfma_f32_16x16x32_bf16 v[164:167], a[172:175], a[88:91], v[164:167]\n"
    "s_waitcnt lgkmcnt(11)\n"
    "ds_read_b64_tr_b16 a[150:151], v209 offset:55296\n"
    "ds_read_b64_tr_b16 a[152:153], v209 offset:57344\n"
    "v_mfma_f32_16x16x32_bf16 v[164:167], a[176:179], a[96:99], v[164:167]\n"
    "buffer_load_dwordx4 a[44:47], v223, s[44:47], s94 offen offset:1024 sc1\n"
    "v_mfma_f32_16x16x32_bf16 v[164:167], a[180:183], a[104:107], v[164:167]\n"
    "ds_read_b64_tr_b16 a[154:155], v209 offset:59392\n"
    "ds_read_b64_tr_b16 a[156:157], v209 offset:61440\n"
    "v_mfma_f32_16x16x32_bf16 v[164:167], a[184:187], a[112:115], v[164:167]\n"
    "ds_read_b64_tr_b16 a[158:159], v209 offset:63488\n"
    "v_lshlrev_b32 v207, 4, v245\n"
    "v_mfma_f32_16x16x32_bf16 v[164:167], a[188:191], a[120:123], v[164:167]\n"
    "buffer_load_dwordx4 a[52:55], v223, s[44:47], s94 offen offset:2048 sc1\n"
    "v_mfma_f32_16x16x32_bf16 v[172:175], a[160:163], a[68:71], 0\n"
    "ds_read_b64_tr_b16 a[160:161], v210 offset:32768\n"
    "ds_read_b64_tr_b16 a[162:163], v210 offset:34816\n"
    "v_mfma_f32_16x16x32_bf16 v[172:175], a[164:167], a[76:79], v[172:175]\n"
    "ds_read_b64_tr_b16 a[164:165], v210 offset:36864\n"
    "ds_read_b64_tr_b16 a[166:167], v210 offset:38912\n"
    "v_mfma_f32_16x16x32_bf16 v[172:175], a[168:171], a[84:87], v[172:175]\n"
    "buffer_load_dwordx4 v244, s[12:15], s41 offen offset:3072 lds\n"
    "v_mfma_f32_16x16x32_bf16 v[172:175], a[172:175], a[92:95], v[172:175]\n"
    "ds_read_b64_tr_b16 a[168:169], v210 offset:40960\n"
    "ds_read_b64_tr_b16 a[170:171], v210 offset:43008\n"
    "v_mfma_f32_16x16x32_bf16 v[172:175], a[176:179], a[100:103], v[172:175]\n"
    "ds_read_b64_tr_b16 a[172:173], v210 offset:45056\n"
    "ds_read_b64_tr_b16 a[174:175], v210 offset:47104\n"
    "v_mfma_f32_16x16x32_bf16 v[172:175], a[180:183], a[108:111], v[172:175]\n"
    "buffer_load_dwordx4 a[60:63], v223, s[44:47], s94 offen offset:3072 sc1\n"
    "v_mfma_f32_16x16x32_bf16 v[172:175], a[184:187], a[116:119], v[172:175]\n"
    "ds_read_b64_tr_b16 a[176:177], v210 offset:49152\n"
    "ds_read_b64_tr_b16 a[178:179], v210 offset:51200\n"
    "v_mfma_f32_16x16x32_bf16 v[172:175], a[188:191], a[124:127], v[172:175]\n"
    "s_waitcnt vmcnt(17)\n"
    "v_rcp_f32 v176, v176\n"
    "v_rcp_f32 v177, v177\n"
    "v_rcp_f32 v180, v178\n"
    "v_rcp_f32 v181, v179\n"
    "v_mul_f32 v176, v176, v178\n"
    "v_mul_f32 v177, v177, v179\n"
    "buffer_store_dword v180, v200, s[80:83], 0 offen offset:0 sc1\n"
    "buffer_store_dword v181, v200, s[80:83], 0 offen offset:64 sc1\n"
    "v_mul_f32 v64, v64, v176\n"
    "v_mul_f32 v65, v65, v176\n"
    "v_mul_f32 v66, v66, v176\n"
    "v_mul_f32 v67, v67, v176\n"
    "s_add_u32 s41, s66, 0x100\n"
    "s_add_u32 m0, s36, 0x18000\n"
    "s_nop 0\n"
    "buffer_load_dwordx4 v241, s[12:15], s41 offen offset:0 lds\n"
    "v_mul_f32 v68, v68, v176\n"
    "v_mul_f32 v69, v69, v176\n"
    "v_mul_f32 v70, v70, v176\n"
    "v_mul_f32 v71, v71, v176\n"
    "v_mul_f32 v72, v72, v177\n"
    "v_mul_f32 v73, v73, v177\n"
    "v_mul_f32 v74, v74, v177\n"
    "v_mul_f32 v75, v75, v177\n"
    "v_mul_f32 v76, v76, v177\n"
    "v_mul_f32 v77, v77, v177\n"
    "v_mul_f32 v78, v78, v177\n"
    "v_mul_f32 v79, v79, v177\n"
    "s_nop 0\n"
    "buffer_load_dwordx4 v242, s[12:15], s41 offen offset:1024 lds\n"
    "v_mul_f32 v80, v80, v176\n"
    "v_mul_f32 v81, v81, v176\n"
    "v_mul_f32 v82, v82, v176\n"
    "v_mul_f32 v83, v83, v176\n"
    "v_mul_f32 v84, v84, v176\n"
    "v_mul_f32 v85, v85, v176\n"
    "v_mul_f32 v86, v86, v176\n"
    "v_mul_f32 v87, v87, v176\n"
    "v_mul_f32 v88, v88, v177\n"
    "v_mul_f32 v89, v89, v177\n"
    "v_mul_f32 v90, v90, v177\n"
    "v_mul_f32 v91, v91, v177\n"
    "s_nop 0\n"
    "buffer_load_dwordx4 v243, s[12:15], s41 offen offset:2048 lds\n"
    "v_mul_f32 v92, v92, v177\n"
    "v_mul_f32 v93, v93, v177\n"
    "v_mul_f32 v94, v94, v177\n"
    "v_mul_f32 v95, v95, v177\n"
    "v_mul_f32 v96, v96, v176\n"
    "v_mul_f32 v97, v97, v176\n"
    "v_mul_f32 v98, v98, v176\n"
    "v_mul_f32 v99, v99, v176\n"
    "v_mul_f32 v100, v100, v176\n"
    "v_mul_f32 v101, v101, v176\n"
    "v_mul_f32 v102, v102, v176\n"
    "v_mul_f32 v103, v103, v176\n"
    "s_nop 0\n"
    "buffer_load_dwordx4 v244, s[12:15], s41 offen offset:3072 lds\n"
    "v_mul_f32 v104, v104, v177\n"
    "v_mul_f32 v105, v105, v177\n"
    "v_mul_f32 v106, v106, v177\n"
    "v_mul_f32 v107, v107, v177\n"
    "v_mul_f32 v108, v108, v177\n"
    "v_mul_f32 v109, v109, v177\n"
    "v_mul_f32 v110, v110, v177\n"
    "v_mul_f32 v111, v111, v177\n"
    "v_mul_f32 v112, v112, v176\n"
    "v_mul_f32 v113, v113, v176\n"
    "v_mul_f32 v114, v114, v176\n"
    "v_mul_f32 v115, v115, v176\n"
    "s_add_u32 s41, s66, 0x1c100\n"
    "s_add_u32 m0, s36, 0x1c000\n"
    "s_nop 0\n"
    "buffer_load_dwordx4 v241, s[12:15], s41 offen offset:0 lds\n"
    "v_mul_f32 v116, v116, v176\n"
    "v_mul_f32 v117, v117, v176\n"
    "v_mul_f32 v118, v118, v176\n"
    "v_mul_f32 v119, v119, v176\n"
    "v_mul_f32 v120, v120, v177\n"
    "v_mul_f32 v121, v121, v177\n"
    "v_mul_f32 v122, v122, v177\n"
    "v_mul_f32 v123, v123, v177\n"
    "v_mul_f32 v124, v124, v177\n"
    "v_mul_f32 v125, v125, v177\n"
    "v_mul_f32 v126, v126, v177\n"
    "v_mul_f32 v127, v127, v177\n"
    "s_nop 0\n"
    "buffer_load_dwordx4 v242, s[12:15], s41 offen offset:1024 lds\n"
    "v_mul_f32 v128, v128, v176\n"
    "v_mul_f32 v129, v129, v176\n"
    "v_mul_f32 v130, v130, v176\n"
    "v_mul_f32 v131, v131, v176\n"
    "v_mul_f32 v132, v132, v176\n"
    "v_mul_f32 v133, v133, v176\n"
    "v_mul_f32 v134, v134, v176\n"
    "v_mul_f32 v135, v135, v176\n"
    "v_mul_f32 v136, v136, v177\n"
    "v_mul_f32 v137, v137, v177\n"
    "v_mul_f32 v138, v138, v177\n"
    "v_mul_f32 v139, v139, v177\n"
    "s_nop 0\n"
    "buffer_load_dwordx4 v243, s[12:15], s41 offen offset:2048 lds\n"
    "v_mul_f32 v140, v140, v177\n"
    "v_mul_f32 v141, v141, v177\n"
    "v_mul_f32 v142, v142, v177\n"
    "v_mul_f32 v143, v143, v177\n"
    "v_mul_f32 v144, v144, v176\n"
    "v_mul_f32 v145, v145, v176\n"
    "v_mul_f32 v146, v146, v176\n"
    "v_mul_f32 v147, v147, v176\n"
    "v_mul_f32 v148, v148, v176\n"
    "v_mul_f32 v149, v149, v176\n"
    "v_mul_f32 v150, v150, v176\n"
    "v_mul_f32 v151, v151, v176\n"
    "s_nop 0\n"
    "buffer_load_dwordx4 v244, s[12:15], s41 offen offset:3072 lds\n"
    "v_mul_f32 v152, v152, v177\n"
    "v_mul_f32 v153, v153, v177\n"
    "v_mul_f32 v154, v154, v177\n"
    "v_mul_f32 v155, v155, v177\n"
    "v_mul_f32 v156, v156, v177\n"
    "v_mul_f32 v157, v157, v177\n"
    "v_mul_f32 v158, v158, v177\n"
    "v_mul_f32 v159, v159, v177\n"
    "v_mul_f32 v160, v160, v176\n"
    "v_mul_f32 v161, v161, v176\n"
    "v_mul_f32 v162, v162, v176\n"
    "v_mul_f32 v163, v163, v176\n"
    "s_add_u32 s41, s66, 0x180\n"
    "s_add_u32 m0, s36, 0x20000\n"
    "v_add_u32 v207, s43, v207\n"
    "s_waitcnt lgkmcnt(15)\n"
    "v_mul_f32 v164, v164, v176\n"
    "v_mul_f32 v165, v165, v176\n"
    "v_mul_f32 v166, v166, v176\n"
    "v_mul_f32 v167, v167, v176\n"
    "v_mul_f32 v168, v168, v177\n"
    "v_mul_f32 v169, v169, v177\n"
    "v_mul_f32 v170, v170, v177\n"
    "v_mul_f32 v171, v171, v177\n"
    "v_mul_f32 v172, v172, v177\n"
    "v_mul_f32 v173, v173, v177\n"
    "v_mul_f32 v174, v174, v177\n"
    "v_mul_f32 v175, v175, v177\n"
    "s_waitcnt vmcnt(19)\n"
    "v_mfma_f32_16x16x32_bf16 v[64:67], a[128:131], a[0:3], v[64:67]\n"
    "ds_read_b64_tr_b16 a[180:181], v210 offset:53248\n"
    "v_mfma_f32_16x16x32_bf16 v[64:67], a[132:135], a[8:11], v[64:67]\n"
    "buffer_load_dwordx4 v241, s[12:15], s41 offen offset:0 lds\n"
    "v_mfma_f32_16x16x32_bf16 v[64:67], a[136:139], a[16:19], v[64:67]\n"
    "ds_read_b64_tr_b16 a[182:183], v210 offset:55296\n"
    "v_mfma_f32_16x16x32_bf16 v[64:67], a[140:143], a[24:27], v[64:67]\n"
    "s_waitcnt lgkmcnt(12)\n"
    "ds_read_b64_tr_b16 a[184:185], v210 offset:57344\n"
    "v_mfma_f32_16x16x32_bf16 v[64:67], a[144:147], a[32:35], v[64:67]\n"
    "buffer_load_dwordx4 v242, s[12:15], s41 offen offset:1024 lds\n"
    "v_mfma_f32_16x16x32_bf16 v[64:67], a[148:151], a[40:43], v[64:67]\n"
    "ds_read_b64_tr_b16 a[186:187], v210 offset:59392\n"
    "ds_read_b64_tr_b16 a[188:189], v210 offset:61440\n"
    "v_mfma_f32_16x16x32_bf16 v[64:67], a[152:155], a[48:51], v[64:67]\n"
    "ds_read_b64_tr_b16 a[190:191], v210 offset:63488\n"
    "v_mfma_f32_16x16x32_bf16 v[64:67], a[156:159], a[56:59], v[64:67]\n"
    "s_waitcnt vmcnt(12)\n"
    "buffer_load_dwordx4 v243, s[12:15], s41 offen offset:2048 lds\n"
    "v_mfma_f32_16x16x32_bf16 v[72:75], a[128:131], a[4:7], v[72:75]\n"
    "v_mfma_f32_16x16x32_bf16 v[72:75], a[132:135], a[12:15], v[72:75]\n"
    "s_barrier\n"
    "v_mfma_f32_16x16x32_bf16 v[72:75], a[136:139], a[20:23], v[72:75]\n"
    "buffer_load_dwordx4 v244, s[12:15], s41 offen offset:3072 lds\n"
    "v_mfma_f32_16x16x32_bf16 v[72:75], a[140:143], a[28:31], v[72:75]\n"
    "ds_read_b64_tr_b16 a[128:129], v211 offset:0\n"
    "s_add_u32 s41, s66, 0x1c180\n"
    "v_mfma_f32_16x16x32_bf16 v[72:75], a[144:147], a[36:39], v[72:75]\n"
    "ds_read_b64_tr_b16 a[130:131], v211 offset:2048\n"
    "s_add_u32 m0, s36, 0x24000\n"
    "v_mfma_f32_16x16x32_bf16 v[72:75], a[148:151], a[44:47], v[72:75]\n"
    "buffer_load_dwordx4 v241, s[12:15], s41 offen offset:0 lds\n"
    "v_mfma_f32_16x16x32_bf16 v[72:75], a[152:155], a[52:55], v[72:75]\n"
    "ds_read_b64_tr_b16 a[132:133], v211 offset:4096\n"
    "ds_read_b64_tr_b16 a[134:135], v211 offset:6144\n"
    "v_mfma_f32_16x16x32_bf16 v[72:75], a[156:159], a[60:63], v[72:75]\n"
    "s_waitcnt lgkmcnt(12)\n"
    "ds_read_b64_tr_b16 a[136:137], v211 offset:8192\n"
    "ds_read_b64_tr_b16 a[138:139], v211 offset:10240\n"
    "v_mfma_f32_16x16x32_bf16 v[68:71], a[160:163], a[0:3], v[68:71]\n"
    "buffer_load_dwordx4 v242, s[12:15], s41 offen offset:1024 lds\n"
    "v_mfma_f32_16x16x32_bf16 v[68:71], a[164:167], a[8:11], v[68:71]\n"
    "ds_read_b64_tr_b16 a[140:141], v211 offset:12288\n"
    "s_mov_b32 s60, 0x0\n"
    "v_mfma_f32_16x16x32_bf16 v[68:71], a[168:171], a[16:19], v[68:71]\n"
    "ds_read_b64_tr_b16 a[142:143], v211 offset:14336\n"
    "ds_read_b64_tr_b16 a[144:145], v211 offset:16384\n"
    "v_mfma_f32_16x16x32_bf16 v[68:71], a[172:175], a[24:27], v[68:71]\n"
    "s_waitcnt lgkmcnt(9)\n"
    "buffer_load_dwordx4 v243, s[12:15], s41 offen offset:2048 lds\n"
    "v_mfma_f32_16x16x32_bf16 v[68:71], a[176:179], a[32:35], v[68:71]\n"
    "ds_read_b64_tr_b16 a[146:147], v211 offset:18432\n"
    "ds_read_b64_tr_b16 a[148:149], v211 offset:20480\n"
    "v_mfma_f32_16x16x32_bf16 v[68:71], a[180:183], a[40:43], v[68:71]\n"
    "ds_read_b64_tr_b16 a[150:151], v211 offset:22528\n"
    "ds_read_b64_tr_b16 a[152:153], v211 offset:24576\n"
    "v_mfma_f32_16x16x32_bf16 v[68:71], a[184:187], a[48:51], v[68:71]\n"
    "buffer_load_dwordx4 v244, s[12:15], s41 offen offset:3072 lds\n"
    "v_mfma_f32_16x16x32_bf16 v[68:71], a[188:191], a[56:59], v[68:71]\n"
    "ds_read_b64_tr_b16 a[154:155], v211 offset:26624\n"
    "s_add_u32 s41, s66, 0x200\n"
    "v_mfma_f32_16x16x32_bf16 v[76:79], a[160:163], a[4:7], v[76:79]\n"
    "ds_read_b64_tr_b16 a[156:157], v211 offset:28672\n"
    "s_add_u32 m0, s36, 0x0\n"
    "v_mfma_f32_16x16x32_bf16 v[76:79], a[164:167], a[12:15], v[76:79]\n"
    "buffer_load_dwordx4 v241, s[12:15], s41 offen offset:0 lds\n"
    "v_mfma_f32_16x16x32_bf16 v[76:79], a[168:171], a[20:23], v[76:79]\n"
    "ds_read_b64_tr_b16 a[158:159], v211 offset:30720\n"
    "ds_read_b64_tr_b16 a[160:161], v212 offset:0\n"
    "v_mfma_f32_16x16x32_bf16 v[76:79], a[172:175], a[28:31], v[76:79]\n"
    "ds_read_b64_tr_b16 a[162:163], v212 offset:2048\n"
    "ds_read_b64_tr_b16 a[164:165], v212 offset:4096\n"
    "v_mfma_f32_16x16x32_bf16 v[76:79], a[176:179], a[36:39], v[76:79]\n"
    "buffer_load_dwordx4 v242, s[12:15], s41 offen offset:1024 lds\n"
    "v_mfma_f32_16x16x32_bf16 v[76:79], a[180:183], a[44:47], v[76:79]\n"
    "ds_read_b64_tr_b16 a[166:167], v212 offset:6144\n"
    "ds_read_b64_tr_b16 a[168:169], v212 offset:8192\n"
    "v_mfma_f32_16x16x32_bf16 v[76:79], a[184:187], a[52:55], v[76:79]\n"
    "ds_read_b64_tr_b16 a[170:171], v212 offset:10240\n"
    "ds_read_b64_tr_b16 a[172:173], v212 offset:12288\n"
    "v_mfma_f32_16x16x32_bf16 v[76:79], a[188:191], a[60:63], v[76:79]\n"
    "s_waitcnt lgkmcnt(15)\n"
    "ds_read_b64_tr_b16 a[174:175], v212 offset:14336\n"
    "v_cvt_pk_bf16_f32 v0, v64, v65\n"
    "v_mfma_f32_16x16x32_bf16 v[80:83], a[128:131], a[0:3], v[80:83]\n"
    "ds_read_b64_tr_b16 a[176:177], v212 offset:16384\n"
    "v_cvt_pk_bf16_f32 v1, v66, v67\n"
    "v_mfma_f32_16x16x32_bf16 v[80:83], a[132:135], a[8:11], v[80:83]\n"
    "ds_read_b64_tr_b16 a[178:179], v212 offset:18432\n"
    "ds_read_b64_tr_b16 a[180:181], v212 offset:20480\n"
    "v_mfma_f32_16x16x32_bf16 v[80:83], a[136:139], a[16:19], v[80:83]\n"
    "buffer_load_dwordx4 v243, s[12:15], s41 offen offset:2048 lds\n"
    "v_mfma_f32_16x16x32_bf16 v[80:83], a[140:143], a[24:27], v[80:83]\n"
    "s_waitcnt lgkmcnt(11)\n"
    "ds_read_b64_tr_b16 a[182:183], v212 offset:22528\n"
    "ds_read_b64_tr_b16 a[184:185], v212 offset:24576\n"
    "v_mfma_f32_16x16x32_bf16 v[80:83], a[144:147], a[32:35], v[80:83]\n"
    "ds_read_b64_tr_b16 a[186:187], v212 offset:26624\n"
    "v_cvt_pk_bf16_f32 v2, v68, v69\n"
    "v_mfma_f32_16x16x32_bf16 v[80:83], a[148:151], a[40:43], v[80:83]\n"
    "buffer_load_dwordx4 v244, s[12:15], s41 offen offset:3072 lds\n"
    "v_mfma_f32_16x16x32_bf16 v[80:83], a[152:155], a[48:51], v[80:83]\n"
    "ds_read_b64_tr_b16 a[188:189], v212 offset:28672\n"
    "ds_read_b64_tr_b16 a[190:191], v212 offset:30720\n"
    "v_mfma_f32_16x16x32_bf16 v[80:83], a[156:159], a[56:59], v[80:83]\n"
    "v_cvt_pk_bf16_f32 v3, v70, v71\n"
    "v_cvt_pk_bf16_f32 v4, v72, v73\n"
    "v_mfma_f32_16x16x32_bf16 v[88:91], a[128:131], a[4:7], v[88:91]\n"
    "buffer_store_dwordx4 v[0:3], v205, s[80:83], s60 offen offset:0 sc1\n"
    "v_mfma_f32_16x16x32_bf16 v[88:91], a[132:135], a[12:15], v[88:91]\n"
    "s_waitcnt vmcnt(13)\n"
    "s_barrier\n"
    "v_mfma_f32_16x16x32_bf16 v[88:91], a[136:139], a[20:23], v[88:91]\n"
    "ds_read_b64_tr_b16 a[128:129], v211 offset:32768\n"
    "s_add_u32 s41, s66, 0x1c200\n"
    "v_mfma_f32_16x16x32_bf16 v[88:91], a[140:143], a[28:31], v[88:91]\n"
    "ds_read_b64_tr_b16 a[130:131], v211 offset:34816\n"
    "s_add_u32 m0, s36, 0x4000\n"
    "v_mfma_f32_16x16x32_bf16 v[88:91], a[144:147], a[36:39], v[88:91]\n"
    "buffer_load_dwordx4 v241, s[12:15], s41 offen offset:0 lds\n"
    "v_mfma_f32_16x16x32_bf16 v[88:91], a[148:151], a[44:47], v[88:91]\n"
    "ds_read_b64_tr_b16 a[132:133], v211 offset:36864\n"
    "ds_read_b64_tr_b16 a[134:135], v211 offset:38912\n"
    "v_mfma_f32_16x16x32_bf16 v[88:91], a[152:155], a[52:55], v[88:91]\n"
    "buffer_load_dwordx4 v242, s[12:15], s41 offen offset:1024 lds\n"
    "v_mfma_f32_16x16x32_bf16 v[88:91], a[156:159], a[60:63], v[88:91]\n"
    "s_waitcnt lgkmcnt(12)\n"
    "ds_read_b64_tr_b16 a[136:137], v211 offset:40960\n"
    "ds_read_b64_tr_b16 a[138:139], v211 offset:43008\n"
    "v_mfma_f32_16x16x32_bf16 v[84:87], a[160:163], a[0:3], v[84:87]\n"
    "ds_read_b64_tr_b16 a[140:141], v211 offset:45056\n"
    "s_mov_b32 s61, 0x2000\n"
    "v_mfma_f32_16x16x32_bf16 v[84:87], a[164:167], a[8:11], v[84:87]\n"
    "buffer_load_dwordx4 v243, s[12:15], s41 offen offset:2048 lds\n"
    "v_mfma_f32_16x16x32_bf16 v[84:87], a[168:171], a[16:19], v[84:87]\n"
    "ds_read_b64_tr_b16 a[142:143], v211 offset:47104\n"
    "ds_read_b64_tr_b16 a[144:145], v211 offset:49152\n"
    "v_mfma_f32_16x16x32_bf16 v[84:87], a[172:175], a[24:27], v[84:87]\n"
    "s_waitcnt lgkmcnt(9)\n"
    "ds_read_b64_tr_b16 a[146:147], v211 offset:51200\n"
    "ds_read_b64_tr_b16 a[148:149], v211 offset:53248\n"
    "v_mfma_f32_16x16x32_bf16 v[84:87], a[176:179], a[32:35], v[84:87]\n"
    "buffer_load_dwordx4 v244, s[12:15], s41 offen offset:3072 lds\n"
    "v_mfma_f32_16x16x32_bf16 v[84:87], a[180:183], a[40:43], v[84:87]\n"
    "ds_read_b64_tr_b16 a[150:151], v211 offset:55296\n"
    "s_add_u32 s41, s66, 0x280\n"
    "v_mfma_f32_16x16x32_bf16 v[84:87], a[184:187], a[48:51], v[84:87]\n"
    "ds_read_b64_tr_b16 a[152:153], v211 offset:57344\n"
    "s_add_u32 m0, s36, 0x8000\n"
    "v_mfma_f32_16x16x32_bf16 v[84:87], a[188:191], a[56:59], v[84:87]\n"
    "buffer_load_dwordx4 v241, s[12:15], s41 offen offset:0 lds\n"
    "v_mfma_f32_16x16x32_bf16 v[92:95], a[160:163], a[4:7], v[92:95]\n"
    "ds_read_b64_tr_b16 a[154:155], v211 offset:59392\n"
    "ds_read_b64_tr_b16 a[156:157], v211 offset:61440\n"
    "v_mfma_f32_16x16x32_bf16 v[92:95], a[164:167], a[12:15], v[92:95]\n"
    "ds_read_b64_tr_b16 a[158:159], v211 offset:63488\n"
    "ds_read_b64_tr_b16 a[160:161], v212 offset:32768\n"
    "v_mfma_f32_16x16x32_bf16 v[92:95], a[168:171], a[20:23], v[92:95]\n"
    "buffer_load_dwordx4 v242, s[12:15], s41 offen offset:1024 lds\n"
    "v_mfma_f32_16x16x32_bf16 v[92:95], a[172:175], a[28:31], v[92:95]\n"
    "ds_read_b64_tr_b16 a[162:163], v212 offset:34816\n"
    "ds_read_b64_tr_b16 a[164:165], v212 offset:36864\n"
    "v_mfma_f32_16x16x32_bf16 v[92:95], a[176:179], a[36:39], v[92:95]\n"
    "ds_read_b64_tr_b16 a[166:167], v212 offset:38912\n"
    "ds_read_b64_tr_b16 a[168:169], v212 offset:40960\n"
    "v_mfma_f32_16x16x32_bf16 v[92:95], a[180:183], a[44:47], v[92:95]\n"
    "buffer_load_dwordx4 v243, s[12:15], s41 offen offset:2048 lds\n"
    "v_mfma_f32_16x16x32_bf16 v[92:95], a[184:187], a[52:55], v[92:95]\n"
    "ds_read_b64_tr_b16 a[170:171], v212 offset:43008\n"
    "ds_read_b64_tr_b16 a[172:173], v212 offset:45056\n"
    "v_mfma_f32_16x16x32_bf16 v[92:95], a[188:191], a[60:63], v[92:95]\n"
    "s_waitcnt lgkmcnt(15)\n"
    "ds_read_b64_tr_b16 a[174:175], v212 offset:47104\n"
    "ds_read_b64_tr_b16 a[176:177], v212 offset:49152\n"
    "v_mfma_f32_16x16x32_bf16 v[96:99], a[128:131], a[0:3], v[96:99]\n"
    "ds_read_b64_tr_b16 a[178:179], v212 offset:51200\n"
    "v_cvt_pk_bf16_f32 v5, v74, v75\n"
    "v_mfma_f32_16x16x32_bf16 v[96:99], a[132:135], a[8:11], v[96:99]\n"
    "ds_read_b64_tr_b16 a[180:181], v212 offset:53248\n"
    "v_cvt_pk_bf16_f32 v6, v76, v77\n"
    "v_mfma_f32_16x16x32_bf16 v[96:99], a[136:139], a[16:19], v[96:99]\n"
    "ds_read_b64_tr_b16 a[182:183], v212 offset:55296\n"
    "v_cvt_pk_bf16_f32 v7, v78, v79\n"
    "v_mfma_f32_16x16x32_bf16 v[96:99], a[140:143], a[24:27], v[96:99]\n"
    "s_waitcnt lgkmcnt(12)\n"
    "buffer_store_dwordx4 v[4:7], v206, s[80:83], s60 offen offset:2048 sc1\n"
    "v_mfma_f32_16x16x32_bf16 v[96:99], a[144:147], a[32:35], v[96:99]\n"
    "buffer_load_dwordx4 v244, s[12:15], s41 offen offset:3072 lds\n"
    "v_mfma_f32_16x16x32_bf16 v[96:99], a[148:151], a[40:43], v[96:99]\n"
    "ds_read_b64_tr_b16 a[184:185], v212 offset:57344\n"
    "s_add_u32 s41, s66, 0x1c280\n"
    "v_mfma_f32_16x16x32_bf16 v[96:99], a[152:155], a[48:51], v[96:99]\n"
    "ds_read_b64_tr_b16 a[186:187], v212 offset:59392\n"
    "s_add_u32 m0, s36, 0xc000\n"
    "v_mfma_f32_16x16x32_bf16 v[96:99], a[156:159], a[56:59], v[96:99]\n"
    "buffer_load_dwordx4 v241, s[12:15], s41 offen offset:0 lds\n"
    "v_mfma_f32_16x16x32_bf16 v[104:107], a[128:131], a[4:7], v[104:107]\n"
    "ds_read_b64_tr_b16 a[188:189], v212 offset:61440\n"
    "ds_read_b64_tr_b16 a[190:191], v212 offset:63488\n"
    "v_mfma_f32_16x16x32_bf16 v[104:107], a[132:135], a[12:15], v[104:107]\n"
    "s_waitcnt vmcnt(15)\n"
    "s_barrier\n"
    "v_mfma_f32_16x16x32_bf16 v[104:107], a[136:139], a[20:23], v[104:107]\n"
    "ds_read_b64_tr_b16 a[128:129], v213 offset:0\n"
    "v_cvt_pk_bf16_f32 v8, v80, v81\n"
    "v_mfma_f32_16x16x32_bf16 v[104:107], a[140:143], a[28:31], v[104:107]\n"
    "ds_read_b64_tr_b16 a[130:131], v213 offset:2048\n"
    "v_cvt_pk_bf16_f32 v9, v82, v83\n"
    "v_mfma_f32_16x16x32_bf16 v[104:107], a[144:147], a[36:39], v[104:107]\n"
    "ds_read_b64_tr_b16 a[132:133], v213 offset:4096\n"
    "ds_read_b64_tr_b16 a[134:135], v213 offset:6144\n"
    "v_mfma_f32_16x16x32_bf16 v[104:107], a[148:151], a[44:47], v[104:107]\n"
    "buffer_load_dwordx4 v242, s[12:15], s41 offen offset:1024 lds\n"
    "v_mfma_f32_16x16x32_bf16 v[104:107], a[152:155], a[52:55], v[104:107]\n"
    "ds_read_b64_tr_b16 a[136:137], v213 offset:8192\n"
    "ds_read_b64_tr_b16 a[138:139], v213 offset:10240\n"
    "v_mfma_f32_16x16x32_bf16 v[104:107], a[156:159], a[60:63], v[104:107]\n"
    "s_waitcnt lgkmcnt(14)\n"
    "ds_read_b64_tr_b16 a[140:141], v213 offset:12288\n"
    "ds_read_b64_tr_b16 a[142:143], v213 offset:14336\n"
    "v_mfma_f32_16x16x32_bf16 v[100:103], a[160:163], a[0:3], v[100:103]\n"
    "buffer_load_dwordx4 v243, s[12:15], s41 offen offset:2048 lds\n"
    "v_mfma_f32_16x16x32_bf16 v[100:103], a[164:167], a[8:11], v[100:103]\n"
    "ds_read_b64_tr_b16 a[144:145], v213 offset:16384\n"
    "s_mov_b32 s60, 0x4000\n"
    "v_mfma_f32_16x16x32_bf16 v[100:103], a[168:171], a[16:19], v[100:103]\n"
    "ds_read_b64_tr_b16 a[146:147], v213 offset:18432\n"
    "ds_read_b64_tr_b16 a[148:149], v213 offset:20480\n"
    "v_mfma_f32_16x16x32_bf16 v[100:103], a[172:175], a[24:27], v[100:103]\n"
    "s_waitcnt lgkmcnt(11)\n"
    "buffer_load_dwordx4 v244, s[12:15], s41 offen offset:3072 lds\n"
    "v_mfma_f32_16x16x32_bf16 v[100:103], a[176:179], a[32:35], v[100:103]\n"
    "ds_read_b64_tr_b16 a[150:151], v213 offset:22528\n"
    "s_add_u32 s41, s66, 0x300\n"
    "v_mfma_f32_16x16x32_bf16 v[100:103], a[180:183], a[40:43], v[100:103]\n"
    "ds_read_b64_tr_b16 a[152:153], v213 offset:24576\n"
    "s_add_u32 m0, s36, 0x10000\n"
    "v_mfma_f32_16x16x32_bf16 v[100:103], a[184:187], a[48:51], v[100:103]\n"
    "buffer_load_dwordx4 v241, s[12:15], s41 offen offset:0 lds\n"
    "v_mfma_f32_16x16x32_bf16 v[100:103], a[188:191], a[56:59], v[100:103]\n"
    "ds_read_b64_tr_b16 a[154:155], v213 offset:26624\n"
    "ds_read_b64_tr_b16 a[156:157], v213 offset:28672\n"
    "v_mfma_f32_16x16x32_bf16 v[108:111], a[160:163], a[4:7], v[108:111]\n"
    "ds_read_b64_tr_b16 a[158:159], v213 offset:30720\n"
    "ds_read_b64_tr_b16 a[160:161], v214 offset:0\n"
    "v_mfma_f32_16x16x32_bf16 v[108:111], a[164:167], a[12:15], v[108:111]\n"
    "buffer_load_dwordx4 v242, s[12:15], s41 offen offset:1024 lds\n"
    "v_mfma_f32_16x16x32_bf16 v[108:111], a[168:171], a[20:23], v[108:111]\n"
    "ds_read_b64_tr_b16 a[162:163], v214 offset:2048\n"
    "ds_read_b64_tr_b16 a[164:165], v214 offset:4096\n"
    "v_mfma_f32_16x16x32_bf16 v[108:111], a[172:175], a[28:31], v[108:111]\n"
    "ds_read_b64_tr_b16 a[166:167], v214 offset:6144\n"
    "v_cvt_pk_bf16_f32 v10, v84, v85\n"
    "v_mfma_f32_16x16x32_bf16 v[108:111], a[176:179], a[36:39], v[108:111]\n"
    "buffer_load_dwordx4 v243, s[12:15], s41 offen offset:2048 lds\n"
    "v_mfma_f32_16x16x32_bf16 v[108:111], a[180:183], a[44:47], v[108:111]\n"
    "ds_read_b64_tr_b16 a[168:169], v214 offset:8192\n"
    "v_cvt_pk_bf16_f32 v11, v86, v87\n"
    "v_mfma_f32_16x16x32_bf16 v[108:111], a[184:187], a[52:55], v[108:111]\n"
    "buffer_store_dwordx4 v[8:11], v205, s[80:83], s61 offen offset:0 sc1\n"
    "v_mfma_f32_16x16x32_bf16 v[108:111], a[188:191], a[60:63], v[108:111]\n"
    "s_waitcnt lgkmcnt(13)\n"
    "ds_read_b64_tr_b16 a[170:171], v214 offset:10240\n"
    "v_cvt_pk_bf16_f32 v12, v88, v89\n"
    "v_mfma_f32_16x16x32_bf16 v[112:115], a[128:131], a[0:3], v[112:115]\n"
    "ds_read_b64_tr_b16 a[172:173], v214 offset:12288\n"
    "v_cvt_pk_bf16_f32 v13, v90, v91\n"
    "v_mfma_f32_16x16x32_bf16 v[112:115], a[132:135], a[8:11], v[112:115]\n"
    "ds_read_b64_tr_b16 a[174:175], v214 offset:14336\n"
    "v_cvt_pk_bf16_f32 v14, v92, v93\n"
    "v_mfma_f32_16x16x32_bf16 v[112:115], a[136:139], a[16:19], v[112:115]\n"
    "buffer_load_dwordx4 v244, s[12:15], s41 offen offset:3072 lds\n"
    "v_mfma_f32_16x16x32_bf16 v[112:115], a[140:143], a[24:27], v[112:115]\n"
    "s_waitcnt lgkmcnt(8)\n"
    "ds_read_b64_tr_b16 a[176:177], v214 offset:16384\n"
    "s_add_u32 s41, s66, 0x1c300\n"
    "v_mfma_f32_16x16x32_bf16 v[112:115], a[144:147], a[32:35], v[112:115]\n"
    "ds_read_b64_tr_b16 a[178:179], v214 offset:18432\n"
    "s_add_u32 m0, s36, 0x14000\n"
    "v_mfma_f32_16x16x32_bf16 v[112:115], a[148:151], a[40:43], v[112:115]\n"
    "buffer_load_dwordx4 v241, s[12:15], s41 offen offset:0 lds\n"
    "v_mfma_f32_16x16x32_bf16 v[112:115], a[152:155], a[48:51], v[112:115]\n"
    "ds_read_b64_tr_b16 a[180:181], v214 offset:20480\n"
    "v_cvt_pk_bf16_f32 v15, v94, v95\n"
    "v_mfma_f32_16x16x32_bf16 v[112:115], a[156:159], a[56:59], v[112:115]\n"
    "buffer_store_dwordx4 v[12:15], v206, s[80:83], s61 offen offset:2048 sc1\n"
    "v_mfma_f32_16x16x32_bf16 v[120:123], a[128:131], a[4:7], v[120:123]\n"
    "ds_read_b64_tr_b16 a[182:183], v214 offset:22528\n"
    "ds_read_b64_tr_b16 a[184:185], v214 offset:24576\n"
    "v_mfma_f32_16x16x32_bf16 v[120:123], a[132:135], a[12:15], v[120:123]\n"
    "s_waitcnt vmcnt(16)\n"
    "s_barrier\n"
    "v_mfma_f32_16x16x32_bf16 v[120:123], a[136:139], a[20:23], v[120:123]\n"
    "ds_read_b64_tr_b16 a[186:187], v214 offset:26624\n"
    "ds_read_b64_tr_b16 a[188:189], v214 offset:28672\n"
    "v_mfma_f32_16x16x32_bf16 v[120:123], a[140:143], a[28:31], v[120:123]\n"
    "buffer_load_dwordx4 v242, s[12:15], s41 offen offset:1024 lds\n"
    "v_mfma_f32_16x16x32_bf16 v[120:123], a[144:147], a[36:39], v[120:123]\n"
    "ds_read_b64_tr_b16 a[190:191], v214 offset:30720\n"
    "v_cvt_pk_bf16_f32 v16, v96, v97\n"
    "v_mfma_f32_16x16x32_bf16 v[120:123], a[148:151], a[44:47], v[120:123]\n"
    "ds_read_b64_tr_b16 a[128:129], v209 offset:0\n"
    "v_cvt_pk_bf16_f32 v17, v98, v99\n"
    "v_mfma_f32_16x16x32_bf16 v[120:123], a[152:155], a[52:55], v[120:123]\n"
    "buffer_load_dwordx4 v243, s[12:15], s41 offen offset:2048 lds\n"
    "v_mfma_f32_16x16x32_bf16 v[120:123], a[156:159], a[60:63], v[120:123]\n"
    "s_waitcnt lgkmcnt(9)\n"
    "ds_read_b64_tr_b16 a[130:131], v209 offset:2048\n"
    "v_cvt_pk_bf16_f32 v18, v100, v101\n"
    "v_mfma_f32_16x16x32_bf16 v[116:119], a[160:163], a[0:3], v[116:119]\n"
    "ds_read_b64_tr_b16 a[132:133], v209 offset:4096\n"
    "s_mov_b32 s61, 0x6000\n"
    "v_mfma_f32_16x16x32_bf16 v[116:119], a[164:167], a[8:11], v[116:119]\n"
    "buffer_load_dwordx4 v244, s[12:15], s41 offen offset:3072 lds\n"
    "v_mfma_f32_16x16x32_bf16 v[116:119], a[168:171], a[16:19], v[116:119]\n"
    "ds_read_b64_tr_b16 a[134:135], v209 offset:6144\n"
    "v_cvt_pk_bf16_f32 v19, v102, v103\n"
    "v_mfma_f32_16x16x32_bf16 v[116:119], a[172:175], a[24:27], v[116:119]\n"
    "s_waitcnt lgkmcnt(4)\n"
    "buffer_store_dwordx4 v[16:19], v205, s[80:83], s60 offen offset:0 sc1\n"
    "v_mfma_f32_16x16x32_bf16 v[116:119], a[176:179], a[32:35], v[116:119]\n"
    "ds_read_b64_tr_b16 a[136:137], v209 offset:8192\n"
    "ds_read_b64_tr_b16 a[138:139], v209 offset:10240\n"
    "v_mfma_f32_16x16x32_bf16 v[116:119], a[180:183], a[40:43], v[116:119]\n"
    "ds_read_b64_tr_b16 a[140:141], v209 offset:12288\n"
    "v_cvt_pk_bf16_f32 v20, v104, v105\n"
    "v_mfma_f32_16x16x32_bf16 v[116:119], a[184:187], a[48:51], v[116:119]\n"
    "ds_read_b64_tr_b16 a[142:143], v209 offset:14336\n"
    "v_cvt_pk_bf16_f32 v21, v106, v107\n"
    "v_mfma_f32_16x16x32_bf16 v[116:119], a[188:191], a[56:59], v[116:119]\n"
    "ds_read_b64_tr_b16 a[144:145], v209 offset:16384\n"
    "v_cvt_pk_bf16_f32 v22, v108, v109\n"
    "v_mfma_f32_16x16x32_bf16 v[124:127], a[160:163], a[4:7], v[124:127]\n"
    "ds_read_b64_tr_b16 a[146:147], v209 offset:18432\n"
    "v_cvt_pk_bf16_f32 v23, v110, v111\n"
    "v_mfma_f32_16x16x32_bf16 v[124:127], a[164:167], a[12:15], v[124:127]\n"
    "buffer_store_dwordx4 v[20:23], v206, s[80:83], s60 offen offset:2048 sc1\n"
    "v_mfma_f32_16x16x32_bf16 v[124:127], a[168:171], a[20:23], v[124:127]\n"
    "ds_read_b64_tr_b16 a[148:149], v209 offset:20480\n"
    "ds_read_b64_tr_b16 a[150:151], v209 offset:22528\n"
    "v_mfma_f32_16x16x32_bf16 v[124:127], a[172:175], a[28:31], v[124:127]\n"
    "ds_read_b64_tr_b16 a[152:153], v209 offset:24576\n"
    "v_cvt_pk_bf16_f32 v24, v112, v113\n"
    "v_mfma_f32_16x16x32_bf16 v[124:127], a[176:179], a[36:39], v[124:127]\n"
    "ds_read_b64_tr_b16 a[154:155], v209 offset:26624\n"
    "v_cvt_pk_bf16_f32 v25, v114, v115\n"
    "v_mfma_f32_16x16x32_bf16 v[124:127], a[180:183], a[44:47], v[124:127]\n"
    "ds_read_b64_tr_b16 a[156:157], v209 offset:28672\n"
    "v_cvt_pk_bf16_f32 v26, v116, v117\n"
    "v_mfma_f32_16x16x32_bf16 v[124:127], a[184:187], a[52:55], v[124:127]\n"
    "ds_read_b64_tr_b16 a[158:159], v209 offset:30720\n"
    "ds_read_b64_tr_b16 a[160:161], v210 offset:0\n"
    "v_mfma_f32_16x16x32_bf16 v[124:127], a[188:191], a[60:63], v[124:127]\n"
    "s_waitcnt lgkmcnt(9)\n"
    "ds_read_b64_tr_b16 a[162:163], v210 offset:2048\n"
    "v_cvt_pk_bf16_f32 v27, v118, v119\n"
    "v_mfma_f32_16x16x32_bf16 v[128:131], a[128:131], a[0:3], v[128:131]\n"
    "buffer_store_dwordx4 v[24:27], v205, s[80:83], s61 offen offset:0 sc1\n"
    "v_mfma_f32_16x16x32_bf16 v[128:131], a[132:135], a[8:11], v[128:131]\n"
    "ds_read_b64_tr_b16 a[164:165], v210 offset:4096\n"
    "v_cvt_pk_bf16_f32 v28, v120, v121\n"
    "v_mfma_f32_16x16x32_bf16 v[128:131], a[136:139], a[16:19], v[128:131]\n"
    "ds_read_b64_tr_b16 a[166:167], v210 offset:6144\n"
    "v_cvt_pk_bf16_f32 v29, v122, v123\n"
    "v_mfma_f32_16x16x32_bf16 v[128:131], a[140:143], a[24:27], v[128:131]\n"
    "s_waitcnt lgkmcnt(4)\n"
    "ds_read_b64_tr_b16 a[168:169], v210 offset:8192\n"
    "v_accvgpr_write_b32 a192, v0\n"
    "v_mfma_f32_16x16x32_bf16 v[128:131], a[144:147], a[32:35], v[128:131]\n"
    "ds_read_b64_tr_b16 a[170:171], v210 offset:10240\n"
    "v_cvt_pk_bf16_f32 v30, v124, v125\n"
    "v_mfma_f32_16x16x32_bf16 v[128:131], a[148:151], a[40:43], v[128:131]\n"
    "ds_read_b64_tr_b16 a[172:173], v210 offset:12288\n"
    "v_cvt_pk_bf16_f32 v31, v126, v127\n"
    "v_mfma_f32_16x16x32_bf16 v[128:131], a[152:155], a[48:51], v[128:131]\n"
    "ds_read_b64_tr_b16 a[174:175], v210 offset:14336\n"
    "ds_read_b64_tr_b16 a[176:177], v210 offset:16384\n"
    "v_mfma_f32_16x16x32_bf16 v[128:131], a[156:159], a[56:59], v[128:131]\n"
    "buffer_store_dwordx4 v[28:31], v206, s[80:83], s61 offen offset:2048 sc1\n"
    "v_mfma_f32_16x16x32_bf16 v[136:139], a[128:131], a[4:7], v[136:139]\n"
    "ds_read_b64_tr_b16 a[178:179], v210 offset:18432\n"
    "ds_read_b64_tr_b16 a[180:181], v210 offset:20480\n"
    "v_mfma_f32_16x16x32_bf16 v[136:139], a[132:135], a[12:15], v[136:139]\n"
    "s_waitcnt vmcnt(14)\n"
    "s_barrier\n"
    "v_mfma_f32_16x16x32_bf16 v[136:139], a[136:139], a[20:23], v[136:139]\n"
    "ds_read_b64_tr_b16 a[182:183], v210 offset:22528\n"
    "v_accvgpr_write_b32 a193, v1\n"
    "v_mfma_f32_16x16x32_bf16 v[136:139], a[140:143], a[28:31], v[136:139]\n"
    "ds_read_b64_tr_b16 a[184:185], v210 offset:24576\n"
    "v_accvgpr_write_b32 a194, v2\n"
    "v_mfma_f32_16x16x32_bf16 v[136:139], a[144:147], a[36:39], v[136:139]\n"
    "ds_read_b64_tr_b16 a[186:187], v210 offset:26624\n"
    "v_accvgpr_write_b32 a195, v3\n"
    "v_mfma_f32_16x16x32_bf16 v[136:139], a[148:151], a[44:47], v[136:139]\n"
    "ds_read_b64_tr_b16 a[188:189], v210 offset:28672\n"
    "v_cvt_pk_bf16_f32 v0, v128, v129\n"
    "v_mfma_f32_16x16x32_bf16 v[136:139], a[152:155], a[52:55], v[136:139]\n"
    "ds_read_b64_tr_b16 a[190:191], v210 offset:30720\n"
    "v_cvt_pk_bf16_f32 v1, v130, v131\n"
    "v_mfma_f32_16x16x32_bf16 v[136:139], a[156:159], a[60:63], v[136:139]\n"
    "s_waitcnt lgkmcnt(8)\n"
    "ds_read_b64_tr_b16 a[128:129], v209 offset:32768\n"
    "v_accvgpr_write_b32 a196, v4\n"
    "v_mfma_f32_16x16x32_bf16 v[132:135], a[160:163], a[0:3], v[132:135]\n"
    "ds_read_b64_tr_b16 a[130:131], v209 offset:34816\n"
    "s_mov_b32 s60, 0x8000\n"
    "v_mfma_f32_16x16x32_bf16 v[132:135], a[164:167], a[8:11], v[132:135]\n"
    "ds_read_b64_tr_b16 a[132:133], v209 offset:36864\n"
    "v_accvgpr_write_b32 a197, v5\n"
    "v_mfma_f32_16x16x32_bf16 v[132:135], a[168:171], a[16:19], v[132:135]\n"
    "ds_read_b64_tr_b16 a[134:135], v209 offset:38912\n"
    "v_accvgpr_write_b32 a198, v6\n"
    "v_mfma_f32_16x16x32_bf16 v[132:135], a[172:175], a[24:27], v[132:135]\n"
    "s_waitcnt lgkmcnt(4)\n"
    "ds_read_b64_tr_b16 a[136:137], v209 offset:40960\n"
    "v_accvgpr_write_b32 a199, v7\n"
    "v_mfma_f32_16x16x32_bf16 v[132:135], a[176:179], a[32:35], v[132:135]\n"
    "ds_read_b64_tr_b16 a[138:139], v209 offset:43008\n"
    "v_cvt_pk_bf16_f32 v4, v136, v137\n"
    "v_mfma_f32_16x16x32_bf16 v[132:135], a[180:183], a[40:43], v[132:135]\n"
    "ds_read_b64_tr_b16 a[140:141], v209 offset:45056\n"
    "v_cvt_pk_bf16_f32 v5, v138, v139\n"
    "v_mfma_f32_16x16x32_bf16 v[132:135], a[184:187], a[48:51], v[132:135]\n"
    "ds_read_b64_tr_b16 a[142:143], v209 offset:47104\n"
    "v_accvgpr_write_b32 a200, v8\n"
    "v_mfma_f32_16x16x32_bf16 v[132:135], a[188:191], a[56:59], v[132:135]\n"
    "ds_read_b64_tr_b16 a[144:145], v209 offset:49152\n"
    "v_accvgpr_write_b32 a201, v9\n"
    "v_mfma_f32_16x16x32_bf16 v[140:143], a[160:163], a[4:7], v[140:143]\n"
    "ds_read_b64_tr_b16 a[146:147], v209 offset:51200\n"
    "v_accvgpr_write_b32 a202, v10\n"
    "v_mfma_f32_16x16x32_bf16 v[140:143], a[164:167], a[12:15], v[140:143]\n"
    "ds_read_b64_tr_b16 a[148:149], v209 offset:53248\n"
    "v_accvgpr_write_b32 a203, v11\n"
    "v_mfma_f32_16x16x32_bf16 v[140:143], a[168:171], a[20:23], v[140:143]\n"
    "ds_read_b64_tr_b16 a[150:151], v209 offset:55296\n"
    "v_accvgpr_write_b32 a204, v12\n"
    "v_mfma_f32_16x16x32_bf16 v[140:143], a[172:175], a[28:31], v[140:143]\n"
    "ds_read_b64_tr_b16 a[152:153], v209 offset:57344\n"
    "v_accvgpr_write_b32 a205, v13\n"
    "v_mfma_f32_16x16x32_bf16 v[140:143], a[176:179], a[36:39], v[140:143]\n"
    "ds_read_b64_tr_b16 a[154:155], v209 offset:59392\n"
    "v_cvt_pk_bf16_f32 v2, v132, v133\n"
    "v_mfma_f32_16x16x32_bf16 v[140:143], a[180:183], a[44:47], v[140:143]\n"
    "ds_read_b64_tr_b16 a[156:157], v209 offset:61440\n"
    "v_cvt_pk_bf16_f32 v3, v134, v135\n"
    "v_mfma_f32_16x16x32_bf16 v[140:143], a[184:187], a[52:55], v[140:143]\n"
    "ds_read_b64_tr_b16 a[158:159], v209 offset:63488\n"
    "ds_read_b64_tr_b16 a[160:161], v210 offset:32768\n"
    "v_mfma_f32_16x16x32_bf16 v[140:143], a[188:191], a[60:63], v[140:143]\n"
    "s_waitcnt lgkmcnt(9)\n"
    "buffer_store_dwordx4 v[0:3], v205, s[80:83], s60 offen offset:0 sc1\n"
    "v_mfma_f32_16x16x32_bf16 v[144:147], a[128:131], a[0:3], v[144:147]\n"
    "ds_read_b64_tr_b16 a[162:163], v210 offset:34816\n"
    "ds_read_b64_tr_b16 a[164:165], v210 offset:36864\n"
    "v_mfma_f32_16x16x32_bf16 v[144:147], a[132:135], a[8:11], v[144:147]\n"
    "ds_read_b64_tr_b16 a[166:167], v210 offset:38912\n"
    "v_accvgpr_write_b32 a206, v14\n"
    "v_mfma_f32_16x16x32_bf16 v[144:147], a[136:139], a[16:19], v[144:147]\n"
    "ds_read_b64_tr_b16 a[168:169], v210 offset:40960\n"
    "v_accvgpr_write_b32 a207, v15\n"
    "v_mfma_f32_16x16x32_bf16 v[144:147], a[140:143], a[24:27], v[144:147]\n"
    "s_waitcnt lgkmcnt(5)\n"
    "ds_read_b64_tr_b16 a[170:171], v210 offset:43008\n"
    "v_accvgpr_write_b32 a208, v16\n"
    "v_mfma_f32_16x16x32_bf16 v[144:147], a[144:147], a[32:35], v[144:147]\n"
    "ds_read_b64_tr_b16 a[172:173], v210 offset:45056\n"
    "v_cvt_pk_bf16_f32 v6, v140, v141\n"
    "v_mfma_f32_16x16x32_bf16 v[144:147], a[148:151], a[40:43], v[144:147]\n"
    "ds_read_b64_tr_b16 a[174:175], v210 offset:47104\n"
    "v_cvt_pk_bf16_f32 v7, v142, v143\n"
    "v_mfma_f32_16x16x32_bf16 v[144:147], a[152:155], a[48:51], v[144:147]\n"
    "ds_read_b64_tr_b16 a[176:177], v210 offset:49152\n"
    "v_accvgpr_write_b32 a209, v17\n"
    "v_mfma_f32_16x16x32_bf16 v[144:147], a[156:159], a[56:59], v[144:147]\n"
    "buffer_store_dwordx4 v[4:7], v206, s[80:83], s60 offen offset:2048 sc1\n"
    "v_mfma_f32_16x16x32_bf16 v[152:155], a[128:131], a[4:7], v[152:155]\n"
    "ds_read_b64_tr_b16 a[178:179], v210 offset:51200\n"
    "ds_read_b64_tr_b16 a[180:181], v210 offset:53248\n"
    "v_mfma_f32_16x16x32_bf16 v[152:155], a[132:135], a[12:15], v[152:155]\n"
    "s_waitcnt vmcnt(4)\n"
    "s_barrier\n"
    "v_mfma_f32_16x16x32_bf16 v[152:155], a[136:139], a[20:23], v[152:155]\n"
    "buffer_store_dword v226, v215, s[52:55], 0 offen offset:0 sc1\n"
    "v_mfma_f32_16x16x32_bf16 v[152:155], a[140:143], a[28:31], v[152:155]\n"
    "ds_read_b64_tr_b16 a[182:183], v210 offset:55296\n"
    "ds_read_b64_tr_b16 a[184:185], v210 offset:57344\n"
    "v_mfma_f32_16x16x32_bf16 v[152:155], a[144:147], a[36:39], v[152:155]\n"
    "ds_read_b64_tr_b16 a[186:187], v210 offset:59392\n"
    "v_cvt_pk_bf16_f32 v8, v144, v145\n"
    "v_mfma_f32_16x16x32_bf16 v[152:155], a[148:151], a[44:47], v[152:155]\n"
    "ds_read_b64_tr_b16 a[188:189], v210 offset:61440\n"
    "v_cvt_pk_bf16_f32 v9, v146, v147\n"
    "v_mfma_f32_16x16x32_bf16 v[152:155], a[152:155], a[52:55], v[152:155]\n"
    "ds_read_b64_tr_b16 a[190:191], v210 offset:63488\n"
    "v_accvgpr_write_b32 a210, v18\n"
    "v_mfma_f32_16x16x32_bf16 v[152:155], a[156:159], a[60:63], v[152:155]\n"
    "s_waitcnt lgkmcnt(8)\n"
    "ds_read_b64_tr_b16 a[128:129], v211 offset:0\n"
    "v_accvgpr_write_b32 a211, v19\n"
    "v_mfma_f32_16x16x32_bf16 v[148:151], a[160:163], a[0:3], v[148:151]\n"
    "ds_read_b64_tr_b16 a[130:131], v211 offset:2048\n"
    "s_mov_b32 s61, 0xa000\n"
    "v_mfma_f32_16x16x32_bf16 v[148:151], a[164:167], a[8:11], v[148:151]\n"
    "ds_read_b64_tr_b16 a[132:133], v211 offset:4096\n"
    "v_accvgpr_write_b32 a212, v20\n"
    "v_mfma_f32_16x16x32_bf16 v[148:151], a[168:171], a[16:19], v[148:151]\n"
    "ds_read_b64_tr_b16 a[134:135], v211 offset:6144\n"
    "v_accvgpr_write_b32 a213, v21\n"
    "v_mfma_f32_16x16x32_bf16 v[148:151], a[172:175], a[24:27], v[148:151]\n"
    "s_waitcnt lgkmcnt(4)\n"
    "ds_read_b64_tr_b16 a[136:137], v211 offset:8192\n"
    "v_accvgpr_write_b32 a214, v22\n"
    "v_mfma_f32_16x16x32_bf16 v[148:151], a[176:179], a[32:35], v[148:151]\n"
    "ds_read_b64_tr_b16 a[138:139], v211 offset:10240\n"
    "v_cvt_pk_bf16_f32 v12, v152, v153\n"
    "v_mfma_f32_16x16x32_bf16 v[148:151], a[180:183], a[40:43], v[148:151]\n"
    "ds_read_b64_tr_b16 a[140:141], v211 offset:12288\n"
    "v_cvt_pk_bf16_f32 v13, v154, v155\n"
    "v_mfma_f32_16x16x32_bf16 v[148:151], a[184:187], a[48:51], v[148:151]\n"
    "ds_read_b64_tr_b16 a[142:143], v211 offset:14336\n"
    "v_accvgpr_write_b32 a215, v23\n"
    "v_mfma_f32_16x16x32_bf16 v[148:151], a[188:191], a[56:59], v[148:151]\n"
    "ds_read_b64_tr_b16 a[144:145], v211 offset:16384\n"
    "v_accvgpr_write_b32 a216, v24\n"
    "v_mfma_f32_16x16x32_bf16 v[156:159], a[160:163], a[4:7], v[156:159]\n"
    "ds_read_b64_tr_b16 a[146:147], v211 offset:18432\n"
    "v_accvgpr_write_b32 a217, v25\n"
    "v_mfma_f32_16x16x32_bf16 v[156:159], a[164:167], a[12:15], v[156:159]\n"
    "ds_read_b64_tr_b16 a[148:149], v211 offset:20480\n"
    "v_accvgpr_write_b32 a218, v26\n"
    "v_mfma_f32_16x16x32_bf16 v[156:159], a[168:171], a[20:23], v[156:159]\n"
    "ds_read_b64_tr_b16 a[150:151], v211 offset:22528\n"
    "v_accvgpr_write_b32 a219, v27\n"
    "v_mfma_f32_16x16x32_bf16 v[156:159], a[172:175], a[28:31], v[156:159]\n"
    "ds_read_b64_tr_b16 a[152:153], v211 offset:24576\n"
    "v_accvgpr_write_b32 a220, v28\n"
    "v_mfma_f32_16x16x32_bf16 v[156:159], a[176:179], a[36:39], v[156:159]\n"
    "ds_read_b64_tr_b16 a[154:155], v211 offset:26624\n"
    "v_cvt_pk_bf16_f32 v10, v148, v149\n"
    "v_mfma_f32_16x16x32_bf16 v[156:159], a[180:183], a[44:47], v[156:159]\n"
    "ds_read_b64_tr_b16 a[156:157], v211 offset:28672\n"
    "v_cvt_pk_bf16_f32 v11, v150, v151\n"
    "v_mfma_f32_16x16x32_bf16 v[156:159], a[184:187], a[52:55], v[156:159]\n"
    "ds_read_b64_tr_b16 a[158:159], v211 offset:30720\n"
    "v_accvgpr_write_b32 a221, v29\n"
    "v_mfma_f32_16x16x32_bf16 v[156:159], a[188:191], a[60:63], v[156:159]\n"
    "s_waitcnt lgkmcnt(8)\n"
    "buffer_store_dwordx4 v[8:11], v205, s[80:83], s61 offen offset:0 sc1\n"
    "v_mfma_f32_16x16x32_bf16 v[160:163], a[128:131], a[0:3], v[160:163]\n"
    "ds_read_b64_tr_b16 a[160:161], v212 offset:0\n"
    "v_accvgpr_write_b32 a222, v30\n"
    "v_mfma_f32_16x16x32_bf16 v[160:163], a[132:135], a[8:11], v[160:163]\n"
    "ds_read_b64_tr_b16 a[162:163], v212 offset:2048\n"
    "v_accvgpr_write_b32 a223, v31\n"
    "v_mfma_f32_16x16x32_bf16 v[160:163], a[136:139], a[16:19], v[160:163]\n"
    "ds_read_b64_tr_b16 a[164:165], v212 offset:4096\n"
    "v_accvgpr_write_b32 a224, v0\n"
    "v_mfma_f32_16x16x32_bf16 v[160:163], a[140:143], a[24:27], v[160:163]\n"
    "s_waitcnt lgkmcnt(3)\n"
    "ds_read_b64_tr_b16 a[166:167], v212 offset:6144\n"
    "v_accvgpr_write_b32 a225, v1\n"
    "v_mfma_f32_16x16x32_bf16 v[160:163], a[144:147], a[32:35], v[160:163]\n"
    "ds_read_b64_tr_b16 a[168:169], v212 offset:8192\n"
    "v_cvt_pk_bf16_f32 v14, v156, v157\n"
    "v_mfma_f32_16x16x32_bf16 v[160:163], a[148:151], a[40:43], v[160:163]\n"
    "ds_read_b64_tr_b16 a[170:171], v212 offset:10240\n"
    "v_cvt_pk_bf16_f32 v15, v158, v159\n"
    "v_mfma_f32_16x16x32_bf16 v[160:163], a[152:155], a[48:51], v[160:163]\n"
    "ds_read_b64_tr_b16 a[172:173], v212 offset:12288\n"
    "v_accvgpr_write_b32 a226, v2\n"
    "v_mfma_f32_16x16x32_bf16 v[160:163], a[156:159], a[56:59], v[160:163]\n"
    "buffer_store_dwordx4 v[12:15], v206, s[80:83], s61 offen offset:2048 sc1\n"
    "v_mfma_f32_16x16x32_bf16 v[168:171], a[128:131], a[4:7], v[168:171]\n"
    "ds_read_b64_tr_b16 a[174:175], v212 offset:14336\n"
    "v_accvgpr_write_b32 a227, v3\n"
    "v_mfma_f32_16x16x32_bf16 v[168:171], a[132:135], a[12:15], v[168:171]\n"
    "ds_read_b64_tr_b16 a[176:177], v212 offset:16384\n"
    "v_accvgpr_write_b32 a228, v4\n"
    "v_mfma_f32_16x16x32_bf16 v[168:171], a[136:139], a[20:23], v[168:171]\n"
    "ds_read_b64_tr_b16 a[178:179], v212 offset:18432\n"
    "v_accvgpr_write_b32 a229, v5\n"
    "v_mfma_f32_16x16x32_bf16 v[168:171], a[140:143], a[28:31], v[168:171]\n"
    "ds_read_b64_tr_b16 a[180:181], v212 offset:20480\n"
    "v_accvgpr_write_b32 a230, v6\n"
    "v_mfma_f32_16x16x32_bf16 v[168:171], a[144:147], a[36:39], v[168:171]\n"
    "ds_read_b64_tr_b16 a[182:183], v212 offset:22528\n"
    "v_cvt_pk_bf16_f32 v16, v160, v161\n"
    "v_mfma_f32_16x16x32_bf16 v[168:171], a[148:151], a[44:47], v[168:171]\n"
    "ds_read_b64_tr_b16 a[184:185], v212 offset:24576\n"
    "v_cvt_pk_bf16_f32 v17, v162, v163\n"
    "v_mfma_f32_16x16x32_bf16 v[168:171], a[152:155], a[52:55], v[168:171]\n"
    "buffer_load_dword v227, v217, s[52:55], 0 offen offset:0 sc1\n"
    "v_mfma_f32_16x16x32_bf16 v[168:171], a[156:159], a[60:63], v[168:171]\n"
    "s_waitcnt lgkmcnt(5)\n"
    "ds_read_b64_tr_b16 a[186:187], v212 offset:26624\n"
    "v_accvgpr_write_b32 a231, v7\n"
    "v_mfma_f32_16x16x32_bf16 v[164:167], a[160:163], a[0:3], v[164:167]\n"
    "ds_read_b64_tr_b16 a[188:189], v212 offset:28672\n"
    "s_mov_b32 s60, 0xc000\n"
    "v_mfma_f32_16x16x32_bf16 v[164:167], a[164:167], a[8:11], v[164:167]\n"
    "ds_read_b64_tr_b16 a[190:191], v212 offset:30720\n"
    "v_accvgpr_write_b32 a232, v8\n"
    "v_mfma_f32_16x16x32_bf16 v[164:167], a[168:171], a[16:19], v[164:167]\n"
    "v_accvgpr_write_b32 a233, v9\n"
    "v_accvgpr_write_b32 a234, v10\n"
    "v_mfma_f32_16x16x32_bf16 v[164:167], a[172:175], a[24:27], v[164:167]\n"
    "s_waitcnt lgkmcnt(0)\n"
    "v_accvgpr_write_b32 a235, v11\n"
    "v_accvgpr_write_b32 a236, v12\n"
    "v_mfma_f32_16x16x32_bf16 v[164:167], a[176:179], a[32:35], v[164:167]\n"
    "v_cvt_pk_bf16_f32 v20, v168, v169\n"
    "v_cvt_pk_bf16_f32 v21, v170, v171\n"
    "v_mfma_f32_16x16x32_bf16 v[164:167], a[180:183], a[40:43], v[164:167]\n"
    "v_accvgpr_write_b32 a237, v13\n"
    "v_accvgpr_write_b32 a238, v14\n"
    "v_mfma_f32_16x16x32_bf16 v[164:167], a[184:187], a[48:51], v[164:167]\n"
    "v_accvgpr_write_b32 a239, v15\n"
    "s_mov_b32 s67, 0\n"
    "v_mfma_f32_16x16x32_bf16 v[164:167], a[188:191], a[56:59], v[164:167]\n"
    "v_mfma_f32_16x16x32_bf16 v[172:175], a[160:163], a[4:7], v[172:175]\n"
    "v_mfma_f32_16x16x32_bf16 v[172:175], a[164:167], a[12:15], v[172:175]\n"
    "v_mfma_f32_16x16x32_bf16 v[172:175], a[168:171], a[20:23], v[172:175]\n"
    "v_mfma_f32_16x16x32_bf16 v[172:175], a[172:175], a[28:31], v[172:175]\n"
    "v_mfma_f32_16x16x32_bf16 v[172:175], a[176:179], a[36:39], v[172:175]\n"
    "s_nop 2\n"
    "v_cvt_pk_bf16_f32 v18, v164, v165\n"
    "v_cvt_pk_bf16_f32 v19, v166, v167\n"
    "v_mfma_f32_16x16x32_bf16 v[172:175], a[180:183], a[44:47], v[172:175]\n"
    "v_accvgpr_write_b32 a240, v16\n"
    "v_accvgpr_write_b32 a241, v17\n"
    "v_mfma_f32_16x16x32_bf16 v[172:175], a[184:187], a[52:55], v[172:175]\n"
    "v_accvgpr_write_b32 a242, v18\n"
    "v_accvgpr_write_b32 a243, v19\n"
    "v_mfma_f32_16x16x32_bf16 v[172:175], a[188:191], a[60:63], v[172:175]\n"
    "buffer_store_dwordx4 v[16:19], v205, s[80:83], s60 offen offset:0 sc1\n"
    "s_nop 6\n"
    "v_cvt_pk_bf16_f32 v22, v172, v173\n"
    "v_cvt_pk_bf16_f32 v23, v174, v175\n"
    "buffer_store_dwordx4 v[20:23], v206, s[80:83], s60 offen offset:2048 sc1\n"
    "v_accvgpr_write_b32 a244, v20\n"
    "v_accvgpr_write_b32 a245, v21\n"
    "v_accvgpr_write_b32 a246, v22\n"
    "v_accvgpr_write_b32 a247, v23\n"
    "s_waitcnt vmcnt(2)\n"
    "v_cmp_ne_u32 vcc, s56, v227\n"
    "s_nop 1\n"
    "s_cbranch_vccnz LSPIN0%=\n"
    "LGO0%=:\n"
    "s_mov_b32 s40, 0xffff0000\n"
    "buffer_load_dword v56, v203, s[84:87], 0 offen offset:0 sc1\n"
    "buffer_load_dword v58, v204, s[84:87], 0 offen offset:0 sc1\n"
    "s_mov_b32 s41, 0x0\n"
    "buffer_load_dwordx4 v[64:67], v201, s[84:87], s41 offen offset:0 sc1\n"
    "buffer_load_dwordx4 v[68:71], v202, s[84:87], s41 offen offset:0 sc1\n"
    "s_mov_b32 s41, 0x2000\n"
    "buffer_load_dwordx4 v[72:75], v201, s[84:87], s41 offen offset:0 sc1\n"
    "buffer_load_dwordx4 v[76:79], v202, s[84:87], s41 offen offset:0 sc1\n"
    "s_mov_b32 s41, 0x4000\n"
    "buffer_load_dwordx4 v[80:83], v201, s[84:87], s41 offen offset:0 sc1\n"
    "buffer_load_dwordx4 v[84:87], v202, s[84:87], s41 offen offset:0 sc1\n"
    "s_cmp_eq_u32 s19, 0\n"
    "s_cbranch_scc0 LOWN%=\n"
    "LOWB%=:\n"
    "s_waitcnt vmcnt(8)\n"
    "s_barrier\n"
    "buffer_store_dword v226, v215, s[52:55], 0 offen offset:64 sc1\n"
    "s_mov_b32 s64, 0\n"
    "ds_read_b128 v[176:179], v233 offset:0\n"
    "ds_read_b128 v[180:183], v233 offset:2048\n"
    "ds_read_b128 v[184:187], v233 offset:4096\n"
    "ds_read_b128 v[188:191], v233 offset:6144\n"
    "ds_read_b128 v[192:195], v233 offset:8192\n"
    "ds_read_b128 v[196:199], v233 offset:10240\n"
    "ds_read_b128 v[60:63], v233 offset:12288\n"
    "s_waitcnt vmcnt(5)\n"
    "s_waitcnt lgkmcnt(6)\n"
    "v_and_or_b32 v64, v176, v234, v64\n"
    "v_and_or_b32 v65, v177, v234, v65\n"
    "v_and_or_b32 v66, v178, v234, v66\n"
    "v_and_or_b32 v67, v179, v234, v67\n"
    "v_lshlrev_b32 v48, 16, v64\n"
    "v_and_b32 v49, s40, v64\n"
    "v_lshlrev_b32 v50, 16, v65\n"
    "v_and_b32 v51, s40, v65\n"
    "v_lshlrev_b32 v52, 16, v66\n"
    "v_and_b32 v53, s40, v66\n"
    "v_lshlrev_b32 v54, 16, v67\n"
    "v_and_b32 v55, s40, v67\n"
    "v_mul_f32 v32, v48, v56\n"
    "v_mul_f32 v33, v49, v56\n"
    "v_mul_f32 v34, v50, v56\n"
    "v_mul_f32 v35, v51, v56\n"
    "v_mul_f32 v36, v52, v56\n"
    "v_mul_f32 v37, v53, v56\n"
    "v_mul_f32 v38, v54, v56\n"
    "v_mul_f32 v39, v55, v56\n"
    "v_lshlrev_b32 v48, 16, v68\n"
    "v_and_b32 v49, s40, v68\n"
    "v_lshlrev_b32 v50, 16, v69\n"
    "v_and_b32 v51, s40, v69\n"
    "v_lshlrev_b32 v52, 16, v70\n"
    "v_and_b32 v53, s40, v70\n"
    "v_lshlrev_b32 v54, 16, v71\n"
    "v_and_b32 v55, s40, v71\n"
    "v_fmac_f32 v32, v48, v58\n"
    "v_fmac_f32 v33, v49, v58\n"
    "v_fmac_f32 v34, v50, v58\n"
    "v_fmac_f32 v35, v51, v58\n"
    "v_fmac_f32 v36, v52, v58\n"
    "v_fmac_f32 v37, v53, v58\n"
    "v_fmac_f32 v38, v54, v58\n"
    "v_fmac_f32 v39, v55, v58\n"
    "v_permlane32_swap_b32 v32, v36\n"
    "v_permlane32_swap_b32 v33, v37\n"
    "v_permlane32_swap_b32 v34, v38\n"
    "v_permlane32_swap_b32 v35, v39\n"
    "v_add_f32 v32, v32, v36\n"
    "v_add_f32 v33, v33, v37\n"
    "v_add_f32 v34, v34, v38\n"
    "v_add_f32 v35, v35, v39\n"
    "s_nop 0\n"
    "v_permlane16_swap_b32 v32, v34\n"
    "v_permlane16_swap_b32 v33, v35\n"
    "v_add_f32 v32, v32, v34\n"
    "v_add_f32 v33, v33, v35\n"
    "v_cvt_pk_bf16_f32 v32, v32, v33\n"
    "buffer_store_dword v32, v221, s[72:75], 0 offen offset:0\n"
    "s_waitcnt vmcnt(4)\n"
    "s_waitcnt lgkmcnt(5)\n"
    "v_and_or_b32 v72, v180, v234, v72\n"
    "v_and_or_b32 v73, v181, v234, v73\n"
    "v_and_or_b32 v74, v182, v234, v74\n"
    "v_and_or_b32 v75, v183, v234, v75\n"
    "v_lshlrev_b32 v48, 16, v72\n"
    "v_and_b32 v49, s40, v72\n"
    "v_lshlrev_b32 v50, 16, v73\n"
    "v_and_b32 v51, s40, v73\n"
    "v_lshlrev_b32 v52, 16, v74\n"
    "v_and_b32 v53, s40, v74\n"
    "v_lshlrev_b32 v54, 16, v75\n"
    "v_and_b32 v55, s40, v75\n"
    "v_mul_f32 v40, v48, v56\n"
    "v_mul_f32 v41, v49, v56\n"
    "v_mul_f32 v42, v50, v56\n"
    "v_mul_f32 v43, v51, v56\n"
    "v_mul_f32 v44, v52, v56\n"
    "v_mul_f32 v45, v53, v56\n"
    "v_mul_f32 v46, v54, v56\n"
    "v_mul_f32 v47, v55, v56\n"
    "v_lshlrev_b32 v48, 16, v76\n"
    "v_and_b32 v49, s40, v76\n"
    "v_lshlrev_b32 v50, 16, v77\n"
    "v_and_b32 v51, s40, v77\n"
    "v_lshlrev_b32 v52, 16, v78\n"
    "v_and_b32 v53, s40, v78\n"
    "v_lshlrev_b32 v54, 16, v79\n"
    "v_and_b32 v55, s40, v79\n"
    "v_fmac_f32 v40, v48, v58\n"
    "v_fmac_f32 v41, v49, v58\n"
    "v_fmac_f32 v42, v50, v58\n"
    "v_fmac_f32 v43, v51, v58\n"
    "v_fmac_f32 v44, v52, v58\n"
    "v_fmac_f32 v45, v53, v58\n"
    "v_fmac_f32 v46, v54, v58\n"
    "v_fmac_f32 v47, v55, v58\n"
    "v_permlane32_swap_b32 v40, v44\n"
    "v_permlane32_swap_b32 v41, v45\n"
    "v_permlane32_swap_b32 v42, v46\n"
    "v_permlane32_swap_b32 v43, v47\n"
    "v_add_f32 v40, v40, v44\n"
    "v_add_f32 v41, v41, v45\n"
    "v_add_f32 v42, v42, v46\n"
    "v_add_f32 v43, v43, v47\n"
    "s_nop 0\n"
    "v_permlane16_swap_b32 v40, v42\n"
    "v_permlane16_swap_b32 v41, v43\n"
    "v_add_f32 v40, v40, v42\n"
    "v_add_f32 v41, v41, v43\n"
    "v_cvt_pk_bf16_f32 v40, v40, v41\n"
    "buffer_store_dword v40, v221, s[72:75], 0 offen offset:128\n"
    "buffer_load_dword v227, v217, s[52:55], 0 offen offset:64 sc1\n"
    "s_waitcnt vmcnt(4)\n"
    "s_waitcnt lgkmcnt(4)\n"
    "v_and_or_b32 v80, v184, v234, v80\n"
    "v_and_or_b32 v81, v185, v234, v81\n"
    "v_and_or_b32 v82, v186, v234, v82\n"
    "v_and_or_b32 v83, v187, v234, v83\n"
    "v_lshlrev_b32 v48, 16, v80\n"
    "v_and_b32 v49, s40, v80\n"
    "v_lshlrev_b32 v50, 16, v81\n"
    "v_and_b32 v51, s40, v81\n"
    "v_lshlrev_b32 v52, 16, v82\n"
    "v_and_b32 v53, s40, v82\n"
    "v_lshlrev_b32 v54, 16, v83\n"
    "v_and_b32 v55, s40, v83\n"
    "v_mul_f32 v32, v48, v56\n"
    "v_mul_f32 v33, v49, v56\n"
    "v_mul_f32 v34, v50, v56\n"
    "v_mul_f32 v35, v51, v56\n"
    "v_mul_f32 v36, v52, v56\n"
    "v_mul_f32 v37, v53, v56\n"
    "v_mul_f32 v38, v54, v56\n"
    "v_mul_f32 v39, v55, v56\n"
    "v_lshlrev_b32 v48, 16, v84\n"
    "v_and_b32 v49, s40, v84\n"
    "v_lshlrev_b32 v50, 16, v85\n"
    "v_and_b32 v51, s40, v85\n"
    "v_lshlrev_b32 v52, 16, v86\n"
    "v_and_b32 v53, s40, v86\n"
    "v_lshlrev_b32 v54, 16, v87\n"
    "v_and_b32 v55, s40, v87\n"
    "v_fmac_f32 v32, v48, v58\n"
    "v_fmac_f32 v33, v49, v58\n"
    "v_fmac_f32 v34, v50, v58\n"
    "v_fmac_f32 v35, v51, v58\n"
    "v_fmac_f32 v36, v52, v58\n"
    "v_fmac_f32 v37, v53, v58\n"
    "v_fmac_f32 v38, v54, v58\n"
    "v_fmac_f32 v39, v55, v58\n"
    "v_permlane32_swap_b32 v32, v36\n"
    "v_permlane32_swap_b32 v33, v37\n"
    "v_permlane32_swap_b32 v34, v38\n"
    "v_permlane32_swap_b32 v35, v39\n"
    "v_add_f32 v32, v32, v36\n"
    "v_add_f32 v33, v33, v37\n"
    "v_add_f32 v34, v34, v38\n"
    "v_add_f32 v35, v35, v39\n"
    "s_nop 0\n"
    "v_permlane16_swap_b32 v32, v34\n"
    "v_permlane16_swap_b32 v33, v35\n"
    "v_add_f32 v32, v32, v34\n"
    "v_add_f32 v33, v33, v35\n"
    "v_cvt_pk_bf16_f32 v32, v32, v33\n"
    "buffer_store_dword v32, v221, s[72:75], 0 offen offset:256\n"
    "s_waitcnt vmcnt(1)\n"
    "v_cmp_ne_u32 vcc, s56, v227\n"
    "s_nop 1\n"
    "s_cbranch_vccnz LSPIN1%=\n"
    "LGO1%=:\n"
    "s_mov_b32 s41, 0x6000\n"
    "buffer_load_dwordx4 v[88:91], v201, s[84:87], s41 offen offset:0 sc1\n"
    "buffer_load_dwordx4 v[92:95], v202, s[84:87], s41 offen offset:0 sc1\n"
    "s_mov_b32 s41, 0x8000\n"
    "buffer_load_dwordx4 v[96:99], v201, s[84:87], s41 offen offset:0 sc1\n"
    "buffer_load_dwordx4 v[100:103], v202, s[84:87], s41 offen offset:0 sc1\n"
    "s_mov_b32 s41, 0xa000\n"
    "buffer_load_dwordx4 v[104:107], v201, s[84:87], s41 offen offset:0 sc1\n"
    "buffer_load_dwordx4 v[108:111], v202, s[84:87], s41 offen offset:0 sc1\n"
    "s_mov_b32 s41, 0xc000\n"
    "buffer_load_dwordx4 v[112:115], v201, s[84:87], s41 offen offset:0 sc1\n"
    "buffer_load_dwordx4 v[116:119], v202, s[84:87], s41 offen offset:0 sc1\n"
    "buffer_load_dword v228, v219, s[52:55], 0 offen offset:0 sc1\n"
    "s_waitcnt vmcnt(7)\n"
    "s_waitcnt lgkmcnt(3)\n"
    "v_and_or_b32 v88, v188, v234, v88\n"
    "v_and_or_b32 v89, v189, v234, v89\n"
    "v_and_or_b32 v90, v190, v234, v90\n"
    "v_and_or_b32 v91, v191, v234, v91\n"
    "v_lshlrev_b32 v48, 16, v88\n"
    "v_and_b32 v49, s40, v88\n"
    "v_lshlrev_b32 v50, 16, v89\n"
    "v_and_b32 v51, s40, v89\n"
    "v_lshlrev_b32 v52, 16, v90\n"
    "v_and_b32 v53, s40, v90\n"
    "v_lshlrev_b32 v54, 16, v91\n"
    "v_and_b32 v55, s40, v91\n"
    "v_mul_f32 v40, v48, v56\n"
    "v_mul_f32 v41, v49, v56\n"
    "v_mul_f32 v42, v50, v56\n"
    "v_mul_f32 v43, v51, v56\n"
    "v_mul_f32 v44, v52, v56\n"
    "v_mul_f32 v45, v53, v56\n"
    "v_mul_f32 v46, v54, v56\n"
    "v_mul_f32 v47, v55, v56\n"
    "v_lshlrev_b32 v48, 16, v92\n"
    "v_and_b32 v49, s40, v92\n"
    "v_lshlrev_b32 v50, 16, v93\n"
    "v_and_b32 v51, s40, v93\n"
    "v_lshlrev_b32 v52, 16, v94\n"
    "v_and_b32 v53, s40, v94\n"
    "v_lshlrev_b32 v54, 16, v95\n"
    "v_and_b32 v55, s40, v95\n"
    "v_fmac_f32 v40, v48, v58\n"
    "v_fmac_f32 v41, v49, v58\n"
    "v_fmac_f32 v42, v50, v58\n"
    "v_fmac_f32 v43, v51, v58\n"
    "v_fmac_f32 v44, v52, v58\n"
    "v_fmac_f32 v45, v53, v58\n"
    "v_fmac_f32 v46, v54, v58\n"
    "v_fmac_f32 v47, v55, v58\n"
    "v_permlane32_swap_b32 v40, v44\n"
    "v_permlane32_swap_b32 v41, v45\n"
    "v_permlane32_swap_b32 v42, v46\n"
    "v_permlane32_swap_b32 v43, v47\n"
    "v_add_f32 v40, v40, v44\n"
    "v_add_f32 v41, v41, v45\n"
    "v_add_f32 v42, v42, v46\n"
    "v_add_f32 v43, v43, v47\n"
    "s_nop 0\n"
    "v_permlane16_swap_b32 v40, v42\n"
    "v_permlane16_swap_b32 v41, v43\n"
    "v_add_f32 v40, v40, v42\n"
    "v_add_f32 v41, v41, v43\n"
    "v_cvt_pk_bf16_f32 v40, v40, v41\n"
    "buffer_store_dword v40, v221, s[72:75], 0 offen offset:384\n"
    "s_waitcnt vmcnt(6)\n"
    "s_waitcnt lgkmcnt(2)\n"
    "v_and_or_b32 v96, v192, v234, v96\n"
    "v_and_or_b32 v97, v193, v234, v97\n"
    "v_and_or_b32 v98, v194, v234, v98\n"
    "v_and_or_b32 v99, v195, v234, v99\n"
    "v_lshlrev_b32 v48, 16, v96\n"
    "v_and_b32 v49, s40, v96\n"
    "v_lshlrev_b32 v50, 16, v97\n"
    "v_and_b32 v51, s40, v97\n"
    "v_lshlrev_b32 v52, 16, v98\n"
    "v_and_b32 v53, s40, v98\n"
    "v_lshlrev_b32 v54, 16, v99\n"
    "v_and_b32 v55, s40, v99\n"
    "v_mul_f32 v32, v48, v56\n"
    "v_mul_f32 v33, v49, v56\n"
    "v_mul_f32 v34, v50, v56\n"
    "v_mul_f32 v35, v51, v56\n"
    "v_mul_f32 v36, v52, v56\n"
    "v_mul_f32 v37, v53, v56\n"
    "v_mul_f32 v38, v54, v56\n"
    "v_mul_f32 v39, v55, v56\n"
    "v_lshlrev_b32 v48, 16, v100\n"
    "v_and_b32 v49, s40, v100\n"
    "v_lshlrev_b32 v50, 16, v101\n"
    "v_and_b32 v51, s40, v101\n"
    "v_lshlrev_b32 v52, 16, v102\n"
    "v_and_b32 v53, s40, v102\n"
    "v_lshlrev_b32 v54, 16, v103\n"
    "v_and_b32 v55, s40, v103\n"
    "v_fmac_f32 v32, v48, v58\n"
    "v_fmac_f32 v33, v49, v58\n"
    "v_fmac_f32 v34, v50, v58\n"
    "v_fmac_f32 v35, v51, v58\n"
    "v_fmac_f32 v36, v52, v58\n"
    "v_fmac_f32 v37, v53, v58\n"
    "v_fmac_f32 v38, v54, v58\n"
    "v_fmac_f32 v39, v55, v58\n"
    "v_permlane32_swap_b32 v32, v36\n"
    "v_permlane32_swap_b32 v33, v37\n"
    "v_permlane32_swap_b32 v34, v38\n"
    "v_permlane32_swap_b32 v35, v39\n"
    "v_add_f32 v32, v32, v36\n"
    "v_add_f32 v33, v33, v37\n"
    "v_add_f32 v34, v34, v38\n"
    "v_add_f32 v35, v35, v39\n"
    "s_nop 0\n"
    "v_permlane16_swap_b32 v32, v34\n"
    "v_permlane16_swap_b32 v33, v35\n"
    "v_add_f32 v32, v32, v34\n"
    "v_add_f32 v33, v33, v35\n"
    "v_cvt_pk_bf16_f32 v32, v32, v33\n"
    "buffer_store_dword v32, v221, s[72:75], 0 offen offset:512\n"
    "s_waitcnt vmcnt(5)\n"
    "s_waitcnt lgkmcnt(1)\n"
    "v_and_or_b32 v104, v196, v234, v104\n"
    "v_and_or_b32 v105, v197, v234, v105\n"
    "v_and_or_b32 v106, v198, v234, v106\n"
    "v_and_or_b32 v107, v199, v234, v107\n"
    "v_lshlrev_b32 v48, 16, v104\n"
    "v_and_b32 v49, s40, v104\n"
    "v_lshlrev_b32 v50, 16, v105\n"
    "v_and_b32 v51, s40, v105\n"
    "v_lshlrev_b32 v52, 16, v106\n"
    "v_and_b32 v53, s40, v106\n"
    "v_lshlrev_b32 v54, 16, v107\n"
    "v_and_b32 v55, s40, v107\n"
    "v_mul_f32 v40, v48, v56\n"
    "v_mul_f32 v41, v49, v56\n"
    "v_mul_f32 v42, v50, v56\n"
    "v_mul_f32 v43, v51, v56\n"
    "v_mul_f32 v44, v52, v56\n"
    "v_mul_f32 v45, v53, v56\n"
    "v_mul_f32 v46, v54, v56\n"
    "v_mul_f32 v47, v55, v56\n"
    "v_lshlrev_b32 v48, 16, v108\n"
    "v_and_b32 v49, s40, v108\n"
    "v_lshlrev_b32 v50, 16, v109\n"
    "v_and_b32 v51, s40, v109\n"
    "v_lshlrev_b32 v52, 16, v110\n"
    "v_and_b32 v53, s40, v110\n"
    "v_lshlrev_b32 v54, 16, v111\n"
    "v_and_b32 v55, s40, v111\n"
    "v_fmac_f32 v40, v48, v58\n"
    "v_fmac_f32 v41, v49, v58\n"
    "v_fmac_f32 v42, v50, v58\n"
    "v_fmac_f32 v43, v51, v58\n"
    "v_fmac_f32 v44, v52, v58\n"
    "v_fmac_f32 v45, v53, v58\n"
    "v_fmac_f32 v46, v54, v58\n"
    "v_fmac_f32 v47, v55, v58\n"
    "v_permlane32_swap_b32 v40, v44\n"
    "v_permlane32_swap_b32 v41, v45\n"
    "v_permlane32_swap_b32 v42, v46\n"
    "v_permlane32_swap_b32 v43, v47\n"
    "v_add_f32 v40, v40, v44\n"
    "v_add_f32 v41, v41, v45\n"
    "v_add_f32 v42, v42, v46\n"
    "v_add_f32 v43, v43, v47\n"
    "s_nop 0\n"
    "v_permlane16_swap_b32 v40, v42\n"
    "v_permlane16_swap_b32 v41, v43\n"
    "v_add_f32 v40, v40, v42\n"
    "v_add_f32 v41, v41, v43\n"
    "v_cvt_pk_bf16_f32 v40, v40, v41\n"
    "buffer_store_dword v40, v221, s[72:75], 0 offen offset:640\n"
    "s_waitcnt vmcnt(4)\n"
    "s_waitcnt lgkmcnt(0)\n"
    "v_and_or_b32 v112, v60, v234, v112\n"
    "v_and_or_b32 v113, v61, v234, v113\n"
    "v_and_or_b32 v114, v62, v234, v114\n"
    "v_and_or_b32 v115, v63, v234, v115\n"
    "v_lshlrev_b32 v48, 16, v112\n"
    "v_and_b32 v49, s40, v112\n"
    "v_lshlrev_b32 v50, 16, v113\n"
    "v_and_b32 v51, s40, v113\n"
    "v_lshlrev_b32 v52, 16, v114\n"
    "v_and_b32 v53, s40, v114\n"
    "v_lshlrev_b32 v54, 16, v115\n"
    "v_and_b32 v55, s40, v115\n"
    "v_mul_f32 v32, v48, v56\n"
    "v_mul_f32 v33, v49, v56\n"
    "v_mul_f32 v34, v50, v56\n"
    "v_mul_f32 v35, v51, v56\n"
    "v_mul_f32 v36, v52, v56\n"
    "v_mul_f32 v37, v53, v56\n"
    "v_mul_f32 v38, v54, v56\n"
    "v_mul_f32 v39, v55, v56\n"
    "v_lshlrev_b32 v48, 16, v116\n"
    "v_and_b32 v49, s40, v116\n"
    "v_lshlrev_b32 v50, 16, v117\n"
    "v_and_b32 v51, s40, v117\n"
    "v_lshlrev_b32 v52, 16, v118\n"
    "v_and_b32 v53, s40, v118\n"
    "v_lshlrev_b32 v54, 16, v119\n"
    "v_and_b32 v55, s40, v119\n"
    "v_fmac_f32 v32, v48, v58\n"
    "v_fmac_f32 v33, v49, v58\n"
    "v_fmac_f32 v34, v50, v58\n"
    "v_fmac_f32 v35, v51, v58\n"
    "v_fmac_f32 v36, v52, v58\n"
    "v_fmac_f32 v37, v53, v58\n"
    "v_fmac_f32 v38, v54, v58\n"
    "v_fmac_f32 v39, v55, v58\n"
    "v_permlane32_swap_b32 v32, v36\n"
    "v_permlane32_swap_b32 v33, v37\n"
    "v_permlane32_swap_b32 v34, v38\n"
    "v_permlane32_swap_b32 v35, v39\n"
    "v_add_f32 v32, v32, v36\n"
    "v_add_f32 v33, v33, v37\n"
    "v_add_f32 v34, v34, v38\n"
    "v_add_f32 v35, v35, v39\n"
    "s_nop 0\n"
    "v_permlane16_swap_b32 v32, v34\n"
    "v_permlane16_swap_b32 v33, v35\n"
    "v_add_f32 v32, v32, v34\n"
    "v_add_f32 v33, v33, v35\n"
    "v_cvt_pk_bf16_f32 v32, v32, v33\n"
    "buffer_store_dword v32, v221, s[72:75], 0 offen offset:768\n"
    "s_waitcnt vmcnt(4)\n"
    "v_cmp_ne_u32 vcc, v226, v228\n"
    "s_nop 1\n"
    "s_cbranch_vccz LFIN%=\n"
    "v_mov_b32 v226, v228\n"
    "buffer_store_dword v226, v216, s[52:55], 0 offen offset:0 sc1\n"
    "buffer_store_dword v226, v215, s[52:55], s67 offen offset:0 sc1\n"
    "buffer_store_dword v226, v215, s[52:55], s64 offen offset:64 sc1\n"
    "LFIN%=:\n"
    "s_branch LEND%=\n"
    "LDF%=:\n"
    "v_mfma_f32_16x16x32_bf16 v[176:179], v[184:187], v[160:163], v[176:179]\n"
    "s_add_u32 s41, s65, 0x180\n"
    "s_add_u32 m0, s36, 0x10000\n"
    "v_mfma_f32_16x16x32_bf16 v[180:183], v[184:187], v[164:167], v[180:183]\n"
    "s_waitcnt lgkmcnt(15)\n"
    "buffer_load_dwordx4 v241, s[12:15], s41 offen offset:0 lds\n"
    "v_mfma_f32_16x16x32_bf16 v[0:3], a[128:131], v[160:163], v[0:3]\n"
    "v_mfma_f32_16x16x32_bf16 v[8:11], a[128:131], v[164:167], v[8:11]\n"
    "ds_read_b64_tr_b16 a[128:129], v213 offset:0\n"
    "ds_read_b64_tr_b16 a[130:131], v213 offset:2048\n"
    "v_mfma_f32_16x16x32_bf16 v[4:7], a[132:135], v[160:163], v[4:7]\n"
    "buffer_load_dwordx4 v242, s[12:15], s41 offen offset:1024 lds\n"
    "v_mfma_f32_16x16x32_bf16 v[12:15], a[132:135], v[164:167], v[12:15]\n"
    "ds_read_b64_tr_b16 a[132:133], v213 offset:4096\n"
    "ds_read_b64_tr_b16 a[134:135], v213 offset:6144\n"
    "v_mfma_f32_16x16x32_bf16 v[16:19], a[136:139], v[160:163], v[16:19]\n"
    "v_mfma_f32_16x16x32_bf16 v[24:27], a[136:139], v[164:167], v[24:27]\n"
    "buffer_store_dword v176, v230, s[88:91], 0 offen offset:0 sc1\n"
    "v_mfma_f32_16x16x32_bf16 v[20:23], a[140:143], v[160:163], v[20:23]\n"
    "ds_read_b64_tr_b16 a[136:137], v213 offset:8192\n"
    "ds_read_b64_tr_b16 a[138:139], v213 offset:10240\n"
    "v_mfma_f32_16x16x32_bf16 v[28:31], a[140:143], v[164:167], v[28:31]\n"
    "ds_read_b64_tr_b16 a[140:141], v213 offset:12288\n"
    "ds_read_b64_tr_b16 a[142:143], v213 offset:14336\n"
    "v_mfma_f32_16x16x32_bf16 v[32:35], a[144:147], v[160:163], v[32:35]\n"
    "buffer_store_dword v180, v230, s[88:91], 0 offen offset:64 sc1\n"
    "v_mfma_f32_16x16x32_bf16 v[40:43], a[144:147], v[164:167], v[40:43]\n"
    "ds_read_b64_tr_b16 a[144:145], v213 offset:16384\n"
    "ds_read_b64_tr_b16 a[146:147], v213 offset:18432\n"
    "v_mfma_f32_16x16x32_bf16 v[36:39], a[148:151], v[160:163], v[36:39]\n"
    "v_cvt_pk_bf16_f32 v0, v0, v1\n"
    "v_cvt_pk_bf16_f32 v1, v2, v3\n"
    "v_mfma_f32_16x16x32_bf16 v[44:47], a[148:151], v[164:167], v[44:47]\n"
    "buffer_load_dwordx4 v243, s[12:15], s41 offen offset:2048 lds\n"
    "v_mfma_f32_16x16x32_bf16 v[48:51], a[152:155], v[160:163], v[48:51]\n"
    "ds_read_b64_tr_b16 a[148:149], v213 offset:20480\n"
    "ds_read_b64_tr_b16 a[150:151], v213 offset:22528\n"
    "v_mfma_f32_16x16x32_bf16 v[56:59], a[152:155], v[164:167], v[56:59]\n"
    "ds_read_b64_tr_b16 a[152:153], v213 offset:24576\n"
    "v_cvt_pk_bf16_f32 v2, v4, v5\n"
    "v_mfma_f32_16x16x32_bf16 v[52:55], a[156:159], v[160:163], v[52:55]\n"
    "buffer_load_dwordx4 v244, s[12:15], s41 offen offset:3072 lds\n"
    "v_mfma_f32_16x16x32_bf16 v[60:63], a[156:159], v[164:167], v[60:63]\n"
    "s_waitcnt lgkmcnt(15)\n"
    "ds_read_b64_tr_b16 a[154:155], v213 offset:26624\n"
    "s_add_u32 s41, s65, 0x1c180\n"
    "v_mfma_f32_16x16x32_bf16 v[64:67], a[160:163], v[160:163], v[64:67]\n"
    "ds_read_b64_tr_b16 a[156:157], v213 offset:28672\n"
    "s_add_u32 m0, s36, 0x14000\n"
    "v_mfma_f32_16x16x32_bf16 v[72:75], a[160:163], v[164:167], v[72:75]\n"
    "buffer_load_dwordx4 v241, s[12:15], s41 offen offset:0 lds\n"
    "v_mfma_f32_16x16x32_bf16 v[68:71], a[164:167], v[160:163], v[68:71]\n"
    "ds_read_b64_tr_b16 a[158:159], v213 offset:30720\n"
    "v_cvt_pk_bf16_f32 v3, v6, v7\n"
    "v_mfma_f32_16x16x32_bf16 v[76:79], a[164:167], v[164:167], v[76:79]\n"
    "v_cvt_pk_bf16_f32 v4, v8, v9\n"
    "v_cvt_pk_bf16_f32 v5, v10, v11\n"
    "v_mfma_f32_16x16x32_bf16 v[80:83], a[168:171], v[160:163], v[80:83]\n"
    "buffer_load_dwordx4 v242, s[12:15], s41 offen offset:1024 lds\n"
    "v_mfma_f32_16x16x32_bf16 v[88:91], a[168:171], v[164:167], v[88:91]\n"
    "v_cvt_pk_bf16_f32 v6, v12, v13\n"
    "v_cvt_pk_bf16_f32 v7, v14, v15\n"
    "v_mfma_f32_16x16x32_bf16 v[84:87], a[172:175], v[160:163], v[84:87]\n"
    "v_cvt_pk_bf16_f32 v8, v16, v17\n"
    "v_cvt_pk_bf16_f32 v9, v18, v19\n"
    "v_mfma_f32_16x16x32_bf16 v[92:95], a[172:175], v[164:167], v[92:95]\n"
    "s_waitcnt lgkmcnt(15)\n"
    "buffer_load_dwordx4 v243, s[12:15], s41 offen offset:2048 lds\n"
    "v_mfma_f32_16x16x32_bf16 v[96:99], a[176:179], v[160:163], v[96:99]\n"
    "s_barrier\n"
    "v_mfma_f32_16x16x32_bf16 v[104:107], a[176:179], v[164:167], v[104:107]\n"
    "ds_write_b128 v224, v[4:7] offset:0\n"
    "v_cvt_pk_bf16_f32 v10, v20, v21\n"
    "v_mfma_f32_16x16x32_bf16 v[100:103], a[180:183], v[160:163], v[100:103]\n"
    "buffer_load_dwordx4 v244, s[12:15], s41 offen offset:3072 lds\n"
    "v_mfma_f32_16x16x32_bf16 v[108:111], a[180:183], v[164:167], v[108:111]\n"
    "v_cvt_pk_bf16_f32 v11, v22, v23\n"
    "v_cvt_pk_bf16_f32 v12, v24, v25\n"
    "v_mfma_f32_16x16x32_bf16 v[112:115], a[184:187], v[160:163], v[112:115]\n"
    "v_cvt_pk_bf16_f32 v13, v26, v27\n"
    "v_cvt_pk_bf16_f32 v14, v28, v29\n"
    "v_mfma_f32_16x16x32_bf16 v[120:123], a[184:187], v[164:167], v[120:123]\n"
    "buffer_store_dwordx4 v[0:3], v222, s[88:91], 0 offen offset:0 sc1\n"
    "v_mfma_f32_16x16x32_bf16 v[116:119], a[188:191], v[160:163], v[116:119]\n"
    "v_cvt_pk_bf16_f32 v15, v30, v31\n"
    "v_cvt_pk_bf16_f32 v16, v32, v33\n"
    "v_mfma_f32_16x16x32_bf16 v[124:127], a[188:191], v[164:167], v[124:127]\n"
    "ds_write_b128 v224, v[12:15] offset:1024\n"
    "v_cvt_pk_bf16_f32 v17, v34, v35\n"
    "buffer_store_dwordx4 v[8:11], v222, s[88:91], 0 offen offset:1024 sc1\n"
    "v_cvt_pk_bf16_f32 v18, v36, v37\n"
    "v_cvt_pk_bf16_f32 v19, v38, v39\n"
    "v_cvt_pk_bf16_f32 v20, v40, v41\n"
    "buffer_store_dwordx4 v[16:19], v222, s[88:91], 0 offen offset:2048 sc1\n"
    "v_cvt_pk_bf16_f32 v21, v42, v43\n"
    "v_cvt_pk_bf16_f32 v22, v44, v45\n"
    "v_cvt_pk_bf16_f32 v23, v46, v47\n"
    "ds_write_b128 v224, v[20:23] offset:2048\n"
    "v_cvt_pk_bf16_f32 v24, v48, v49\n"
    "v_cvt_pk_bf16_f32 v25, v50, v51\n"
    "v_cvt_pk_bf16_f32 v26, v52, v53\n"
    "v_cvt_pk_bf16_f32 v27, v54, v55\n"
    "v_cvt_pk_bf16_f32 v28, v56, v57\n"
    "v_cvt_pk_bf16_f32 v29, v58, v59\n"
    "buffer_store_dwordx4 v[24:27], v222, s[88:91], 0 offen offset:3072 sc1\n"
    "v_cvt_pk_bf16_f32 v30, v60, v61\n"
    "v_cvt_pk_bf16_f32 v31, v62, v63\n"
    "v_cvt_pk_bf16_f32 v32, v64, v65\n"
    "ds_write_b128 v224, v[28:31] offset:3072\n"
    "v_cvt_pk_bf16_f32 v33, v66, v67\n"
    "v_cvt_pk_bf16_f32 v34, v68, v69\n"
    "v_cvt_pk_bf16_f32 v35, v70, v71\n"
    "buffer_store_dwordx4 v[32:35], v222, s[88:91], s92 offen offset:0 sc1\n"
    "v_cvt_pk_bf16_f32 v36, v72, v73\n"
    "v_cvt_pk_bf16_f32 v37, v74, v75\n"
    "v_cvt_pk_bf16_f32 v38, v76, v77\n"
    "v_cvt_pk_bf16_f32 v39, v78, v79\n"
    "v_cvt_pk_bf16_f32 v40, v80, v81\n"
    "v_cvt_pk_bf16_f32 v41, v82, v83\n"
    "ds_write_b128 v224, v[36:39] offset:4096\n"
    "v_cvt_pk_bf16_f32 v42, v84, v85\n"
    "v_cvt_pk_bf16_f32 v43, v86, v87\n"
    "v_cvt_pk_bf16_f32 v44, v88, v89\n"
    "buffer_store_dwordx4 v[40:43], v222, s[88:91], s92 offen offset:1024 sc1\n"
    "v_cvt_pk_bf16_f32 v45, v90, v91\n"
    "v_cvt_pk_bf16_f32 v46, v92, v93\n"
    "v_cvt_pk_bf16_f32 v47, v94, v95\n"
    "ds_write_b128 v224, v[44:47] offset:5120\n"
    "v_cvt_pk_bf16_f32 v48, v96, v97\n"
    "v_cvt_pk_bf16_f32 v49, v98, v99\n"
    "v_cvt_pk_bf16_f32 v50, v100, v101\n"
    "v_cvt_pk_bf16_f32 v51, v102, v103\n"
    "v_cvt_pk_bf16_f32 v52, v104, v105\n"
    "v_cvt_pk_bf16_f32 v53, v106, v107\n"
    "buffer_store_dwordx4 v[48:51], v222, s[88:91], s92 offen offset:2048 sc1\n"
    "v_cvt_pk_bf16_f32 v54, v108, v109\n"
    "v_cvt_pk_bf16_f32 v55, v110, v111\n"
    "v_cvt_pk_bf16_f32 v56, v112, v113\n"
    "ds_write_b128 v224, v[52:55] offset:6144\n"
    "v_cvt_pk_bf16_f32 v57, v114, v115\n"
    "v_cvt_pk_bf16_f32 v58, v116, v117\n"
    "v_cvt_pk_bf16_f32 v59, v118, v119\n"
    "buffer_store_dwordx4 v[56:59], v222, s[88:91], s92 offen offset:3072 sc1\n"
    "v_cvt_pk_bf16_f32 v60, v120, v121\n"
    "v_cvt_pk_bf16_f32 v61, v122, v123\n"
    "v_cvt_pk_bf16_f32 v62, v124, v125\n"
    "v_cvt_pk_bf16_f32 v63, v126, v127\n"
    "ds_write_b128 v224, v[60:63] offset:7168\n"
    "s_branch LDJ%=\n"
    "LOWN%=:\n"
    "s_cmp_eq_u32 s19, 1\n"
    "s_cbranch_scc0 LOWQ%=\n"
    "ds_write_b128 v207, a[192:195] offset:0\n"
    "ds_write_b128 v207, a[200:203] offset:2048\n"
    "ds_write_b128 v207, a[208:211] offset:4096\n"
    "ds_write_b128 v207, a[216:219] offset:6144\n"
    "ds_write_b128 v207, a[224:227] offset:8192\n"
    "ds_write_b128 v207, a[232:235] offset:10240\n"
    "ds_write_b128 v207, a[240:243] offset:12288\n"
    "s_waitcnt lgkmcnt(0)\n"
    "s_branch LOWB%=\n"
    "LOWQ%=:\n"
    "ds_write_b128 v207, a[196:199] offset:0\n"
    "ds_write_b128 v207, a[204:207] offset:2048\n"
    "ds_write_b128 v207, a[212:215] offset:4096\n"
    "ds_write_b128 v207, a[220:223] offset:6144\n"
    "ds_write_b128 v207, a[228:231] offset:8192\n"
    "ds_write_b128 v207, a[236:239] offset:10240\n"
    "ds_write_b128 v207, a[244:247] offset:12288\n"
    "s_waitcnt lgkmcnt(0)\n"
    "s_branch LOWB%=\n"
    "LSPINX%=:\n"
    "s_mov_b32 s58, 0x20000\n"
    "LSPX%=:\n"
    "buffer_load_dword v227, v218, s[52:55], 0 offen offset:96 sc1\n"
    "buffer_load_dword v228, v219, s[52:55], 0 offen sc1\n"
    "s_waitcnt vmcnt(0)\n"
    "v_cmp_ne_u32 vcc, v226, v228\n"
    "s_nop 1\n"
    "s_cbranch_vccz LSQX%=\n"
    "v_mov_b32 v226, v228\n"
    "buffer_store_dword v226, v216, s[52:55], 0 offen offset:0 sc1\n"
    "buffer_store_dword v226, v215, s[52:55], s67 offen offset:0 sc1\n"
    "buffer_store_dword v226, v215, s[52:55], s64 offen offset:64 sc1\n"
    "s_waitcnt vmcnt(0)\n"
    "LSQX%=:\n"
    "v_cmp_ne_u32 vcc, s56, v227\n"
    "s_nop 1\n"
    "s_cbranch_vccz LGOX%=\n"
    "s_sleep 2\n"
    "s_sub_u32 s58, s58, 1\n"
    "s_cmp_lg_u32 s58, 0\n"
    "s_cbranch_scc1 LSPX%=\n"
    "s_branch LGOX%=\n"
    "LSPIN0%=:\n"
    "s_mov_b32 s58, 0x20000\n"
    "LSP0%=:\n"
    "buffer_load_dword v227, v217, s[52:55], 0 offen offset:0 sc1\n"
    "buffer_load_dword v228, v219, s[52:55], 0 offen sc1\n"
    "s_waitcnt vmcnt(0)\n"
    "v_cmp_ne_u32 vcc, v226, v228\n"
    "s_nop 1\n"
    "s_cbranch_vccz LSQ0%=\n"
    "v_mov_b32 v226, v228\n"
    "buffer_store_dword v226, v216, s[52:55], 0 offen offset:0 sc1\n"
    "buffer_store_dword v226, v215, s[52:55], s67 offen offset:0 sc1\n"
    "buffer_store_dword v226, v215, s[52:55], s64 offen offset:64 sc1\n"
    "s_waitcnt vmcnt(0)\n"
    "LSQ0%=:\n"
    "v_cmp_ne_u32 vcc, s56, v227\n"
    "s_nop 1\n"
    "s_cbranch_vccz LGO0%=\n"
    "s_sleep 2\n"
    "s_sub_u32 s58, s58, 1\n"
    "s_cmp_lg_u32 s58, 0\n"
    "s_cbranch_scc1 LSP0%=\n"
    "s_branch LGO0%=\n"
    "LSPIN1%=:\n"
    "s_mov_b32 s58, 0x20000\n"
    "LSP1%=:\n"
    "buffer_load_dword v227, v217, s[52:55], 0 offen offset:64 sc1\n"
    "buffer_load_dword v228, v219, s[52:55], 0 offen sc1\n"
    "s_waitcnt vmcnt(0)\n"
    "v_cmp_ne_u32 vcc, v226, v228\n"
    "s_nop 1\n"
    "s_cbranch_vccz LSQ1%=\n"
    "v_mov_b32 v226, v228\n"
    "buffer_store_dword v226, v216, s[52:55], 0 offen offset:0 sc1\n"
    "buffer_store_dword v226, v215, s[52:55], s67 offen offset:0 sc1\n"
    "buffer_store_dword v226, v215, s[52:55], s64 offen offset:64 sc1\n"
    "s_waitcnt vmcnt(0)\n"
    "LSQ1%=:\n"
    "v_cmp_ne_u32 vcc, s56, v227\n"
    "s_nop 1\n"
    "s_cbranch_vccz LGO1%=\n"
    "s_sleep 2\n"
    "s_sub_u32 s58, s58, 1\n"
    "s_cmp_lg_u32 s58, 0\n"
    "s_cbranch_scc1 LSP1%=\n"
    "s_branch LGO1%=\n"
    "LNON%=:\n"
    "s_addk_i32 s56, 1\n"
    "v_cmp_eq_u32 vcc, s56, v162\n"
    "s_nop 3\n"
    "s_cbranch_vccnz LNON%=\n"
    "s_branch LNOB%=\n"
    "LPS%=:\n"
    "s_memrealtime s[58:59]\n"
    "s_waitcnt lgkmcnt(0)\n"
    "LPQ%=:\n"
    "s_cmp_le_u32 s78, 800\n"
    "s_cbranch_scc1 LPW%=\n"
    "buffer_load_dword v167, v165, s[60:63], 0 offen sc1\n"
    "s_memrealtime s[20:21]\n"
    "s_waitcnt vmcnt(0) lgkmcnt(0)\n"
    "v_readfirstlane_b32 s78, v167\n"
    "s_sub_u32 s20, s20, s58\n"
    "s_cmp_gt_u32 s20, 400\n"
    "s_cbranch_scc1 LPB%=\n"
    "s_sub_u32 s78, s78, s76\n"
    "s_add_u32 s78, s78, 400\n"
    "s_branch LPQ%=\n"
    "LPW%=:\n"
    "s_sub_u32 s78, s78, 400\n"
    "LPT%=:\n"
    "s_memrealtime s[20:21]\n"
    "s_waitcnt lgkmcnt(0)\n"
    "s_sub_u32 s20, s20, s58\n"
    "s_cmp_lt_i32 s20, s78\n"
    "s_cbranch_scc1 LPT%=\n"
    "s_branch LPB%=\n"
    "LRARE%=:\n"
    "LR1%=:\n"
    "s_nop 7\n"
    "s_nop 3\n"
    "v_max3_f32 v253, v128, v129, v130\n"
    "v_max3_f32 v252, v131, v136, v137\n"
    "v_max3_f32 v253, v253, v138, v139\n"
    "v_max_f32 v253, v253, v252\n"
    "v_mov_b32 v252, v253\n"
    "s_nop 1\n"
    "v_permlane32_swap_b32 v253, v252\n"
    "v_max_f32 v253, v253, v252\n"
    "v_mov_b32 v252, v253\n"
    "s_nop 1\n"
    "v_permlane16_swap_b32 v253, v252\n"
    "v_max_f32 v253, v253, v252\n"
    "v_add_f32 v253, 0x40e00000, v253\n"
    "v_max_f32 v253, 0, v253\n"
    "v_sub_f32 v254, 0, v253\n"
    "v_exp_f32 v254, v254\n"
    "v_sub_f32 v128, v128, v253\n"
    "v_sub_f32 v129, v129, v253\n"
    "v_sub_f32 v130, v130, v253\n"
    "v_sub_f32 v131, v131, v253\n"
    "v_sub_f32 v136, v136, v253\n"
    "v_sub_f32 v137, v137, v253\n"
    "v_sub_f32 v138, v138, v253\n"
    "v_sub_f32 v139, v139, v253\n"
    "v_sub_f32 v144, v144, v253\n"
    "v_sub_f32 v145, v145, v253\n"
    "v_sub_f32 v146, v146, v253\n"
    "v_sub_f32 v147, v147, v253\n"
    "v_mul_f32 v176, v176, v254\n"
    "v_mul_f32 v177, v177, v254\n"
    "v_mul_f32 v178, v178, v254\n"
    "v_mul_f32 v179, v179, v254\n"
    "v_mul_f32 v0, v0, v254\n"
    "v_mul_f32 v1, v1, v254\n"
    "v_mul_f32 v2, v2, v254\n"
    "v_mul_f32 v3, v3, v254\n"
    "v_mul_f32 v4, v4, v254\n"
    "v_mul_f32 v5, v5, v254\n"
    "v_mul_f32 v6, v6, v254\n"
    "v_mul_f32 v7, v7, v254\n"
    "v_mul_f32 v16, v16, v254\n"
    "v_mul_f32 v17, v17, v254\n"
    "v_mul_f32 v18, v18, v254\n"
    "v_mul_f32 v19, v19, v254\n"
    "v_mul_f32 v20, v20, v254\n"
    "v_mul_f32 v21, v21, v254\n"
    "v_mul_f32 v22, v22, v254\n"
    "v_mul_f32 v23, v23, v254\n"
    "v_mul_f32 v32, v32, v254\n"
    "v_mul_f32 v33, v33, v254\n"
    "v_mul_f32 v34, v34, v254\n"
    "v_mul_f32 v35, v35, v254\n"
    "v_mul_f32 v36, v36, v254\n"
    "v_mul_f32 v37, v37, v254\n"
    "v_mul_f32 v38, v38, v254\n"
    "v_mul_f32 v39, v39, v254\n"
    "v_mul_f32 v48, v48, v254\n"
    "v_mul_f32 v49, v49, v254\n"
    "v_mul_f32 v50, v50, v254\n"
    "v_mul_f32 v51, v51, v254\n"
    "v_mul_f32 v52, v52, v254\n"
    "v_mul_f32 v53, v53, v254\n"
    "v_mul_f32 v54, v54, v254\n"
    "v_mul_f32 v55, v55, v254\n"
    "v_mul_f32 v64, v64, v254\n"
    "v_mul_f32 v65, v65, v254\n"
    "v_mul_f32 v66, v66, v254\n"
    "v_mul_f32 v67, v67, v254\n"
    "v_mul_f32 v68, v68, v254\n"
    "v_mul_f32 v69, v69, v254\n"
    "v_mul_f32 v70, v70, v254\n"
    "v_mul_f32 v71, v71, v254\n"
    "v_mul_f32 v80, v80, v254\n"
    "v_mul_f32 v81, v81, v254\n"
    "v_mul_f32 v82, v82, v254\n"
    "v_mul_f32 v83, v83, v254\n"
    "v_mul_f32 v84, v84, v254\n"
    "v_mul_f32 v85, v85, v254\n"
    "v_mul_f32 v86, v86, v254\n"
    "v_mul_f32 v87, v87, v254\n"
    "v_mul_f32 v96, v96, v254\n"
    "v_mul_f32 v97, v97, v254\n"
    "v_mul_f32 v98, v98, v254\n"
    "v_mul_f32 v99, v99, v254\n"
    "v_mul_f32 v100, v100, v254\n"
    "v_mul_f32 v101, v101, v254\n"
    "v_mul_f32 v102, v102, v254\n"
    "v_mul_f32 v103, v103, v254\n"
    "v_mul_f32 v112, v112, v254\n"
    "v_mul_f32 v113, v113, v254\n"
    "v_mul_f32 v114, v114, v254\n"
    "v_mul_f32 v115, v115, v254\n"
    "v_mul_f32 v116, v116, v254\n"
    "v_mul_f32 v117, v117, v254\n"
    "v_mul_f32 v118, v118, v254\n"
    "v_mul_f32 v119, v119, v254\n"
    "v_max3_f32 v253, v132, v133, v134\n"
    "v_max3_f32 v252, v135, v140, v141\n"
    "v_max3_f32 v253, v253, v142, v143\n"
    "v_max_f32 v253, v253, v252\n"
    "v_mov_b32 v252, v253\n"
    "s_nop 1\n"
    "v_permlane32_swap_b32 v253, v252\n"
    "v_max_f32 v253, v253, v252\n"
    "v_mov_b32 v252, v253\n"
    "s_nop 1\n"
    "v_permlane16_swap_b32 v253, v252\n"
    "v_max_f32 v253, v253, v252\n"
    "v_add_f32 v253, 0x40e00000, v253\n"
    "v_max_f32 v253, 0, v253\n"
    "v_sub_f32 v254, 0, v253\n"
    "v_exp_f32 v254, v254\n"
    "v_sub_f32 v132, v132, v253\n"
    "v_sub_f32 v133, v133, v253\n"
    "v_sub_f32 v134, v134, v253\n"
    "v_sub_f32 v135, v135, v253\n"
    "v_sub_f32 v140, v140, v253\n"
    "v_sub_f32 v141, v141, v253\n"
    "v_sub_f32 v142, v142, v253\n"
    "v_sub_f32 v143, v143, v253\n"
    "v_sub_f32 v148, v148, v253\n"
    "v_sub_f32 v149, v149, v253\n"
    "v_sub_f32 v150, v150, v253\n"
    "v_sub_f32 v151, v151, v253\n"
    "v_mul_f32 v180, v180, v254\n"
    "v_mul_f32 v181, v181, v254\n"
    "v_mul_f32 v182, v182, v254\n"
    "v_mul_f32 v183, v183, v254\n"
    "v_mul_f32 v8, v8, v254\n"
    "v_mul_f32 v9, v9, v254\n"
    "v_mul_f32 v10, v10, v254\n"
    "v_mul_f32 v11, v11, v254\n"
    "v_mul_f32 v12, v12, v254\n"
    "v_mul_f32 v13, v13, v254\n"
    "v_mul_f32 v14, v14, v254\n"
    "v_mul_f32 v15, v15, v254\n"
    "v_mul_f32 v24, v24, v254\n"
    "v_mul_f32 v25, v25, v254\n"
    "v_mul_f32 v26, v26, v254\n"
    "v_mul_f32 v27, v27, v254\n"
    "v_mul_f32 v28, v28, v254\n"
    "v_mul_f32 v29, v29, v254\n"
    "v_mul_f32 v30, v30, v254\n"
    "v_mul_f32 v31, v31, v254\n"
    "v_mul_f32 v40, v40, v254\n"
    "v_mul_f32 v41, v41, v254\n"
    "v_mul_f32 v42, v42, v254\n"
    "v_mul_f32 v43, v43, v254\n"
    "v_mul_f32 v44, v44, v254\n"
    "v_mul_f32 v45, v45, v254\n"
    "v_mul_f32 v46, v46, v254\n"
    "v_mul_f32 v47, v47, v254\n"
    "v_mul_f32 v56, v56, v254\n"
    "v_mul_f32 v57, v57, v254\n"
    "v_mul_f32 v58, v58, v254\n"
    "v_mul_f32 v59, v59, v254\n"
    "v_mul_f32 v60, v60, v254\n"
    "v_mul_f32 v61, v61, v254\n"
    "v_mul_f32 v62, v62, v254\n"
    "v_mul_f32 v63, v63, v254\n"
    "v_mul_f32 v72, v72, v254\n"
    "v_mul_f32 v73, v73, v254\n"
    "v_mul_f32 v74, v74, v254\n"
    "v_mul_f32 v75, v75, v254\n"
    "v_mul_f32 v76, v76, v254\n"
    "v_mul_f32 v77, v77, v254\n"
    "v_mul_f32 v78, v78, v254\n"
    "v_mul_f32 v79, v79, v254\n"
    "v_mul_f32 v88, v88, v254\n"
    "v_mul_f32 v89, v89, v254\n"
    "v_mul_f32 v90, v90, v254\n"
    "v_mul_f32 v91, v91, v254\n"
    "v_mul_f32 v92, v92, v254\n"
    "v_mul_f32 v93, v93, v254\n"
    "v_mul_f32 v94, v94, v254\n"
    "v_mul_f32 v95, v95, v254\n"
    "v_mul_f32 v104, v104, v254\n"
    "v_mul_f32 v105, v105, v254\n"
    "v_mul_f32 v106, v106, v254\n"
    "v_mul_f32 v107, v107, v254\n"
    "v_mul_f32 v108, v108, v254\n"
    "v_mul_f32 v109, v109, v254\n"
    "v_mul_f32 v110, v110, v254\n"
    "v_mul_f32 v111, v111, v254\n"
    "v_mul_f32 v120, v120, v254\n"
    "v_mul_f32 v121, v121, v254\n"
    "v_mul_f32 v122, v122, v254\n"
    "v_mul_f32 v123, v123, v254\n"
    "v_mul_f32 v124, v124, v254\n"
    "v_mul_f32 v125, v125, v254\n"
    "v_mul_f32 v126, v126, v254\n"
    "v_mul_f32 v127, v127, v254\n"
    "v_exp_f32 v168, v128\n"
    "v_exp_f32 v169, v129\n"
    "v_exp_f32 v170, v130\n"
    "v_exp_f32 v171, v131\n"
    "v_cvt_pk_bf16_f32 v160, v168, v169\n"
    "v_cvt_pk_bf16_f32 v161, v170, v171\n"
    "v_exp_f32 v172, v132\n"
    "v_exp_f32 v173, v133\n"
    "v_exp_f32 v174, v134\n"
    "v_exp_f32 v175, v135\n"
    "v_cvt_pk_bf16_f32 v164, v172, v173\n"
    "v_cvt_pk_bf16_f32 v165, v174, v175\n"
    "v_exp_f32 v233, v136\n"
    "v_exp_f32 v234, v137\n"
    "v_exp_f32 v235, v138\n"
    "v_exp_f32 v236, v139\n"
    "v_cvt_pk_bf16_f32 v162, v233, v234\n"
    "v_cvt_pk_bf16_f32 v163, v235, v236\n"
    "v_exp_f32 v237, v140\n"
    "v_exp_f32 v238, v141\n"
    "v_exp_f32 v239, v142\n"
    "v_exp_f32 v240, v143\n"
    "v_cvt_pk_bf16_f32 v166, v237, v238\n"
    "v_cvt_pk_bf16_f32 v167, v239, v240\n"
    "s_nop 3\n"
    "s_branch LB1%=\n"
    "LR2%=:\n"
    "s_nop 7\n"
    "s_nop 3\n"
    "v_max3_f32 v253, v128, v129, v130\n"
    "v_max3_f32 v252, v131, v136, v137\n"
    "v_max3_f32 v253, v253, v138, v139\n"
    "v_max_f32 v253, v253, v252\n"
    "v_mov_b32 v252, v253\n"
    "s_nop 1\n"
    "v_permlane32_swap_b32 v253, v252\n"
    "v_max_f32 v253, v253, v252\n"
    "v_mov_b32 v252, v253\n"
    "s_nop 1\n"
    "v_permlane16_swap_b32 v253, v252\n"
    "v_max_f32 v253, v253, v252\n"
    "v_add_f32 v253, 0x40e00000, v253\n"
    "v_max_f32 v253, 0, v253\n"
    "v_sub_f32 v254, 0, v253\n"
    "v_exp_f32 v254, v254\n"
    "v_sub_f32 v128, v128, v253\n"
    "v_sub_f32 v129, v129, v253\n"
    "v_sub_f32 v130, v130, v253\n"
    "v_sub_f32 v131, v131, v253\n"
    "v_sub_f32 v136, v136, v253\n"
    "v_sub_f32 v137, v137, v253\n"
    "v_sub_f32 v138, v138, v253\n"
    "v_sub_f32 v139, v139, v253\n"
    "v_sub_f32 v144, v144, v253\n"
    "v_sub_f32 v145, v145, v253\n"
    "v_sub_f32 v146, v146, v253\n"
    "v_sub_f32 v147, v147, v253\n"
    "v_mul_f32 v176, v176, v254\n"
    "v_mul_f32 v177, v177, v254\n"
    "v_mul_f32 v178, v178, v254\n"
    "v_mul_f32 v179, v179, v254\n"
    "v_mul_f32 v0, v0, v254\n"
    "v_mul_f32 v1, v1, v254\n"
    "v_mul_f32 v2, v2, v254\n"
    "v_mul_f32 v3, v3, v254\n"
    "v_mul_f32 v4, v4, v254\n"
    "v_mul_f32 v5, v5, v254\n"
    "v_mul_f32 v6, v6, v254\n"
    "v_mul_f32 v7, v7, v254\n"
    "v_mul_f32 v16, v16, v254\n"
    "v_mul_f32 v17, v17, v254\n"
    "v_mul_f32 v18, v18, v254\n"
    "v_mul_f32 v19, v19, v254\n"
    "v_mul_f32 v20, v20, v254\n"
    "v_mul_f32 v21, v21, v254\n"
    "v_mul_f32 v22, v22, v254\n"
    "v_mul_f32 v23, v23, v254\n"
    "v_mul_f32 v32, v32, v254\n"
    "v_mul_f32 v33, v33, v254\n"
    "v_mul_f32 v34, v34, v254\n"
    "v_mul_f32 v35, v35, v254\n"
    "v_mul_f32 v36, v36, v254\n"
    "v_mul_f32 v37, v37, v254\n"
    "v_mul_f32 v38, v38, v254\n"
    "v_mul_f32 v39, v39, v254\n"
    "v_mul_f32 v48, v48, v254\n"
    "v_mul_f32 v49, v49, v254\n"
    "v_mul_f32 v50, v50, v254\n"
    "v_mul_f32 v51, v51, v254\n"
    "v_mul_f32 v52, v52, v254\n"
    "v_mul_f32 v53, v53, v254\n"
    "v_mul_f32 v54, v54, v254\n"
    "v_mul_f32 v55, v55, v254\n"
    "v_mul_f32 v64, v64, v254\n"
    "v_mul_f32 v65, v65, v254\n"
    "v_mul_f32 v66, v66, v254\n"
    "v_mul_f32 v67, v67, v254\n"
    "v_mul_f32 v68, v68, v254\n"
    "v_mul_f32 v69, v69, v254\n"
    "v_mul_f32 v70, v70, v254\n"
    "v_mul_f32 v71, v71, v254\n"
    "v_mul_f32 v80, v80, v254\n"
    "v_mul_f32 v81, v81, v254\n"
    "v_mul_f32 v82, v82, v254\n"
    "v_mul_f32 v83, v83, v254\n"
    "v_mul_f32 v84, v84, v254\n"
    "v_mul_f32 v85, v85, v254\n"
    "v_mul_f32 v86, v86, v254\n"
    "v_mul_f32 v87, v87, v254\n"
    "v_mul_f32 v96, v96, v254\n"
    "v_mul_f32 v97, v97, v254\n"
    "v_mul_f32 v98, v98, v254\n"
    "v_mul_f32 v99, v99, v254\n"
    "v_mul_f32 v100, v100, v254\n"
    "v_mul_f32 v101, v101, v254\n"
    "v_mul_f32 v102, v102, v254\n"
    "v_mul_f32 v103, v103, v254\n"
    "v_mul_f32 v112, v112, v254\n"
    "v_mul_f32 v113, v113, v254\n"
    "v_mul_f32 v114, v114, v254\n"
    "v_mul_f32 v115, v115, v254\n"
    "v_mul_f32 v116, v116, v254\n"
    "v_mul_f32 v117, v117, v254\n"
    "v_mul_f32 v118, v118, v254\n"
    "v_mul_f32 v119, v119, v254\n"
    "v_max3_f32 v253, v132, v133, v134\n"
    "v_max3_f32 v252, v135, v140, v141\n"
    "v_max3_f32 v253, v253, v142, v143\n"
    "v_max_f32 v253, v253, v252\n"
    "v_mov_b32 v252, v253\n"
    "s_nop 1\n"
    "v_permlane32_swap_b32 v253, v252\n"
    "v_max_f32 v253, v253, v252\n"
    "v_mov_b32 v252, v253\n"
    "s_nop 1\n"
    "v_permlane16_swap_b32 v253, v252\n"
    "v_max_f32 v253, v253, v252\n"
    "v_add_f32 v253, 0x40e00000, v253\n"
    "v_max_f32 v253, 0, v253\n"
    "v_sub_f32 v254, 0, v253\n"
    "v_exp_f32 v254, v254\n"
    "v_sub_f32 v132, v132, v253\n"
    "v_sub_f32 v133, v133, v253\n"
    "v_sub_f32 v134, v134, v253\n"
    "v_sub_f32 v135, v135, v253\n"
    "v_sub_f32 v140, v140, v253\n"
    "v_sub_f32 v141, v141, v253\n"
    "v_sub_f32 v142, v142, v253\n"
    "v_sub_f32 v143, v143, v253\n"
    "v_sub_f32 v148, v148, v253\n"
    "v_sub_f32 v149, v149, v253\n"
    "v_sub_f32 v150, v150, v253\n"
    "v_sub_f32 v151, v151, v253\n"
    "v_mul_f32 v180, v180, v254\n"
    "v_mul_f32 v181, v181, v254\n"
    "v_mul_f32 v182, v182, v254\n"
    "v_mul_f32 v183, v183, v254\n"
    "v_mul_f32 v8, v8, v254\n"
    "v_mul_f32 v9, v9, v254\n"
    "v_mul_f32 v10, v10, v254\n"
    "v_mul_f32 v11, v11, v254\n"
    "v_mul_f32 v12, v12, v254\n"
    "v_mul_f32 v13, v13, v254\n"
    "v_mul_f32 v14, v14, v254\n"
    "v_mul_f32 v15, v15, v254\n"
    "v_mul_f32 v24, v24, v254\n"
    "v_mul_f32 v25, v25, v254\n"
    "v_mul_f32 v26, v26, v254\n"
    "v_mul_f32 v27, v27, v254\n"
    "v_mul_f32 v28, v28, v254\n"
    "v_mul_f32 v29, v29, v254\n"
    "v_mul_f32 v30, v30, v254\n"
    "v_mul_f32 v31, v31, v254\n"
    "v_mul_f32 v40, v40, v254\n"
    "v_mul_f32 v41, v41, v254\n"
    "v_mul_f32 v42, v42, v254\n"
    "v_mul_f32 v43, v43, v254\n"
    "v_mul_f32 v44, v44, v254\n"
    "v_mul_f32 v45, v45, v254\n"
    "v_mul_f32 v46, v46, v254\n"
    "v_mul_f32 v47, v47, v254\n"
    "v_mul_f32 v56, v56, v254\n"
    "v_mul_f32 v57, v57, v254\n"
    "v_mul_f32 v58, v58, v254\n"
    "v_mul_f32 v59, v59, v254\n"
    "v_mul_f32 v60, v60, v254\n"
    "v_mul_f32 v61, v61, v254\n"
    "v_mul_f32 v62, v62, v254\n"
    "v_mul_f32 v63, v63, v254\n"
    "v_mul_f32 v72, v72, v254\n"
    "v_mul_f32 v73, v73, v254\n"
    "v_mul_f32 v74, v74, v254\n"
    "v_mul_f32 v75, v75, v254\n"
    "v_mul_f32 v76, v76, v254\n"
    "v_mul_f32 v77, v77, v254\n"
    "v_mul_f32 v78, v78, v254\n"
    "v_mul_f32 v79, v79, v254\n"
    "v_mul_f32 v88, v88, v254\n"
    "v_mul_f32 v89, v89, v254\n"
    "v_mul_f32 v90, v90, v254\n"
    "v_mul_f32 v91, v91, v254\n"
    "v_mul_f32 v92, v92, v254\n"
    "v_mul_f32 v93, v93, v254\n"
    "v_mul_f32 v94, v94, v254\n"
    "v_mul_f32 v95, v95, v254\n"
    "v_mul_f32 v104, v104, v254\n"
    "v_mul_f32 v105, v105, v254\n"
    "v_mul_f32 v106, v106, v254\n"
    "v_mul_f32 v107, v107, v254\n"
    "v_mul_f32 v108, v108, v254\n"
    "v_mul_f32 v109, v109, v254\n"
    "v_mul_f32 v110, v110, v254\n"
    "v_mul_f32 v111, v111, v254\n"
    "v_mul_f32 v120, v120, v254\n"
    "v_mul_f32 v121, v121, v254\n"
    "v_mul_f32 v122, v122, v254\n"
    "v_mul_f32 v123, v123, v254\n"
    "v_mul_f32 v124, v124, v254\n"
    "v_mul_f32 v125, v125, v254\n"
    "v_mul_f32 v126, v126, v254\n"
    "v_mul_f32 v127, v127, v254\n"
    "v_exp_f32 v168, v128\n"
    "v_exp_f32 v169, v129\n"
    "v_exp_f32 v170, v130\n"
    "v_exp_f32 v171, v131\n"
    "v_cvt_pk_bf16_f32 v152, v168, v169\n"
    "v_cvt_pk_bf16_f32 v153, v170, v171\n"
    "v_exp_f32 v172, v132\n"
    "v_exp_f32 v173, v133\n"
    "v_exp_f32 v174, v134\n"
    "v_exp_f32 v175, v135\n"
    "v_cvt_pk_bf16_f32 v156, v172, v173\n"
    "v_cvt_pk_bf16_f32 v157, v174, v175\n"
    "v_exp_f32 v233, v136\n"
    "v_exp_f32 v234, v137\n"
    "v_exp_f32 v235, v138\n"
    "v_exp_f32 v236, v139\n"
    "v_cvt_pk_bf16_f32 v154, v233, v234\n"
    "v_cvt_pk_bf16_f32 v155, v235, v236\n"
    "v_exp_f32 v237, v140\n"
    "v_exp_f32 v238, v141\n"
    "v_exp_f32 v239, v142\n"
    "v_exp_f32 v240, v143\n"
    "v_cvt_pk_bf16_f32 v158, v237, v238\n"
    "v_cvt_pk_bf16_f32 v159, v239, v240\n"
    "s_nop 3\n"
    "s_branch LB2%=\n"
    "LR3%=:\n"
    "s_nop 7\n"
    "s_nop 3\n"
    "v_max3_f32 v253, v128, v129, v130\n"
    "v_max3_f32 v252, v131, v136, v137\n"
    "v_max3_f32 v253, v253, v138, v139\n"
    "v_max_f32 v253, v253, v252\n"
    "v_mov_b32 v252, v253\n"
    "s_nop 1\n"
    "v_permlane32_swap_b32 v253, v252\n"
    "v_max_f32 v253, v253, v252\n"
    "v_mov_b32 v252, v253\n"
    "s_nop 1\n"
    "v_permlane16_swap_b32 v253, v252\n"
    "v_max_f32 v253, v253, v252\n"
    "v_add_f32 v253, 0x40e00000, v253\n"
    "v_max_f32 v253, 0, v253\n"
    "v_sub_f32 v254, 0, v253\n"
    "v_exp_f32 v254, v254\n"
    "v_sub_f32 v128, v128, v253\n"
    "v_sub_f32 v129, v129, v253\n"
    "v_sub_f32 v130, v130, v253\n"
    "v_sub_f32 v131, v131, v253\n"
    "v_sub_f32 v136, v136, v253\n"
    "v_sub_f32 v137, v137, v253\n"
    "v_sub_f32 v138, v138, v253\n"
    "v_sub_f32 v139, v139, v253\n"
    "v_sub_f32 v144, v144, v253\n"
    "v_sub_f32 v145, v145, v253\n"
    "v_sub_f32 v146, v146, v253\n"
    "v_sub_f32 v147, v147, v253\n"
    "v_mul_f32 v176, v176, v254\n"
    "v_mul_f32 v177, v177, v254\n"
    "v_mul_f32 v178, v178, v254\n"
    "v_mul_f32 v179, v179, v254\n"
    "v_mul_f32 v0, v0, v254\n"
    "v_mul_f32 v1, v1, v254\n"
    "v_mul_f32 v2, v2, v254\n"
    "v_mul_f32 v3, v3, v254\n"
    "v_mul_f32 v4, v4, v254\n"
    "v_mul_f32 v5, v5, v254\n"
    "v_mul_f32 v6, v6, v254\n"
    "v_mul_f32 v7, v7, v254\n"
    "v_mul_f32 v16, v16, v254\n"
    "v_mul_f32 v17, v17, v254\n"
    "v_mul_f32 v18, v18, v254\n"
    "v_mul_f32 v19, v19, v254\n"
    "v_mul_f32 v20, v20, v254\n"
    "v_mul_f32 v21, v21, v254\n"
    "v_mul_f32 v22, v22, v254\n"
    "v_mul_f32 v23, v23, v254\n"
    "v_mul_f32 v32, v32, v254\n"
    "v_mul_f32 v33, v33, v254\n"
    "v_mul_f32 v34, v34, v254\n"
    "v_mul_f32 v35, v35, v254\n"
    "v_mul_f32 v36, v36, v254\n"
    "v_mul_f32 v37, v37, v254\n"
    "v_mul_f32 v38, v38, v254\n"
    "v_mul_f32 v39, v39, v254\n"
    "v_mul_f32 v48, v48, v254\n"
    "v_mul_f32 v49, v49, v254\n"
    "v_mul_f32 v50, v50, v254\n"
    "v_mul_f32 v51, v51, v254\n"
    "v_mul_f32 v52, v52, v254\n"
    "v_mul_f32 v53, v53, v254\n"
    "v_mul_f32 v54, v54, v254\n"
    "v_mul_f32 v55, v55, v254\n"
    "v_mul_f32 v64, v64, v254\n"
    "v_mul_f32 v65, v65, v254\n"
    "v_mul_f32 v66, v66, v254\n"
    "v_mul_f32 v67, v67, v254\n"
    "v_mul_f32 v68, v68, v254\n"
    "v_mul_f32 v69, v69, v254\n"
    "v_mul_f32 v70, v70, v254\n"
    "v_mul_f32 v71, v71, v254\n"
    "v_mul_f32 v80, v80, v254\n"
    "v_mul_f32 v81, v81, v254\n"
    "v_mul_f32 v82, v82, v254\n"
    "v_mul_f32 v83, v83, v254\n"
    "v_mul_f32 v84, v84, v254\n"
    "v_mul_f32 v85, v85, v254\n"
    "v_mul_f32 v86, v86, v254\n"
    "v_mul_f32 v87, v87, v254\n"
    "v_mul_f32 v96, v96, v254\n"
    "v_mul_f32 v97, v97, v254\n"
    "v_mul_f32 v98, v98, v254\n"
    "v_mul_f32 v99, v99, v254\n"
    "v_mul_f32 v100, v100, v254\n"
    "v_mul_f32 v101, v101, v254\n"
    "v_mul_f32 v102, v102, v254\n"
    "v_mul_f32 v103, v103, v254\n"
    "v_mul_f32 v112, v112, v254\n"
    "v_mul_f32 v113, v113, v254\n"
    "v_mul_f32 v114, v114, v254\n"
    "v_mul_f32 v115, v115, v254\n"
    "v_mul_f32 v116, v116, v254\n"
    "v_mul_f32 v117, v117, v254\n"
    "v_mul_f32 v118, v118, v254\n"
    "v_mul_f32 v119, v119, v254\n"
    "v_max3_f32 v253, v132, v133, v134\n"
    "v_max3_f32 v252, v135, v140, v141\n"
    "v_max3_f32 v253, v253, v142, v143\n"
    "v_max_f32 v253, v253, v252\n"
    "v_mov_b32 v252, v253\n"
    "s_nop 1\n"
    "v_permlane32_swap_b32 v253, v252\n"
    "v_max_f32 v253, v253, v252\n"
    "v_mov_b32 v252, v253\n"
    "s_nop 1\n"
    "v_permlane16_swap_b32 v253, v252\n"
    "v_max_f32 v253, v253, v252\n"
    "v_add_f32 v253, 0x40e00000, v253\n"
    "v_max_f32 v253, 0, v253\n"
    "v_sub_f32 v254, 0, v253\n"
    "v_exp_f32 v254, v254\n"
    "v_sub_f32 v132, v132, v253\n"
    "v_sub_f32 v133, v133, v253\n"
    "v_sub_f32 v134, v134, v253\n"
    "v_sub_f32 v135, v135, v253\n"
    "v_sub_f32 v140, v140, v253\n"
    "v_sub_f32 v141, v141, v253\n"
    "v_sub_f32 v142, v142, v253\n"
    "v_sub_f32 v143, v143, v253\n"
    "v_sub_f32 v148, v148, v253\n"
    "v_sub_f32 v149, v149, v253\n"
    "v_sub_f32 v150, v150, v253\n"
    "v_sub_f32 v151, v151, v253\n"
    "v_mul_f32 v180, v180, v254\n"
    "v_mul_f32 v181, v181, v254\n"
    "v_mul_f32 v182, v182, v254\n"
    "v_mul_f32 v183, v183, v254\n"
    "v_mul_f32 v8, v8, v254\n"
    "v_mul_f32 v9, v9, v254\n"
    "v_mul_f32 v10, v10, v254\n"
    "v_mul_f32 v11, v11, v254\n"
    "v_mul_f32 v12, v12, v254\n"
    "v_mul_f32 v13, v13, v254\n"
    "v_mul_f32 v14, v14, v254\n"
    "v_mul_f32 v15, v15, v254\n"
    "v_mul_f32 v24, v24, v254\n"
    "v_mul_f32 v25, v25, v254\n"
    "v_mul_f32 v26, v26, v254\n"
    "v_mul_f32 v27, v27, v254\n"
    "v_mul_f32 v28, v28, v254\n"
    "v_mul_f32 v29, v29, v254\n"
    "v_mul_f32 v30, v30, v254\n"
    "v_mul_f32 v31, v31, v254\n"
    "v_mul_f32 v40, v40, v254\n"
    "v_mul_f32 v41, v41, v254\n"
    "v_mul_f32 v42, v42, v254\n"
    "v_mul_f32 v43, v43, v254\n"
    "v_mul_f32 v44, v44, v254\n"
    "v_mul_f32 v45, v45, v254\n"
    "v_mul_f32 v46, v46, v254\n"
    "v_mul_f32 v47, v47, v254\n"
    "v_mul_f32 v56, v56, v254\n"
    "v_mul_f32 v57, v57, v254\n"
    "v_mul_f32 v58, v58, v254\n"
    "v_mul_f32 v59, v59, v254\n"
    "v_mul_f32 v60, v60, v254\n"
    "v_mul_f32 v61, v61, v254\n"
    "v_mul_f32 v62, v62, v254\n"
    "v_mul_f32 v63, v63, v254\n"
    "v_mul_f32 v72, v72, v254\n"
    "v_mul_f32 v73, v73, v254\n"
    "v_mul_f32 v74, v74, v254\n"
    "v_mul_f32 v75, v75, v254\n"
    "v_mul_f32 v76, v76, v254\n"
    "v_mul_f32 v77, v77, v254\n"
    "v_mul_f32 v78, v78, v254\n"
    "v_mul_f32 v79, v79, v254\n"
    "v_mul_f32 v88, v88, v254\n"
    "v_mul_f32 v89, v89, v254\n"
    "v_mul_f32 v90, v90, v254\n"
    "v_mul_f32 v91, v91, v254\n"
    "v_mul_f32 v92, v92, v254\n"
    "v_mul_f32 v93, v93, v254\n"
    "v_mul_f32 v94, v94, v254\n"
    "v_mul_f32 v95, v95, v254\n"
    "v_mul_f32 v104, v104, v254\n"
    "v_mul_f32 v105, v105, v254\n"
    "v_mul_f32 v106, v106, v254\n"
    "v_mul_f32 v107, v107, v254\n"
    "v_mul_f32 v108, v108, v254\n"
    "v_mul_f32 v109, v109, v254\n"
    "v_mul_f32 v110, v110, v254\n"
    "v_mul_f32 v111, v111, v254\n"
    "v_mul_f32 v120, v120, v254\n"
    "v_mul_f32 v121, v121, v254\n"
    "v_mul_f32 v122, v122, v254\n"
    "v_mul_f32 v123, v123, v254\n"
    "v_mul_f32 v124, v124, v254\n"
    "v_mul_f32 v125, v125, v254\n"
    "v_mul_f32 v126, v126, v254\n"
    "v_mul_f32 v127, v127, v254\n"
    "v_exp_f32 v168, v128\n"
    "v_exp_f32 v169, v129\n"
    "v_exp_f32 v170, v130\n"
    "v_exp_f32 v171, v131\n"
    "v_cvt_pk_bf16_f32 v160, v168, v169\n"
    "v_cvt_pk_bf16_f32 v161, v170, v171\n"
    "v_exp_f32 v172, v132\n"
    "v_exp_f32 v173, v133\n"
    "v_exp_f32 v174, v134\n"
    "v_exp_f32 v175, v135\n"
    "v_cvt_pk_bf16_f32 v164, v172, v173\n"
    "v_cvt_pk_bf16_f32 v165, v174, v175\n"
    "v_exp_f32 v233, v136\n"
    "v_exp_f32 v234, v137\n"
    "v_exp_f32 v235, v138\n"
    "v_exp_f32 v236, v139\n"
    "v_cvt_pk_bf16_f32 v162, v233, v234\n"
    "v_cvt_pk_bf16_f32 v163, v235, v236\n"
    "v_exp_f32 v237, v140\n"
    "v_exp_f32 v238, v141\n"
    "v_exp_f32 v239, v142\n"
    "v_exp_f32 v240, v143\n"
    "v_cvt_pk_bf16_f32 v166, v237, v238\n"
    "v_cvt_pk_bf16_f32 v167, v239, v240\n"
    "s_nop 3\n"
    "s_branch LB3%=\n"
    "LRL0%=:\n"
    "s_nop 7\n"
    "s_nop 3\n"
    "v_max3_f32 v253, v128, v129, v130\n"
    "v_max3_f32 v252, v131, v136, v137\n"
    "v_max3_f32 v253, v253, v138, v139\n"
    "v_max_f32 v253, v253, v252\n"
    "v_mov_b32 v252, v253\n"
    "s_nop 1\n"
    "v_permlane32_swap_b32 v253, v252\n"
    "v_max_f32 v253, v253, v252\n"
    "v_mov_b32 v252, v253\n"
    "s_nop 1\n"
    "v_permlane16_swap_b32 v253, v252\n"
    "v_max_f32 v253, v253, v252\n"
    "v_add_f32 v253, 0x40e00000, v253\n"
    "v_max_f32 v253, 0, v253\n"
    "v_sub_f32 v254, 0, v253\n"
    "v_exp_f32 v254, v254\n"
    "v_sub_f32 v128, v128, v253\n"
    "v_sub_f32 v129, v129, v253\n"
    "v_sub_f32 v130, v130, v253\n"
    "v_sub_f32 v131, v131, v253\n"
    "v_sub_f32 v136, v136, v253\n"
    "v_sub_f32 v137, v137, v253\n"
    "v_sub_f32 v138, v138, v253\n"
    "v_sub_f32 v139, v139, v253\n"
    "v_sub_f32 v144, v144, v253\n"
    "v_sub_f32 v145, v145, v253\n"
    "v_sub_f32 v146, v146, v253\n"
    "v_sub_f32 v147, v147, v253\n"
    "v_mul_f32 v176, v176, v254\n"
    "v_mul_f32 v177, v177, v254\n"
    "v_mul_f32 v178, v178, v254\n"
    "v_mul_f32 v179, v179, v254\n"
    "v_mul_f32 v0, v0, v254\n"
    "v_mul_f32 v1, v1, v254\n"
    "v_mul_f32 v2, v2, v254\n"
    "v_mul_f32 v3, v3, v254\n"
    "v_mul_f32 v4, v4, v254\n"
    "v_mul_f32 v5, v5, v254\n"
    "v_mul_f32 v6, v6, v254\n"
    "v_mul_f32 v7, v7, v254\n"
    "v_mul_f32 v16, v16, v254\n"
    "v_mul_f32 v17, v17, v254\n"
    "v_mul_f32 v18, v18, v254\n"
    "v_mul_f32 v19, v19, v254\n"
    "v_mul_f32 v20, v20, v254\n"
    "v_mul_f32 v21, v21, v254\n"
    "v_mul_f32 v22, v22, v254\n"
    "v_mul_f32 v23, v23, v254\n"
    "v_mul_f32 v32, v32, v254\n"
    "v_mul_f32 v33, v33, v254\n"
    "v_mul_f32 v34, v34, v254\n"
    "v_mul_f32 v35, v35, v254\n"
    "v_mul_f32 v36, v36, v254\n"
    "v_mul_f32 v37, v37, v254\n"
    "v_mul_f32 v38, v38, v254\n"
    "v_mul_f32 v39, v39, v254\n"
    "v_mul_f32 v48, v48, v254\n"
    "v_mul_f32 v49, v49, v254\n"
    "v_mul_f32 v50, v50, v254\n"
    "v_mul_f32 v51, v51, v254\n"
    "v_mul_f32 v52, v52, v254\n"
    "v_mul_f32 v53, v53, v254\n"
    "v_mul_f32 v54, v54, v254\n"
    "v_mul_f32 v55, v55, v254\n"
    "v_mul_f32 v64, v64, v254\n"
    "v_mul_f32 v65, v65, v254\n"
    "v_mul_f32 v66, v66, v254\n"
    "v_mul_f32 v67, v67, v254\n"
    "v_mul_f32 v68, v68, v254\n"
    "v_mul_f32 v69, v69, v254\n"
    "v_mul_f32 v70, v70, v254\n"
    "v_mul_f32 v71, v71, v254\n"
    "v_mul_f32 v80, v80, v254\n"
    "v_mul_f32 v81, v81, v254\n"
    "v_mul_f32 v82, v82, v254\n"
    "v_mul_f32 v83, v83, v254\n"
    "v_mul_f32 v84, v84, v254\n"
    "v_mul_f32 v85, v85, v254\n"
    "v_mul_f32 v86, v86, v254\n"
    "v_mul_f32 v87, v87, v254\n"
    "v_mul_f32 v96, v96, v254\n"
    "v_mul_f32 v97, v97, v254\n"
    "v_mul_f32 v98, v98, v254\n"
    "v_mul_f32 v99, v99, v254\n"
    "v_mul_f32 v100, v100, v254\n"
    "v_mul_f32 v101, v101, v254\n"
    "v_mul_f32 v102, v102, v254\n"
    "v_mul_f32 v103, v103, v254\n"
    "v_mul_f32 v112, v112, v254\n"
    "v_mul_f32 v113, v113, v254\n"
    "v_mul_f32 v114, v114, v254\n"
    "v_mul_f32 v115, v115, v254\n"
    "v_mul_f32 v116, v116, v254\n"
    "v_mul_f32 v117, v117, v254\n"
    "v_mul_f32 v118, v118, v254\n"
    "v_mul_f32 v119, v119, v254\n"
    "v_max3_f32 v253, v132, v133, v134\n"
    "v_max3_f32 v252, v135, v140, v141\n"
    "v_max3_f32 v253, v253, v142, v143\n"
    "v_max_f32 v253, v253, v252\n"
    "v_mov_b32 v252, v253\n"
    "s_nop 1\n"
    "v_permlane32_swap_b32 v253, v252\n"
    "v_max_f32 v253, v253, v252\n"
    "v_mov_b32 v252, v253\n"
    "s_nop 1\n"
    "v_permlane16_swap_b32 v253, v252\n"
    "v_max_f32 v253, v253, v252\n"
    "v_add_f32 v253, 0x40e00000, v253\n"
    "v_max_f32 v253, 0, v253\n"
    "v_sub_f32 v254, 0, v253\n"
    "v_exp_f32 v254, v254\n"
    "v_sub_f32 v132, v132, v253\n"
    "v_sub_f32 v133, v133, v253\n"
    "v_sub_f32 v134, v134, v253\n"
    "v_sub_f32 v135, v135, v253\n"
    "v_sub_f32 v140, v140, v253\n"
    "v_sub_f32 v141, v141, v253\n"
    "v_sub_f32 v142, v142, v253\n"
    "v_sub_f32 v143, v143, v253\n"
    "v_sub_f32 v148, v148, v253\n"
    "v_sub_f32 v149, v149, v253\n"
    "v_sub_f32 v150, v150, v253\n"
    "v_sub_f32 v151, v151, v253\n"
    "v_mul_f32 v180, v180, v254\n"
    "v_mul_f32 v181, v181, v254\n"
    "v_mul_f32 v182, v182, v254\n"
    "v_mul_f32 v183, v183, v254\n"
    "v_mul_f32 v8, v8, v254\n"
    "v_mul_f32 v9, v9, v254\n"
    "v_mul_f32 v10, v10, v254\n"
    "v_mul_f32 v11, v11, v254\n"
    "v_mul_f32 v12, v12, v254\n"
    "v_mul_f32 v13, v13, v254\n"
    "v_mul_f32 v14, v14, v254\n"
    "v_mul_f32 v15, v15, v254\n"
    "v_mul_f32 v24, v24, v254\n"
    "v_mul_f32 v25, v25, v254\n"
    "v_mul_f32 v26, v26, v254\n"
    "v_mul_f32 v27, v27, v254\n"
    "v_mul_f32 v28, v28, v254\n"
    "v_mul_f32 v29, v29, v254\n"
    "v_mul_f32 v30, v30, v254\n"
    "v_mul_f32 v31, v31, v254\n"
    "v_mul_f32 v40, v40, v254\n"
    "v_mul_f32 v41, v41, v254\n"
    "v_mul_f32 v42, v42, v254\n"
    "v_mul_f32 v43, v43, v254\n"
    "v_mul_f32 v44, v44, v254\n"
    "v_mul_f32 v45, v45, v254\n"
    "v_mul_f32 v46, v46, v254\n"
    "v_mul_f32 v47, v47, v254\n"
    "v_mul_f32 v56, v56, v254\n"
    "v_mul_f32 v57, v57, v254\n"
    "v_mul_f32 v58, v58, v254\n"
    "v_mul_f32 v59, v59, v254\n"
    "v_mul_f32 v60, v60, v254\n"
    "v_mul_f32 v61, v61, v254\n"
    "v_mul_f32 v62, v62, v254\n"
    "v_mul_f32 v63, v63, v254\n"
    "v_mul_f32 v72, v72, v254\n"
    "v_mul_f32 v73, v73, v254\n"
    "v_mul_f32 v74, v74, v254\n"
    "v_mul_f32 v75, v75, v254\n"
    "v_mul_f32 v76, v76, v254\n"
    "v_mul_f32 v77, v77, v254\n"
    "v_mul_f32 v78, v78, v254\n"
    "v_mul_f32 v79, v79, v254\n"
    "v_mul_f32 v88, v88, v254\n"
    "v_mul_f32 v89, v89, v254\n"
    "v_mul_f32 v90, v90, v254\n"
    "v_mul_f32 v91, v91, v254\n"
    "v_mul_f32 v92, v92, v254\n"
    "v_mul_f32 v93, v93, v254\n"
    "v_mul_f32 v94, v94, v254\n"
    "v_mul_f32 v95, v95, v254\n"
    "v_mul_f32 v104, v104, v254\n"
    "v_mul_f32 v105, v105, v254\n"
    "v_mul_f32 v106, v106, v254\n"
    "v_mul_f32 v107, v107, v254\n"
    "v_mul_f32 v108, v108, v254\n"
    "v_mul_f32 v109, v109, v254\n"
    "v_mul_f32 v110, v110, v254\n"
    "v_mul_f32 v111, v111, v254\n"
    "v_mul_f32 v120, v120, v254\n"
    "v_mul_f32 v121, v121, v254\n"
    "v_mul_f32 v122, v122, v254\n"
    "v_mul_f32 v123, v123, v254\n"
    "v_mul_f32 v124, v124, v254\n"
    "v_mul_f32 v125, v125, v254\n"
    "v_mul_f32 v126, v126, v254\n"
    "v_mul_f32 v127, v127, v254\n"
    "v_exp_f32 v168, v128\n"
    "v_exp_f32 v169, v129\n"
    "v_exp_f32 v170, v130\n"
    "v_exp_f32 v171, v131\n"
    "v_cvt_pk_bf16_f32 v152, v168, v169\n"
    "v_cvt_pk_bf16_f32 v153, v170, v171\n"
    "v_exp_f32 v172, v132\n"
    "v_exp_f32 v173, v133\n"
    "v_exp_f32 v174, v134\n"
    "v_exp_f32 v175, v135\n"
    "v_cvt_pk_bf16_f32 v156, v172, v173\n"
    "v_cvt_pk_bf16_f32 v157, v174, v175\n"
    "v_exp_f32 v233, v136\n"
    "v_exp_f32 v234, v137\n"
    "v_exp_f32 v235, v138\n"
    "v_exp_f32 v236, v139\n"
    "v_cvt_pk_bf16_f32 v154, v233, v234\n"
    "v_cvt_pk_bf16_f32 v155, v235, v236\n"
    "v_exp_f32 v237, v140\n"
    "v_exp_f32 v238, v141\n"
    "v_exp_f32 v239, v142\n"
    "v_exp_f32 v240, v143\n"
    "v_cvt_pk_bf16_f32 v158, v237, v238\n"
    "v_cvt_pk_bf16_f32 v159, v239, v240\n"
    "s_nop 3\n"
    "s_branch LBL0%=\n"
    "LRL1%=:\n"
    "s_nop 7\n"
    "s_nop 3\n"
    "v_max3_f32 v253, v128, v129, v130\n"
    "v_max3_f32 v252, v131, v136, v137\n"
    "v_max3_f32 v253, v253, v138, v139\n"
    "v_max_f32 v253, v253, v252\n"
    "v_mov_b32 v252, v253\n"
    "s_nop 1\n"
    "v_permlane32_swap_b32 v253, v252\n"
    "v_max_f32 v253, v253, v252\n"
    "v_mov_b32 v252, v253\n"
    "s_nop 1\n"
    "v_permlane16_swap_b32 v253, v252\n"
    "v_max_f32 v253, v253, v252\n"
    "v_add_f32 v253, 0x40e00000, v253\n"
    "v_max_f32 v253, 0, v253\n"
    "v_sub_f32 v254, 0, v253\n"
    "v_exp_f32 v254, v254\n"
    "v_sub_f32 v128, v128, v253\n"
    "v_sub_f32 v129, v129, v253\n"
    "v_sub_f32 v130, v130, v253\n"
    "v_sub_f32 v131, v131, v253\n"
    "v_sub_f32 v136, v136, v253\n"
    "v_sub_f32 v137, v137, v253\n"
    "v_sub_f32 v138, v138, v253\n"
    "v_sub_f32 v139, v139, v253\n"
    "v_sub_f32 v144, v144, v253\n"
    "v_sub_f32 v145, v145, v253\n"
    "v_sub_f32 v146, v146, v253\n"
    "v_sub_f32 v147, v147, v253\n"
    "v_mul_f32 v176, v176, v254\n"
    "v_mul_f32 v177, v177, v254\n"
    "v_mul_f32 v178, v178, v254\n"
    "v_mul_f32 v179, v179, v254\n"
    "v_mul_f32 v0, v0, v254\n"
    "v_mul_f32 v1, v1, v254\n"
    "v_mul_f32 v2, v2, v254\n"
    "v_mul_f32 v3, v3, v254\n"
    "v_mul_f32 v4, v4, v254\n"
    "v_mul_f32 v5, v5, v254\n"
    "v_mul_f32 v6, v6, v254\n"
    "v_mul_f32 v7, v7, v254\n"
    "v_mul_f32 v16, v16, v254\n"
    "v_mul_f32 v17, v17, v254\n"
    "v_mul_f32 v18, v18, v254\n"
    "v_mul_f32 v19, v19, v254\n"
    "v_mul_f32 v20, v20, v254\n"
    "v_mul_f32 v21, v21, v254\n"
    "v_mul_f32 v22, v22, v254\n"
    "v_mul_f32 v23, v23, v254\n"
    "v_mul_f32 v32, v32, v254\n"
    "v_mul_f32 v33, v33, v254\n"
    "v_mul_f32 v34, v34, v254\n"
    "v_mul_f32 v35, v35, v254\n"
    "v_mul_f32 v36, v36, v254\n"
    "v_mul_f32 v37, v37, v254\n"
    "v_mul_f32 v38, v38, v254\n"
    "v_mul_f32 v39, v39, v254\n"
    "v_mul_f32 v48, v48, v254\n"
    "v_mul_f32 v49, v49, v254\n"
    "v_mul_f32 v50, v50, v254\n"
    "v_mul_f32 v51, v51, v254\n"
    "v_mul_f32 v52, v52, v254\n"
    "v_mul_f32 v53, v53, v254\n"
    "v_mul_f32 v54, v54, v254\n"
    "v_mul_f32 v55, v55, v254\n"
    "v_mul_f32 v64, v64, v254\n"
    "v_mul_f32 v65, v65, v254\n"
    "v_mul_f32 v66, v66, v254\n"
    "v_mul_f32 v67, v67, v254\n"
    "v_mul_f32 v68, v68, v254\n"
    "v_mul_f32 v69, v69, v254\n"
    "v_mul_f32 v70, v70, v254\n"
    "v_mul_f32 v71, v71, v254\n"
    "v_mul_f32 v80, v80, v254\n"
    "v_mul_f32 v81, v81, v254\n"
    "v_mul_f32 v82, v82, v254\n"
    "v_mul_f32 v83, v83, v254\n"
    "v_mul_f32 v84, v84, v254\n"
    "v_mul_f32 v85, v85, v254\n"
    "v_mul_f32 v86, v86, v254\n"
    "v_mul_f32 v87, v87, v254\n"
    "v_mul_f32 v96, v96, v254\n"
    "v_mul_f32 v97, v97, v254\n"
    "v_mul_f32 v98, v98, v254\n"
    "v_mul_f32 v99, v99, v254\n"
    "v_mul_f32 v100, v100, v254\n"
    "v_mul_f32 v101, v101, v254\n"
    "v_mul_f32 v102, v102, v254\n"
    "v_mul_f32 v103, v103, v254\n"
    "v_mul_f32 v112, v112, v254\n"
    "v_mul_f32 v113, v113, v254\n"
    "v_mul_f32 v114, v114, v254\n"
    "v_mul_f32 v115, v115, v254\n"
    "v_mul_f32 v116, v116, v254\n"
    "v_mul_f32 v117, v117, v254\n"
    "v_mul_f32 v118, v118, v254\n"
    "v_mul_f32 v119, v119, v254\n"
    "v_max3_f32 v253, v132, v133, v134\n"
    "v_max3_f32 v252, v135, v140, v141\n"
    "v_max3_f32 v253, v253, v142, v143\n"
    "v_max_f32 v253, v253, v252\n"
    "v_mov_b32 v252, v253\n"
    "s_nop 1\n"
    "v_permlane32_swap_b32 v253, v252\n"
    "v_max_f32 v253, v253, v252\n"
    "v_mov_b32 v252, v253\n"
    "s_nop 1\n"
    "v_permlane16_swap_b32 v253, v252\n"
    "v_max_f32 v253, v253, v252\n"
    "v_add_f32 v253, 0x40e00000, v253\n"
    "v_max_f32 v253, 0, v253\n"
    "v_sub_f32 v254, 0, v253\n"
    "v_exp_f32 v254, v254\n"
    "v_sub_f32 v132, v132, v253\n"
    "v_sub_f32 v133, v133, v253\n"
    "v_sub_f32 v134, v134, v253\n"
    "v_sub_f32 v135, v135, v253\n"
    "v_sub_f32 v140, v140, v253\n"
    "v_sub_f32 v141, v141, v253\n"
    "v_sub_f32 v142, v142, v253\n"
    "v_sub_f32 v143, v143, v253\n"
    "v_sub_f32 v148, v148, v253\n"
    "v_sub_f32 v149, v149, v253\n"
    "v_sub_f32 v150, v150, v253\n"
    "v_sub_f32 v151, v151, v253\n"
    "v_mul_f32 v180, v180, v254\n"
    "v_mul_f32 v181, v181, v254\n"
    "v_mul_f32 v182, v182, v254\n"
    "v_mul_f32 v183, v183, v254\n"
    "v_mul_f32 v8, v8, v254\n"
    "v_mul_f32 v9, v9, v254\n"
    "v_mul_f32 v10, v10, v254\n"
    "v_mul_f32 v11, v11, v254\n"
    "v_mul_f32 v12, v12, v254\n"
    "v_mul_f32 v13, v13, v254\n"
    "v_mul_f32 v14, v14, v254\n"
    "v_mul_f32 v15, v15, v254\n"
    "v_mul_f32 v24, v24, v254\n"
    "v_mul_f32 v25, v25, v254\n"
    "v_mul_f32 v26, v26, v254\n"
    "v_mul_f32 v27, v27, v254\n"
    "v_mul_f32 v28, v28, v254\n"
    "v_mul_f32 v29, v29, v254\n"
    "v_mul_f32 v30, v30, v254\n"
    "v_mul_f32 v31, v31, v254\n"
    "v_mul_f32 v40, v40, v254\n"
    "v_mul_f32 v41, v41, v254\n"
    "v_mul_f32 v42, v42, v254\n"
    "v_mul_f32 v43, v43, v254\n"
    "v_mul_f32 v44, v44, v254\n"
    "v_mul_f32 v45, v45, v254\n"
    "v_mul_f32 v46, v46, v254\n"
    "v_mul_f32 v47, v47, v254\n"
    "v_mul_f32 v56, v56, v254\n"
    "v_mul_f32 v57, v57, v254\n"
    "v_mul_f32 v58, v58, v254\n"
    "v_mul_f32 v59, v59, v254\n"
    "v_mul_f32 v60, v60, v254\n"
    "v_mul_f32 v61, v61, v254\n"
    "v_mul_f32 v62, v62, v254\n"
    "v_mul_f32 v63, v63, v254\n"
    "v_mul_f32 v72, v72, v254\n"
    "v_mul_f32 v73, v73, v254\n"
    "v_mul_f32 v74, v74, v254\n"
    "v_mul_f32 v75, v75, v254\n"
    "v_mul_f32 v76, v76, v254\n"
    "v_mul_f32 v77, v77, v254\n"
    "v_mul_f32 v78, v78, v254\n"
    "v_mul_f32 v79, v79, v254\n"
    "v_mul_f32 v88, v88, v254\n"
    "v_mul_f32 v89, v89, v254\n"
    "v_mul_f32 v90, v90, v254\n"
    "v_mul_f32 v91, v91, v254\n"
    "v_mul_f32 v92, v92, v254\n"
    "v_mul_f32 v93, v93, v254\n"
    "v_mul_f32 v94, v94, v254\n"
    "v_mul_f32 v95, v95, v254\n"
    "v_mul_f32 v104, v104, v254\n"
    "v_mul_f32 v105, v105, v254\n"
    "v_mul_f32 v106, v106, v254\n"
    "v_mul_f32 v107, v107, v254\n"
    "v_mul_f32 v108, v108, v254\n"
    "v_mul_f32 v109, v109, v254\n"
    "v_mul_f32 v110, v110, v254\n"
    "v_mul_f32 v111, v111, v254\n"
    "v_mul_f32 v120, v120, v254\n"
    "v_mul_f32 v121, v121, v254\n"
    "v_mul_f32 v122, v122, v254\n"
    "v_mul_f32 v123, v123, v254\n"
    "v_mul_f32 v124, v124, v254\n"
    "v_mul_f32 v125, v125, v254\n"
    "v_mul_f32 v126, v126, v254\n"
    "v_mul_f32 v127, v127, v254\n"
    "v_exp_f32 v168, v128\n"
    "v_exp_f32 v169, v129\n"
    "v_exp_f32 v170, v130\n"
    "v_exp_f32 v171, v131\n"
    "v_cvt_pk_bf16_f32 v160, v168, v169\n"
    "v_cvt_pk_bf16_f32 v161, v170, v171\n"
    "v_exp_f32 v172, v132\n"
    "v_exp_f32 v173, v133\n"
    "v_exp_f32 v174, v134\n"
    "v_exp_f32 v175, v135\n"
    "v_cvt_pk_bf16_f32 v164, v172, v173\n"
    "v_cvt_pk_bf16_f32 v165, v174, v175\n"
    "v_exp_f32 v233, v136\n"
    "v_exp_f32 v234, v137\n"
    "v_exp_f32 v235, v138\n"
    "v_exp_f32 v236, v139\n"
    "v_cvt_pk_bf16_f32 v162, v233, v234\n"
    "v_cvt_pk_bf16_f32 v163, v235, v236\n"
    "v_exp_f32 v237, v140\n"
    "v_exp_f32 v238, v141\n"
    "v_exp_f32 v239, v142\n"
    "v_exp_f32 v240, v143\n"
    "v_cvt_pk_bf16_f32 v166, v237, v238\n"
    "v_cvt_pk_bf16_f32 v167, v239, v240\n"
    "s_nop 3\n"
    "s_branch LBL1%=\n"
    "LRL2%=:\n"
    "s_nop 7\n"
    "s_nop 3\n"
    "v_max3_f32 v253, v128, v129, v130\n"
    "v_max3_f32 v252, v131, v136, v137\n"
    "v_max3_f32 v253, v253, v138, v139\n"
    "v_max_f32 v253, v253, v252\n"
    "v_mov_b32 v252, v253\n"
    "s_nop 1\n"
    "v_permlane32_swap_b32 v253, v252\n"
    "v_max_f32 v253, v253, v252\n"
    "v_mov_b32 v252, v253\n"
    "s_nop 1\n"
    "v_permlane16_swap_b32 v253, v252\n"
    "v_max_f32 v253, v253, v252\n"
    "v_add_f32 v253, 0x40e00000, v253\n"
    "v_max_f32 v253, 0, v253\n"
    "v_sub_f32 v254, 0, v253\n"
    "v_exp_f32 v254, v254\n"
    "v_sub_f32 v128, v128, v253\n"
    "v_sub_f32 v129, v129, v253\n"
    "v_sub_f32 v130, v130, v253\n"
    "v_sub_f32 v131, v131, v253\n"
    "v_sub_f32 v136, v136, v253\n"
    "v_sub_f32 v137, v137, v253\n"
    "v_sub_f32 v138, v138, v253\n"
    "v_sub_f32 v139, v139, v253\n"
    "v_sub_f32 v144, v144, v253\n"
    "v_sub_f32 v145, v145, v253\n"
    "v_sub_f32 v146, v146, v253\n"
    "v_sub_f32 v147, v147, v253\n"
    "v_mul_f32 v176, v176, v254\n"
    "v_mul_f32 v177, v177, v254\n"
    "v_mul_f32 v178, v178, v254\n"
    "v_mul_f32 v179, v179, v254\n"
    "v_mul_f32 v0, v0, v254\n"
    "v_mul_f32 v1, v1, v254\n"
    "v_mul_f32 v2, v2, v254\n"
    "v_mul_f32 v3, v3, v254\n"
    "v_mul_f32 v4, v4, v254\n"
    "v_mul_f32 v5, v5, v254\n"
    "v_mul_f32 v6, v6, v254\n"
    "v_mul_f32 v7, v7, v254\n"
    "v_mul_f32 v16, v16, v254\n"
    "v_mul_f32 v17, v17, v254\n"
    "v_mul_f32 v18, v18, v254\n"
    "v_mul_f32 v19, v19, v254\n"
    "v_mul_f32 v20, v20, v254\n"
    "v_mul_f32 v21, v21, v254\n"
    "v_mul_f32 v22, v22, v254\n"
    "v_mul_f32 v23, v23, v254\n"
    "v_mul_f32 v32, v32, v254\n"
    "v_mul_f32 v33, v33, v254\n"
    "v_mul_f32 v34, v34, v254\n"
    "v_mul_f32 v35, v35, v254\n"
    "v_mul_f32 v36, v36, v254\n"
    "v_mul_f32 v37, v37, v254\n"
    "v_mul_f32 v38, v38, v254\n"
    "v_mul_f32 v39, v39, v254\n"
    "v_mul_f32 v48, v48, v254\n"
    "v_mul_f32 v49, v49, v254\n"
    "v_mul_f32 v50, v50, v254\n"
    "v_mul_f32 v51, v51, v254\n"
    "v_mul_f32 v52, v52, v254\n"
    "v_mul_f32 v53, v53, v254\n"
    "v_mul_f32 v54, v54, v254\n"
    "v_mul_f32 v55, v55, v254\n"
    "v_mul_f32 v64, v64, v254\n"
    "v_mul_f32 v65, v65, v254\n"
    "v_mul_f32 v66, v66, v254\n"
    "v_mul_f32 v67, v67, v254\n"
    "v_mul_f32 v68, v68, v254\n"
    "v_mul_f32 v69, v69, v254\n"
    "v_mul_f32 v70, v70, v254\n"
    "v_mul_f32 v71, v71, v254\n"
    "v_mul_f32 v80, v80, v254\n"
    "v_mul_f32 v81, v81, v254\n"
    "v_mul_f32 v82, v82, v254\n"
    "v_mul_f32 v83, v83, v254\n"
    "v_mul_f32 v84, v84, v254\n"
    "v_mul_f32 v85, v85, v254\n"
    "v_mul_f32 v86, v86, v254\n"
    "v_mul_f32 v87, v87, v254\n"
    "v_mul_f32 v96, v96, v254\n"
    "v_mul_f32 v97, v97, v254\n"
    "v_mul_f32 v98, v98, v254\n"
    "v_mul_f32 v99, v99, v254\n"
    "v_mul_f32 v100, v100, v254\n"
    "v_mul_f32 v101, v101, v254\n"
    "v_mul_f32 v102, v102, v254\n"
    "v_mul_f32 v103, v103, v254\n"
    "v_mul_f32 v112, v112, v254\n"
    "v_mul_f32 v113, v113, v254\n"
    "v_mul_f32 v114, v114, v254\n"
    "v_mul_f32 v115, v115, v254\n"
    "v_mul_f32 v116, v116, v254\n"
    "v_mul_f32 v117, v117, v254\n"
    "v_mul_f32 v118, v118, v254\n"
    "v_mul_f32 v119, v119, v254\n"
    "v_max3_f32 v253, v132, v133, v134\n"
    "v_max3_f32 v252, v135, v140, v141\n"
    "v_max3_f32 v253, v253, v142, v143\n"
    "v_max_f32 v253, v253, v252\n"
    "v_mov_b32 v252, v253\n"
    "s_nop 1\n"
    "v_permlane32_swap_b32 v253, v252\n"
    "v_max_f32 v253, v253, v252\n"
    "v_mov_b32 v252, v253\n"
    "s_nop 1\n"
    "v_permlane16_swap_b32 v253, v252\n"
    "v_max_f32 v253, v253, v252\n"
    "v_add_f32 v253, 0x40e00000, v253\n"
    "v_max_f32 v253, 0, v253\n"
    "v_sub_f32 v254, 0, v253\n"
    "v_exp_f32 v254, v254\n"
    "v_sub_f32 v132, v132, v253\n"
    "v_sub_f32 v133, v133, v253\n"
    "v_sub_f32 v134, v134, v253\n"
    "v_sub_f32 v135, v135, v253\n"
    "v_sub_f32 v140, v140, v253\n"
    "v_sub_f32 v141, v141, v253\n"
    "v_sub_f32 v142, v142, v253\n"
    "v_sub_f32 v143, v143, v253\n"
    "v_sub_f32 v148, v148, v253\n"
    "v_sub_f32 v149, v149, v253\n"
    "v_sub_f32 v150, v150, v253\n"
    "v_sub_f32 v151, v151, v253\n"
    "v_mul_f32 v180, v180, v254\n"
    "v_mul_f32 v181, v181, v254\n"
    "v_mul_f32 v182, v182, v254\n"
    "v_mul_f32 v183, v183, v254\n"
    "v_mul_f32 v8, v8, v254\n"
    "v_mul_f32 v9, v9, v254\n"
    "v_mul_f32 v10, v10, v254\n"
    "v_mul_f32 v11, v11, v254\n"
    "v_mul_f32 v12, v12, v254\n"
    "v_mul_f32 v13, v13, v254\n"
    "v_mul_f32 v14, v14, v254\n"
    "v_mul_f32 v15, v15, v254\n"
    "v_mul_f32 v24, v24, v254\n"
    "v_mul_f32 v25, v25, v254\n"
    "v_mul_f32 v26, v26, v254\n"
    "v_mul_f32 v27, v27, v254\n"
    "v_mul_f32 v28, v28, v254\n"
    "v_mul_f32 v29, v29, v254\n"
    "v_mul_f32 v30, v30, v254\n"
    "v_mul_f32 v31, v31, v254\n"
    "v_mul_f32 v40, v40, v254\n"
    "v_mul_f32 v41, v41, v254\n"
    "v_mul_f32 v42, v42, v254\n"
    "v_mul_f32 v43, v43, v254\n"
    "v_mul_f32 v44, v44, v254\n"
    "v_mul_f32 v45, v45, v254\n"
    "v_mul_f32 v46, v46, v254\n"
    "v_mul_f32 v47, v47, v254\n"
    "v_mul_f32 v56, v56, v254\n"
    "v_mul_f32 v57, v57, v254\n"
    "v_mul_f32 v58, v58, v254\n"
    "v_mul_f32 v59, v59, v254\n"
    "v_mul_f32 v60, v60, v254\n"
    "v_mul_f32 v61, v61, v254\n"
    "v_mul_f32 v62, v62, v254\n"
    "v_mul_f32 v63, v63, v254\n"
    "v_mul_f32 v72, v72, v254\n"
    "v_mul_f32 v73, v73, v254\n"
    "v_mul_f32 v74, v74, v254\n"
    "v_mul_f32 v75, v75, v254\n"
    "v_mul_f32 v76, v76, v254\n"
    "v_mul_f32 v77, v77, v254\n"
    "v_mul_f32 v78, v78, v254\n"
    "v_mul_f32 v79, v79, v254\n"
    "v_mul_f32 v88, v88, v254\n"
    "v_mul_f32 v89, v89, v254\n"
    "v_mul_f32 v90, v90, v254\n"
    "v_mul_f32 v91, v91, v254\n"
    "v_mul_f32 v92, v92, v254\n"
    "v_mul_f32 v93, v93, v254\n"
    "v_mul_f32 v94, v94, v254\n"
    "v_mul_f32 v95, v95, v254\n"
    "v_mul_f32 v104, v104, v254\n"
    "v_mul_f32 v105, v105, v254\n"
    "v_mul_f32 v106, v106, v254\n"
    "v_mul_f32 v107, v107, v254\n"
    "v_mul_f32 v108, v108, v254\n"
    "v_mul_f32 v109, v109, v254\n"
    "v_mul_f32 v110, v110, v254\n"
    "v_mul_f32 v111, v111, v254\n"
    "v_mul_f32 v120, v120, v254\n"
    "v_mul_f32 v121, v121, v254\n"
    "v_mul_f32 v122, v122, v254\n"
    "v_mul_f32 v123, v123, v254\n"
    "v_mul_f32 v124, v124, v254\n"
    "v_mul_f32 v125, v125, v254\n"
    "v_mul_f32 v126, v126, v254\n"
    "v_mul_f32 v127, v127, v254\n"
    "v_exp_f32 v168, v128\n"
    "v_exp_f32 v169, v129\n"
    "v_exp_f32 v170, v130\n"
    "v_exp_f32 v171, v131\n"
    "v_cvt_pk_bf16_f32 v152, v168, v169\n"
    "v_cvt_pk_bf16_f32 v153, v170, v171\n"
    "v_exp_f32 v172, v132\n"
    "v_exp_f32 v173, v133\n"
    "v_exp_f32 v174, v134\n"
    "v_exp_f32 v175, v135\n"
    "v_cvt_pk_bf16_f32 v156, v172, v173\n"
    "v_cvt_pk_bf16_f32 v157, v174, v175\n"
    "v_exp_f32 v233, v136\n"
    "v_exp_f32 v234, v137\n"
    "v_exp_f32 v235, v138\n"
    "v_exp_f32 v236, v139\n"
    "v_cvt_pk_bf16_f32 v154, v233, v234\n"
    "v_cvt_pk_bf16_f32 v155, v235, v236\n"
    "v_exp_f32 v237, v140\n"
    "v_exp_f32 v238, v141\n"
    "v_exp_f32 v239, v142\n"
    "v_exp_f32 v240, v143\n"
    "v_cvt_pk_bf16_f32 v158, v237, v238\n"
    "v_cvt_pk_bf16_f32 v159, v239, v240\n"
    "s_nop 3\n"
    "s_branch LBL2%=\n"
    "LRL3%=:\n"
    "s_nop 7\n"
    "s_nop 3\n"
    "v_max3_f32 v253, v128, v129, v130\n"
    "v_max3_f32 v252, v131, v136, v137\n"
    "v_max3_f32 v253, v253, v138, v139\n"
    "v_max_f32 v253, v253, v252\n"
    "v_mov_b32 v252, v253\n"
    "s_nop 1\n"
    "v_permlane32_swap_b32 v253, v252\n"
    "v_max_f32 v253, v253, v252\n"
    "v_mov_b32 v252, v253\n"
    "s_nop 1\n"
    "v_permlane16_swap_b32 v253, v252\n"
    "v_max_f32 v253, v253, v252\n"
    "v_add_f32 v253, 0x40e00000, v253\n"
    "v_max_f32 v253, 0, v253\n"
    "v_sub_f32 v254, 0, v253\n"
    "v_exp_f32 v254, v254\n"
    "v_sub_f32 v128, v128, v253\n"
    "v_sub_f32 v129, v129, v253\n"
    "v_sub_f32 v130, v130, v253\n"
    "v_sub_f32 v131, v131, v253\n"
    "v_sub_f32 v136, v136, v253\n"
    "v_sub_f32 v137, v137, v253\n"
    "v_sub_f32 v138, v138, v253\n"
    "v_sub_f32 v139, v139, v253\n"
    "v_sub_f32 v144, v144, v253\n"
    "v_sub_f32 v145, v145, v253\n"
    "v_sub_f32 v146, v146, v253\n"
    "v_sub_f32 v147, v147, v253\n"
    "v_mul_f32 v176, v176, v254\n"
    "v_mul_f32 v177, v177, v254\n"
    "v_mul_f32 v178, v178, v254\n"
    "v_mul_f32 v179, v179, v254\n"
    "v_mul_f32 v0, v0, v254\n"
    "v_mul_f32 v1, v1, v254\n"
    "v_mul_f32 v2, v2, v254\n"
    "v_mul_f32 v3, v3, v254\n"
    "v_mul_f32 v4, v4, v254\n"
    "v_mul_f32 v5, v5, v254\n"
    "v_mul_f32 v6, v6, v254\n"
    "v_mul_f32 v7, v7, v254\n"
    "v_mul_f32 v16, v16, v254\n"
    "v_mul_f32 v17, v17, v254\n"
    "v_mul_f32 v18, v18, v254\n"
    "v_mul_f32 v19, v19, v254\n"
    "v_mul_f32 v20, v20, v254\n"
    "v_mul_f32 v21, v21, v254\n"
    "v_mul_f32 v22, v22, v254\n"
    "v_mul_f32 v23, v23, v254\n"
    "v_mul_f32 v32, v32, v254\n"
    "v_mul_f32 v33, v33, v254\n"
    "v_mul_f32 v34, v34, v254\n"
    "v_mul_f32 v35, v35, v254\n"
    "v_mul_f32 v36, v36, v254\n"
    "v_mul_f32 v37, v37, v254\n"
    "v_mul_f32 v38, v38, v254\n"
    "v_mul_f32 v39, v39, v254\n"
    "v_mul_f32 v48, v48, v254\n"
    "v_mul_f32 v49, v49, v254\n"
    "v_mul_f32 v50, v50, v254\n"
    "v_mul_f32 v51, v51, v254\n"
    "v_mul_f32 v52, v52, v254\n"
    "v_mul_f32 v53, v53, v254\n"
    "v_mul_f32 v54, v54, v254\n"
    "v_mul_f32 v55, v55, v254\n"
    "v_mul_f32 v64, v64, v254\n"
    "v_mul_f32 v65, v65, v254\n"
    "v_mul_f32 v66, v66, v254\n"
    "v_mul_f32 v67, v67, v254\n"
    "v_mul_f32 v68, v68, v254\n"
    "v_mul_f32 v69, v69, v254\n"
    "v_mul_f32 v70, v70, v254\n"
    "v_mul_f32 v71, v71, v254\n"
    "v_mul_f32 v80, v80, v254\n"
    "v_mul_f32 v81, v81, v254\n"
    "v_mul_f32 v82, v82, v254\n"
    "v_mul_f32 v83, v83, v254\n"
    "v_mul_f32 v84, v84, v254\n"
    "v_mul_f32 v85, v85, v254\n"
    "v_mul_f32 v86, v86, v254\n"
    "v_mul_f32 v87, v87, v254\n"
    "v_mul_f32 v96, v96, v254\n"
    "v_mul_f32 v97, v97, v254\n"
    "v_mul_f32 v98, v98, v254\n"
    "v_mul_f32 v99, v99, v254\n"
    "v_mul_f32 v100, v100, v254\n"
    "v_mul_f32 v101, v101, v254\n"
    "v_mul_f32 v102, v102, v254\n"
    "v_mul_f32 v103, v103, v254\n"
    "v_mul_f32 v112, v112, v254\n"
    "v_mul_f32 v113, v113, v254\n"
    "v_mul_f32 v114, v114, v254\n"
    "v_mul_f32 v115, v115, v254\n"
    "v_mul_f32 v116, v116, v254\n"
    "v_mul_f32 v117, v117, v254\n"
    "v_mul_f32 v118, v118, v254\n"
    "v_mul_f32 v119, v119, v254\n"
    "v_max3_f32 v253, v132, v133, v134\n"
    "v_max3_f32 v252, v135, v140, v141\n"
    "v_max3_f32 v253, v253, v142, v143\n"
    "v_max_f32 v253, v253, v252\n"
    "v_mov_b32 v252, v253\n"
    "s_nop 1\n"
    "v_permlane32_swap_b32 v253, v252\n"
    "v_max_f32 v253, v253, v252\n"
    "v_mov_b32 v252, v253\n"
    "s_nop 1\n"
    "v_permlane16_swap_b32 v253, v252\n"
    "v_max_f32 v253, v253, v252\n"
    "v_add_f32 v253, 0x40e00000, v253\n"
    "v_max_f32 v253, 0, v253\n"
    "v_sub_f32 v254, 0, v253\n"
    "v_exp_f32 v254, v254\n"
    "v_sub_f32 v132, v132, v253\n"
    "v_sub_f32 v133, v133, v253\n"
    "v_sub_f32 v134, v134, v253\n"
    "v_sub_f32 v135, v135, v253\n"
    "v_sub_f32 v140, v140, v253\n"
    "v_sub_f32 v141, v141, v253\n"
    "v_sub_f32 v142, v142, v253\n"
    "v_sub_f32 v143, v143, v253\n"
    "v_sub_f32 v148, v148, v253\n"
    "v_sub_f32 v149, v149, v253\n"
    "v_sub_f32 v150, v150, v253\n"
    "v_sub_f32 v151, v151, v253\n"
    "v_mul_f32 v180, v180, v254\n"
    "v_mul_f32 v181, v181, v254\n"
    "v_mul_f32 v182, v182, v254\n"
    "v_mul_f32 v183, v183, v254\n"
    "v_mul_f32 v8, v8, v254\n"
    "v_mul_f32 v9, v9, v254\n"
    "v_mul_f32 v10, v10, v254\n"
    "v_mul_f32 v11, v11, v254\n"
    "v_mul_f32 v12, v12, v254\n"
    "v_mul_f32 v13, v13, v254\n"
    "v_mul_f32 v14, v14, v254\n"
    "v_mul_f32 v15, v15, v254\n"
    "v_mul_f32 v24, v24, v254\n"
    "v_mul_f32 v25, v25, v254\n"
    "v_mul_f32 v26, v26, v254\n"
    "v_mul_f32 v27, v27, v254\n"
    "v_mul_f32 v28, v28, v254\n"
    "v_mul_f32 v29, v29, v254\n"
    "v_mul_f32 v30, v30, v254\n"
    "v_mul_f32 v31, v31, v254\n"
    "v_mul_f32 v40, v40, v254\n"
    "v_mul_f32 v41, v41, v254\n"
    "v_mul_f32 v42, v42, v254\n"
    "v_mul_f32 v43, v43, v254\n"
    "v_mul_f32 v44, v44, v254\n"
    "v_mul_f32 v45, v45, v254\n"
    "v_mul_f32 v46, v46, v254\n"
    "v_mul_f32 v47, v47, v254\n"
    "v_mul_f32 v56, v56, v254\n"
    "v_mul_f32 v57, v57, v254\n"
    "v_mul_f32 v58, v58, v254\n"
    "v_mul_f32 v59, v59, v254\n"
    "v_mul_f32 v60, v60, v254\n"
    "v_mul_f32 v61, v61, v254\n"
    "v_mul_f32 v62, v62, v254\n"
    "v_mul_f32 v63, v63, v254\n"
    "v_mul_f32 v72, v72, v254\n"
    "v_mul_f32 v73, v73, v254\n"
    "v_mul_f32 v74, v74, v254\n"
    "v_mul_f32 v75, v75, v254\n"
    "v_mul_f32 v76, v76, v254\n"
    "v_mul_f32 v77, v77, v254\n"
    "v_mul_f32 v78, v78, v254\n"
    "v_mul_f32 v79, v79, v254\n"
    "v_mul_f32 v88, v88, v254\n"
    "v_mul_f32 v89, v89, v254\n"
    "v_mul_f32 v90, v90, v254\n"
    "v_mul_f32 v91, v91, v254\n"
    "v_mul_f32 v92, v92, v254\n"
    "v_mul_f32 v93, v93, v254\n"
    "v_mul_f32 v94, v94, v254\n"
    "v_mul_f32 v95, v95, v254\n"
    "v_mul_f32 v104, v104, v254\n"
    "v_mul_f32 v105, v105, v254\n"
    "v_mul_f32 v106, v106, v254\n"
    "v_mul_f32 v107, v107, v254\n"
    "v_mul_f32 v108, v108, v254\n"
    "v_mul_f32 v109, v109, v254\n"
    "v_mul_f32 v110, v110, v254\n"
    "v_mul_f32 v111, v111, v254\n"
    "v_mul_f32 v120, v120, v254\n"
    "v_mul_f32 v121, v121, v254\n"
    "v_mul_f32 v122, v122, v254\n"
    "v_mul_f32 v123, v123, v254\n"
    "v_mul_f32 v124, v124, v254\n"
    "v_mul_f32 v125, v125, v254\n"
    "v_mul_f32 v126, v126, v254\n"
    "v_mul_f32 v127, v127, v254\n"
    "v_exp_f32 v168, v128\n"
    "v_exp_f32 v169, v129\n"
    "v_exp_f32 v170, v130\n"
    "v_exp_f32 v171, v131\n"
    "v_cvt_pk_bf16_f32 v160, v168, v169\n"
    "v_cvt_pk_bf16_f32 v161, v170, v171\n"
    "v_exp_f32 v172, v132\n"
    "v_exp_f32 v173, v133\n"
    "v_exp_f32 v174, v134\n"
    "v_exp_f32 v175, v135\n"
    "v_cvt_pk_bf16_f32 v164, v172, v173\n"
    "v_cvt_pk_bf16_f32 v165, v174, v175\n"
    "v_exp_f32 v233, v136\n"
    "v_exp_f32 v234, v137\n"
    "v_exp_f32 v235, v138\n"
    "v_exp_f32 v236, v139\n"
    "v_cvt_pk_bf16_f32 v162, v233, v234\n"
    "v_cvt_pk_bf16_f32 v163, v235, v236\n"
    "v_exp_f32 v237, v140\n"
    "v_exp_f32 v238, v141\n"
    "v_exp_f32 v239, v142\n"
    "v_exp_f32 v240, v143\n"
    "v_cvt_pk_bf16_f32 v166, v237, v238\n"
    "v_cvt_pk_bf16_f32 v167, v239, v240\n"
    "s_nop 3\n"
    "s_branch LBL3%=\n"
    "LR28%=:\n"
    "s_nop 7\n"
    "s_nop 3\n"
    "v_max3_f32 v253, v128, v129, v130\n"
    "v_max3_f32 v252, v131, v136, v137\n"
    "v_max3_f32 v253, v253, v138, v139\n"
    "v_max_f32 v253, v253, v252\n"
    "v_mov_b32 v252, v253\n"
    "s_nop 1\n"
    "v_permlane32_swap_b32 v253, v252\n"
    "v_max_f32 v253, v253, v252\n"
    "v_mov_b32 v252, v253\n"
    "s_nop 1\n"
    "v_permlane16_swap_b32 v253, v252\n"
    "v_max_f32 v253, v253, v252\n"
    "v_add_f32 v253, 0x40e00000, v253\n"
    "v_max_f32 v253, 0, v253\n"
    "v_sub_f32 v254, 0, v253\n"
    "v_exp_f32 v254, v254\n"
    "v_sub_f32 v128, v128, v253\n"
    "v_sub_f32 v129, v129, v253\n"
    "v_sub_f32 v130, v130, v253\n"
    "v_sub_f32 v131, v131, v253\n"
    "v_sub_f32 v136, v136, v253\n"
    "v_sub_f32 v137, v137, v253\n"
    "v_sub_f32 v138, v138, v253\n"
    "v_sub_f32 v139, v139, v253\n"
    "v_sub_f32 v144, v144, v253\n"
    "v_sub_f32 v145, v145, v253\n"
    "v_sub_f32 v146, v146, v253\n"
    "v_sub_f32 v147, v147, v253\n"
    "v_mul_f32 v176, v176, v254\n"
    "v_mul_f32 v177, v177, v254\n"
    "v_mul_f32 v178, v178, v254\n"
    "v_mul_f32 v179, v179, v254\n"
    "v_mul_f32 v0, v0, v254\n"
    "v_mul_f32 v1, v1, v254\n"
    "v_mul_f32 v2, v2, v254\n"
    "v_mul_f32 v3, v3, v254\n"
    "v_mul_f32 v4, v4, v254\n"
    "v_mul_f32 v5, v5, v254\n"
    "v_mul_f32 v6, v6, v254\n"
    "v_mul_f32 v7, v7, v254\n"
    "v_mul_f32 v16, v16, v254\n"
    "v_mul_f32 v17, v17, v254\n"
    "v_mul_f32 v18, v18, v254\n"
    "v_mul_f32 v19, v19, v254\n"
    "v_mul_f32 v20, v20, v254\n"
    "v_mul_f32 v21, v21, v254\n"
    "v_mul_f32 v22, v22, v254\n"
    "v_mul_f32 v23, v23, v254\n"
    "v_mul_f32 v32, v32, v254\n"
    "v_mul_f32 v33, v33, v254\n"
    "v_mul_f32 v34, v34, v254\n"
    "v_mul_f32 v35, v35, v254\n"
    "v_mul_f32 v36, v36, v254\n"
    "v_mul_f32 v37, v37, v254\n"
    "v_mul_f32 v38, v38, v254\n"
    "v_mul_f32 v39, v39, v254\n"
    "v_mul_f32 v48, v48, v254\n"
    "v_mul_f32 v49, v49, v254\n"
    "v_mul_f32 v50, v50, v254\n"
    "v_mul_f32 v51, v51, v254\n"
    "v_mul_f32 v52, v52, v254\n"
    "v_mul_f32 v53, v53, v254\n"
    "v_mul_f32 v54, v54, v254\n"
    "v_mul_f32 v55, v55, v254\n"
    "v_mul_f32 v64, v64, v254\n"
    "v_mul_f32 v65, v65, v254\n"
    "v_mul_f32 v66, v66, v254\n"
    "v_mul_f32 v67, v67, v254\n"
    "v_mul_f32 v68, v68, v254\n"
    "v_mul_f32 v69, v69, v254\n"
    "v_mul_f32 v70, v70, v254\n"
    "v_mul_f32 v71, v71, v254\n"
    "v_mul_f32 v80, v80, v254\n"
    "v_mul_f32 v81, v81, v254\n"
    "v_mul_f32 v82, v82, v254\n"
    "v_mul_f32 v83, v83, v254\n"
    "v_mul_f32 v84, v84, v254\n"
    "v_mul_f32 v85, v85, v254\n"
    "v_mul_f32 v86, v86, v254\n"
    "v_mul_f32 v87, v87, v254\n"
    "v_mul_f32 v96, v96, v254\n"
    "v_mul_f32 v97, v97, v254\n"
    "v_mul_f32 v98, v98, v254\n"
    "v_mul_f32 v99, v99, v254\n"
    "v_mul_f32 v100, v100, v254\n"
    "v_mul_f32 v101, v101, v254\n"
    "v_mul_f32 v102, v102, v254\n"
    "v_mul_f32 v103, v103, v254\n"
    "v_mul_f32 v112, v112, v254\n"
    "v_mul_f32 v113, v113, v254\n"
    "v_mul_f32 v114, v114, v254\n"
    "v_mul_f32 v115, v115, v254\n"
    "v_mul_f32 v116, v116, v254\n"
    "v_mul_f32 v117, v117, v254\n"
    "v_mul_f32 v118, v118, v254\n"
    "v_mul_f32 v119, v119, v254\n"
    "v_max3_f32 v253, v132, v133, v134\n"
    "v_max3_f32 v252, v135, v140, v141\n"
    "v_max3_f32 v253, v253, v142, v143\n"
    "v_max_f32 v253, v253, v252\n"
    "v_mov_b32 v252, v253\n"
    "s_nop 1\n"
    "v_permlane32_swap_b32 v253, v252\n"
    "v_max_f32 v253, v253, v252\n"
    "v_mov_b32 v252, v253\n"
    "s_nop 1\n"
    "v_permlane16_swap_b32 v253, v252\n"
    "v_max_f32 v253, v253, v252\n"
    "v_add_f32 v253, 0x40e00000, v253\n"
    "v_max_f32 v253, 0, v253\n"
    "v_sub_f32 v254, 0, v253\n"
    "v_exp_f32 v254, v254\n"
    "v_sub_f32 v132, v132, v253\n"
    "v_sub_f32 v133, v133, v253\n"
    "v_sub_f32 v134, v134, v253\n"
    "v_sub_f32 v135, v135, v253\n"
    "v_sub_f32 v140, v140, v253\n"
    "v_sub_f32 v141, v141, v253\n"
    "v_sub_f32 v142, v142, v253\n"
    "v_sub_f32 v143, v143, v253\n"
    "v_sub_f32 v148, v148, v253\n"
    "v_sub_f32 v149, v149, v253\n"
    "v_sub_f32 v150, v150, v253\n"
    "v_sub_f32 v151, v151, v253\n"
    "v_mul_f32 v180, v180, v254\n"
    "v_mul_f32 v181, v181, v254\n"
    "v_mul_f32 v182, v182, v254\n"
    "v_mul_f32 v183, v183, v254\n"
    "v_mul_f32 v8, v8, v254\n"
    "v_mul_f32 v9, v9, v254\n"
    "v_mul_f32 v10, v10, v254\n"
    "v_mul_f32 v11, v11, v254\n"
    "v_mul_f32 v12, v12, v254\n"
    "v_mul_f32 v13, v13, v254\n"
    "v_mul_f32 v14, v14, v254\n"
    "v_mul_f32 v15, v15, v254\n"
    "v_mul_f32 v24, v24, v254\n"
    "v_mul_f32 v25, v25, v254\n"
    "v_mul_f32 v26, v26, v254\n"
    "v_mul_f32 v27, v27, v254\n"
    "v_mul_f32 v28, v28, v254\n"
    "v_mul_f32 v29, v29, v254\n"
    "v_mul_f32 v30, v30, v254\n"
    "v_mul_f32 v31, v31, v254\n"
    "v_mul_f32 v40, v40, v254\n"
    "v_mul_f32 v41, v41, v254\n"
    "v_mul_f32 v42, v42, v254\n"
    "v_mul_f32 v43, v43, v254\n"
    "v_mul_f32 v44, v44, v254\n"
    "v_mul_f32 v45, v45, v254\n"
    "v_mul_f32 v46, v46, v254\n"
    "v_mul_f32 v47, v47, v254\n"
    "v_mul_f32 v56, v56, v254\n"
    "v_mul_f32 v57, v57, v254\n"
    "v_mul_f32 v58, v58, v254\n"
    "v_mul_f32 v59, v59, v254\n"
    "v_mul_f32 v60, v60, v254\n"
    "v_mul_f32 v61, v61, v254\n"
    "v_mul_f32 v62, v62, v254\n"
    "v_mul_f32 v63, v63, v254\n"
    "v_mul_f32 v72, v72, v254\n"
    "v_mul_f32 v73, v73, v254\n"
    "v_mul_f32 v74, v74, v254\n"
    "v_mul_f32 v75, v75, v254\n"
    "v_mul_f32 v76, v76, v254\n"
    "v_mul_f32 v77, v77, v254\n"
    "v_mul_f32 v78, v78, v254\n"
    "v_mul_f32 v79, v79, v254\n"
    "v_mul_f32 v88, v88, v254\n"
    "v_mul_f32 v89, v89, v254\n"
    "v_mul_f32 v90, v90, v254\n"
    "v_mul_f32 v91, v91, v254\n"
    "v_mul_f32 v92, v92, v254\n"
    "v_mul_f32 v93, v93, v254\n"
    "v_mul_f32 v94, v94, v254\n"
    "v_mul_f32 v95, v95, v254\n"
    "v_mul_f32 v104, v104, v254\n"
    "v_mul_f32 v105, v105, v254\n"
    "v_mul_f32 v106, v106, v254\n"
    "v_mul_f32 v107, v107, v254\n"
    "v_mul_f32 v108, v108, v254\n"
    "v_mul_f32 v109, v109, v254\n"
    "v_mul_f32 v110, v110, v254\n"
    "v_mul_f32 v111, v111, v254\n"
    "v_mul_f32 v120, v120, v254\n"
    "v_mul_f32 v121, v121, v254\n"
    "v_mul_f32 v122, v122, v254\n"
    "v_mul_f32 v123, v123, v254\n"
    "v_mul_f32 v124, v124, v254\n"
    "v_mul_f32 v125, v125, v254\n"
    "v_mul_f32 v126, v126, v254\n"
    "v_mul_f32 v127, v127, v254\n"
    "v_exp_f32 v168, v128\n"
    "v_exp_f32 v169, v129\n"
    "v_exp_f32 v170, v130\n"
    "v_exp_f32 v171, v131\n"
    "v_cvt_pk_bf16_f32 v152, v168, v169\n"
    "v_cvt_pk_bf16_f32 v153, v170, v171\n"
    "v_exp_f32 v172, v132\n"
    "v_exp_f32 v173, v133\n"
    "v_exp_f32 v174, v134\n"
    "v_exp_f32 v175, v135\n"
    "v_cvt_pk_bf16_f32 v156, v172, v173\n"
    "v_cvt_pk_bf16_f32 v157, v174, v175\n"
    "v_exp_f32 v233, v136\n"
    "v_exp_f32 v234, v137\n"
    "v_exp_f32 v235, v138\n"
    "v_exp_f32 v236, v139\n"
    "v_cvt_pk_bf16_f32 v154, v233, v234\n"
    "v_cvt_pk_bf16_f32 v155, v235, v236\n"
    "v_exp_f32 v237, v140\n"
    "v_exp_f32 v238, v141\n"
    "v_exp_f32 v239, v142\n"
    "v_exp_f32 v240, v143\n"
    "v_cvt_pk_bf16_f32 v158, v237, v238\n"
    "v_cvt_pk_bf16_f32 v159, v239, v240\n"
    "s_nop 3\n"
    "s_branch LB28%=\n"
    "LR29%=:\n"
    "s_nop 7\n"
    "s_nop 3\n"
    "v_max3_f32 v253, v128, v129, v130\n"
    "v_max3_f32 v252, v131, v136, v137\n"
    "v_max3_f32 v253, v253, v138, v139\n"
    "v_max_f32 v253, v253, v252\n"
    "v_mov_b32 v252, v253\n"
    "s_nop 1\n"
    "v_permlane32_swap_b32 v253, v252\n"
    "v_max_f32 v253, v253, v252\n"
    "v_mov_b32 v252, v253\n"
    "s_nop 1\n"
    "v_permlane16_swap_b32 v253, v252\n"
    "v_max_f32 v253, v253, v252\n"
    "v_add_f32 v253, 0x40e00000, v253\n"
    "v_max_f32 v253, 0, v253\n"
    "v_sub_f32 v254, 0, v253\n"
    "v_exp_f32 v254, v254\n"
    "v_sub_f32 v128, v128, v253\n"
    "v_sub_f32 v129, v129, v253\n"
    "v_sub_f32 v130, v130, v253\n"
    "v_sub_f32 v131, v131, v253\n"
    "v_sub_f32 v136, v136, v253\n"
    "v_sub_f32 v137, v137, v253\n"
    "v_sub_f32 v138, v138, v253\n"
    "v_sub_f32 v139, v139, v253\n"
    "v_sub_f32 v144, v144, v253\n"
    "v_sub_f32 v145, v145, v253\n"
    "v_sub_f32 v146, v146, v253\n"
    "v_sub_f32 v147, v147, v253\n"
    "v_mul_f32 v176, v176, v254\n"
    "v_mul_f32 v177, v177, v254\n"
    "v_mul_f32 v178, v178, v254\n"
    "v_mul_f32 v179, v179, v254\n"
    "v_mul_f32 v0, v0, v254\n"
    "v_mul_f32 v1, v1, v254\n"
    "v_mul_f32 v2, v2, v254\n"
    "v_mul_f32 v3, v3, v254\n"
    "v_mul_f32 v4, v4, v254\n"
    "v_mul_f32 v5, v5, v254\n"
    "v_mul_f32 v6, v6, v254\n"
    "v_mul_f32 v7, v7, v254\n"
    "v_mul_f32 v16, v16, v254\n"
    "v_mul_f32 v17, v17, v254\n"
    "v_mul_f32 v18, v18, v254\n"
    "v_mul_f32 v19, v19, v254\n"
    "v_mul_f32 v20, v20, v254\n"
    "v_mul_f32 v21, v21, v254\n"
    "v_mul_f32 v22, v22, v254\n"
    "v_mul_f32 v23, v23, v254\n"
    "v_mul_f32 v32, v32, v254\n"
    "v_mul_f32 v33, v33, v254\n"
    "v_mul_f32 v34, v34, v254\n"
    "v_mul_f32 v35, v35, v254\n"
    "v_mul_f32 v36, v36, v254\n"
    "v_mul_f32 v37, v37, v254\n"
    "v_mul_f32 v38, v38, v254\n"
    "v_mul_f32 v39, v39, v254\n"
    "v_mul_f32 v48, v48, v254\n"
    "v_mul_f32 v49, v49, v254\n"
    "v_mul_f32 v50, v50, v254\n"
    "v_mul_f32 v51, v51, v254\n"
    "v_mul_f32 v52, v52, v254\n"
    "v_mul_f32 v53, v53, v254\n"
    "v_mul_f32 v54, v54, v254\n"
    "v_mul_f32 v55, v55, v254\n"
    "v_mul_f32 v64, v64, v254\n"
    "v_mul_f32 v65, v65, v254\n"
    "v_mul_f32 v66, v66, v254\n"
    "v_mul_f32 v67, v67, v254\n"
    "v_mul_f32 v68, v68, v254\n"
    "v_mul_f32 v69, v69, v254\n"
    "v_mul_f32 v70, v70, v254\n"
    "v_mul_f32 v71, v71, v254\n"
    "v_mul_f32 v80, v80, v254\n"
    "v_mul_f32 v81, v81, v254\n"
    "v_mul_f32 v82, v82, v254\n"
    "v_mul_f32 v83, v83, v254\n"
    "v_mul_f32 v84, v84, v254\n"
    "v_mul_f32 v85, v85, v254\n"
    "v_mul_f32 v86, v86, v254\n"
    "v_mul_f32 v87, v87, v254\n"
    "v_mul_f32 v96, v96, v254\n"
    "v_mul_f32 v97, v97, v254\n"
    "v_mul_f32 v98, v98, v254\n"
    "v_mul_f32 v99, v99, v254\n"
    "v_mul_f32 v100, v100, v254\n"
    "v_mul_f32 v101, v101, v254\n"
    "v_mul_f32 v102, v102, v254\n"
    "v_mul_f32 v103, v103, v254\n"
    "v_mul_f32 v112, v112, v254\n"
    "v_mul_f32 v113, v113, v254\n"
    "v_mul_f32 v114, v114, v254\n"
    "v_mul_f32 v115, v115, v254\n"
    "v_mul_f32 v116, v116, v254\n"
    "v_mul_f32 v117, v117, v254\n"
    "v_mul_f32 v118, v118, v254\n"
    "v_mul_f32 v119, v119, v254\n"
    "v_max3_f32 v253, v132, v133, v134\n"
    "v_max3_f32 v252, v135, v140, v141\n"
    "v_max3_f32 v253, v253, v142, v143\n"
    "v_max_f32 v253, v253, v252\n"
    "v_mov_b32 v252, v253\n"
    "s_nop 1\n"
    "v_permlane32_swap_b32 v253, v252\n"
    "v_max_f32 v253, v253, v252\n"
    "v_mov_b32 v252, v253\n"
    "s_nop 1\n"
    "v_permlane16_swap_b32 v253, v252\n"
    "v_max_f32 v253, v253, v252\n"
    "v_add_f32 v253, 0x40e00000, v253\n"
    "v_max_f32 v253, 0, v253\n"
    "v_sub_f32 v254, 0, v253\n"
    "v_exp_f32 v254, v254\n"
    "v_sub_f32 v132, v132, v253\n"
    "v_sub_f32 v133, v133, v253\n"
    "v_sub_f32 v134, v134, v253\n"
    "v_sub_f32 v135, v135, v253\n"
    "v_sub_f32 v140, v140, v253\n"
    "v_sub_f32 v141, v141, v253\n"
    "v_sub_f32 v142, v142, v253\n"
    "v_sub_f32 v143, v143, v253\n"
    "v_sub_f32 v148, v148, v253\n"
    "v_sub_f32 v149, v149, v253\n"
    "v_sub_f32 v150, v150, v253\n"
    "v_sub_f32 v151, v151, v253\n"
    "v_mul_f32 v180, v180, v254\n"
    "v_mul_f32 v181, v181, v254\n"
    "v_mul_f32 v182, v182, v254\n"
    "v_mul_f32 v183, v183, v254\n"
    "v_mul_f32 v8, v8, v254\n"
    "v_mul_f32 v9, v9, v254\n"
    "v_mul_f32 v10, v10, v254\n"
    "v_mul_f32 v11, v11, v254\n"
    "v_mul_f32 v12, v12, v254\n"
    "v_mul_f32 v13, v13, v254\n"
    "v_mul_f32 v14, v14, v254\n"
    "v_mul_f32 v15, v15, v254\n"
    "v_mul_f32 v24, v24, v254\n"
    "v_mul_f32 v25, v25, v254\n"
    "v_mul_f32 v26, v26, v254\n"
    "v_mul_f32 v27, v27, v254\n"
    "v_mul_f32 v28, v28, v254\n"
    "v_mul_f32 v29, v29, v254\n"
    "v_mul_f32 v30, v30, v254\n"
    "v_mul_f32 v31, v31, v254\n"
    "v_mul_f32 v40, v40, v254\n"
    "v_mul_f32 v41, v41, v254\n"
    "v_mul_f32 v42, v42, v254\n"
    "v_mul_f32 v43, v43, v254\n"
    "v_mul_f32 v44, v44, v254\n"
    "v_mul_f32 v45, v45, v254\n"
    "v_mul_f32 v46, v46, v254\n"
    "v_mul_f32 v47, v47, v254\n"
    "v_mul_f32 v56, v56, v254\n"
    "v_mul_f32 v57, v57, v254\n"
    "v_mul_f32 v58, v58, v254\n"
    "v_mul_f32 v59, v59, v254\n"
    "v_mul_f32 v60, v60, v254\n"
    "v_mul_f32 v61, v61, v254\n"
    "v_mul_f32 v62, v62, v254\n"
    "v_mul_f32 v63, v63, v254\n"
    "v_mul_f32 v72, v72, v254\n"
    "v_mul_f32 v73, v73, v254\n"
    "v_mul_f32 v74, v74, v254\n"
    "v_mul_f32 v75, v75, v254\n"
    "v_mul_f32 v76, v76, v254\n"
    "v_mul_f32 v77, v77, v254\n"
    "v_mul_f32 v78, v78, v254\n"
    "v_mul_f32 v79, v79, v254\n"
    "v_mul_f32 v88, v88, v254\n"
    "v_mul_f32 v89, v89, v254\n"
    "v_mul_f32 v90, v90, v254\n"
    "v_mul_f32 v91, v91, v254\n"
    "v_mul_f32 v92, v92, v254\n"
    "v_mul_f32 v93, v93, v254\n"
    "v_mul_f32 v94, v94, v254\n"
    "v_mul_f32 v95, v95, v254\n"
    "v_mul_f32 v104, v104, v254\n"
    "v_mul_f32 v105, v105, v254\n"
    "v_mul_f32 v106, v106, v254\n"
    "v_mul_f32 v107, v107, v254\n"
    "v_mul_f32 v108, v108, v254\n"
    "v_mul_f32 v109, v109, v254\n"
    "v_mul_f32 v110, v110, v254\n"
    "v_mul_f32 v111, v111, v254\n"
    "v_mul_f32 v120, v120, v254\n"
    "v_mul_f32 v121, v121, v254\n"
    "v_mul_f32 v122, v122, v254\n"
    "v_mul_f32 v123, v123, v254\n"
    "v_mul_f32 v124, v124, v254\n"
    "v_mul_f32 v125, v125, v254\n"
    "v_mul_f32 v126, v126, v254\n"
    "v_mul_f32 v127, v127, v254\n"
    "v_exp_f32 v168, v128\n"
    "v_exp_f32 v169, v129\n"
    "v_exp_f32 v170, v130\n"
    "v_exp_f32 v171, v131\n"
    "v_cvt_pk_bf16_f32 v160, v168, v169\n"
    "v_cvt_pk_bf16_f32 v161, v170, v171\n"
    "v_exp_f32 v172, v132\n"
    "v_exp_f32 v173, v133\n"
    "v_exp_f32 v174, v134\n"
    "v_exp_f32 v175, v135\n"
    "v_cvt_pk_bf16_f32 v164, v172, v173\n"
    "v_cvt_pk_bf16_f32 v165, v174, v175\n"
    "v_exp_f32 v233, v136\n"
    "v_exp_f32 v234, v137\n"
    "v_exp_f32 v235, v138\n"
    "v_exp_f32 v236, v139\n"
    "v_cvt_pk_bf16_f32 v162, v233, v234\n"
    "v_cvt_pk_bf16_f32 v163, v235, v236\n"
    "v_exp_f32 v237, v140\n"
    "v_exp_f32 v238, v141\n"
    "v_exp_f32 v239, v142\n"
    "v_exp_f32 v240, v143\n"
    "v_cvt_pk_bf16_f32 v166, v237, v238\n"
    "v_cvt_pk_bf16_f32 v167, v239, v240\n"
    "s_nop 3\n"
    "s_branch LB29%=\n"
    "LR30%=:\n"
    "s_nop 7\n"
    "s_nop 3\n"
    "v_max3_f32 v253, v128, v129, v130\n"
    "v_max3_f32 v252, v131, v136, v137\n"
    "v_max3_f32 v253, v253, v138, v139\n"
    "v_max_f32 v253, v253, v252\n"
    "v_mov_b32 v252, v253\n"
    "s_nop 1\n"
    "v_permlane32_swap_b32 v253, v252\n"
    "v_max_f32 v253, v253, v252\n"
    "v_mov_b32 v252, v253\n"
    "s_nop 1\n"
    "v_permlane16_swap_b32 v253, v252\n"
    "v_max_f32 v253, v253, v252\n"
    "v_add_f32 v253, 0x40e00000, v253\n"
    "v_max_f32 v253, 0, v253\n"
    "v_sub_f32 v254, 0, v253\n"
    "v_exp_f32 v254, v254\n"
    "v_sub_f32 v128, v128, v253\n"
    "v_sub_f32 v129, v129, v253\n"
    "v_sub_f32 v130, v130, v253\n"
    "v_sub_f32 v131, v131, v253\n"
    "v_sub_f32 v136, v136, v253\n"
    "v_sub_f32 v137, v137, v253\n"
    "v_sub_f32 v138, v138, v253\n"
    "v_sub_f32 v139, v139, v253\n"
    "v_sub_f32 v144, v144, v253\n"
    "v_sub_f32 v145, v145, v253\n"
    "v_sub_f32 v146, v146, v253\n"
    "v_sub_f32 v147, v147, v253\n"
    "v_mul_f32 v176, v176, v254\n"
    "v_mul_f32 v177, v177, v254\n"
    "v_mul_f32 v178, v178, v254\n"
    "v_mul_f32 v179, v179, v254\n"
    "v_mul_f32 v0, v0, v254\n"
    "v_mul_f32 v1, v1, v254\n"
    "v_mul_f32 v2, v2, v254\n"
    "v_mul_f32 v3, v3, v254\n"
    "v_mul_f32 v4, v4, v254\n"
    "v_mul_f32 v5, v5, v254\n"
    "v_mul_f32 v6, v6, v254\n"
    "v_mul_f32 v7, v7, v254\n"
    "v_mul_f32 v16, v16, v254\n"
    "v_mul_f32 v17, v17, v254\n"
    "v_mul_f32 v18, v18, v254\n"
    "v_mul_f32 v19, v19, v254\n"
    "v_mul_f32 v20, v20, v254\n"
    "v_mul_f32 v21, v21, v254\n"
    "v_mul_f32 v22, v22, v254\n"
    "v_mul_f32 v23, v23, v254\n"
    "v_mul_f32 v32, v32, v254\n"
    "v_mul_f32 v33, v33, v254\n"
    "v_mul_f32 v34, v34, v254\n"
    "v_mul_f32 v35, v35, v254\n"
    "v_mul_f32 v36, v36, v254\n"
    "v_mul_f32 v37, v37, v254\n"
    "v_mul_f32 v38, v38, v254\n"
    "v_mul_f32 v39, v39, v254\n"
    "v_mul_f32 v48, v48, v254\n"
    "v_mul_f32 v49, v49, v254\n"
    "v_mul_f32 v50, v50, v254\n"
    "v_mul_f32 v51, v51, v254\n"
    "v_mul_f32 v52, v52, v254\n"
    "v_mul_f32 v53, v53, v254\n"
    "v_mul_f32 v54, v54, v254\n"
    "v_mul_f32 v55, v55, v254\n"
    "v_mul_f32 v64, v64, v254\n"
    "v_mul_f32 v65, v65, v254\n"
    "v_mul_f32 v66, v66, v254\n"
    "v_mul_f32 v67, v67, v254\n"
    "v_mul_f32 v68, v68, v254\n"
    "v_mul_f32 v69, v69, v254\n"
    "v_mul_f32 v70, v70, v254\n"
    "v_mul_f32 v71, v71, v254\n"
    "v_mul_f32 v80, v80, v254\n"
    "v_mul_f32 v81, v81, v254\n"
    "v_mul_f32 v82, v82, v254\n"
    "v_mul_f32 v83, v83, v254\n"
    "v_mul_f32 v84, v84, v254\n"
    "v_mul_f32 v85, v85, v254\n"
    "v_mul_f32 v86, v86, v254\n"
    "v_mul_f32 v87, v87, v254\n"
    "v_mul_f32 v96, v96, v254\n"
    "v_mul_f32 v97, v97, v254\n"
    "v_mul_f32 v98, v98, v254\n"
    "v_mul_f32 v99, v99, v254\n"
    "v_mul_f32 v100, v100, v254\n"
    "v_mul_f32 v101, v101, v254\n"
    "v_mul_f32 v102, v102, v254\n"
    "v_mul_f32 v103, v103, v254\n"
    "v_mul_f32 v112, v112, v254\n"
    "v_mul_f32 v113, v113, v254\n"
    "v_mul_f32 v114, v114, v254\n"
    "v_mul_f32 v115, v115, v254\n"
    "v_mul_f32 v116, v116, v254\n"
    "v_mul_f32 v117, v117, v254\n"
    "v_mul_f32 v118, v118, v254\n"
    "v_mul_f32 v119, v119, v254\n"
    "v_max3_f32 v253, v132, v133, v134\n"
    "v_max3_f32 v252, v135, v140, v141\n"
    "v_max3_f32 v253, v253, v142, v143\n"
    "v_max_f32 v253, v253, v252\n"
    "v_mov_b32 v252, v253\n"
    "s_nop 1\n"
    "v_permlane32_swap_b32 v253, v252\n"
    "v_max_f32 v253, v253, v252\n"
    "v_mov_b32 v252, v253\n"
    "s_nop 1\n"
    "v_permlane16_swap_b32 v253, v252\n"
    "v_max_f32 v253, v253, v252\n"
    "v_add_f32 v253, 0x40e00000, v253\n"
    "v_max_f32 v253, 0, v253\n"
    "v_sub_f32 v254, 0, v253\n"
    "v_exp_f32 v254, v254\n"
    "v_sub_f32 v132, v132, v253\n"
    "v_sub_f32 v133, v133, v253\n"
    "v_sub_f32 v134, v134, v253\n"
    "v_sub_f32 v135, v135, v253\n"
    "v_sub_f32 v140, v140, v253\n"
    "v_sub_f32 v141, v141, v253\n"
    "v_sub_f32 v142, v142, v253\n"
    "v_sub_f32 v143, v143, v253\n"
    "v_sub_f32 v148, v148, v253\n"
    "v_sub_f32 v149, v149, v253\n"
    "v_sub_f32 v150, v150, v253\n"
    "v_sub_f32 v151, v151, v253\n"
    "v_mul_f32 v180, v180, v254\n"
    "v_mul_f32 v181, v181, v254\n"
    "v_mul_f32 v182, v182, v254\n"
    "v_mul_f32 v183, v183, v254\n"
    "v_mul_f32 v8, v8, v254\n"
    "v_mul_f32 v9, v9, v254\n"
    "v_mul_f32 v10, v10, v254\n"
    "v_mul_f32 v11, v11, v254\n"
    "v_mul_f32 v12, v12, v254\n"
    "v_mul_f32 v13, v13, v254\n"
    "v_mul_f32 v14, v14, v254\n"
    "v_mul_f32 v15, v15, v254\n"
    "v_mul_f32 v24, v24, v254\n"
    "v_mul_f32 v25, v25, v254\n"
    "v_mul_f32 v26, v26, v254\n"
    "v_mul_f32 v27, v27, v254\n"
    "v_mul_f32 v28, v28, v254\n"
    "v_mul_f32 v29, v29, v254\n"
    "v_mul_f32 v30, v30, v254\n"
    "v_mul_f32 v31, v31, v254\n"
    "v_mul_f32 v40, v40, v254\n"
    "v_mul_f32 v41, v41, v254\n"
    "v_mul_f32 v42, v42, v254\n"
    "v_mul_f32 v43, v43, v254\n"
    "v_mul_f32 v44, v44, v254\n"
    "v_mul_f32 v45, v45, v254\n"
    "v_mul_f32 v46, v46, v254\n"
    "v_mul_f32 v47, v47, v254\n"
    "v_mul_f32 v56, v56, v254\n"
    "v_mul_f32 v57, v57, v254\n"
    "v_mul_f32 v58, v58, v254\n"
    "v_mul_f32 v59, v59, v254\n"
    "v_mul_f32 v60, v60, v254\n"
    "v_mul_f32 v61, v61, v254\n"
    "v_mul_f32 v62, v62, v254\n"
    "v_mul_f32 v63, v63, v254\n"
    "v_mul_f32 v72, v72, v254\n"
    "v_mul_f32 v73, v73, v254\n"
    "v_mul_f32 v74, v74, v254\n"
    "v_mul_f32 v75, v75, v254\n"
    "v_mul_f32 v76, v76, v254\n"
    "v_mul_f32 v77, v77, v254\n"
    "v_mul_f32 v78, v78, v254\n"
    "v_mul_f32 v79, v79, v254\n"
    "v_mul_f32 v88, v88, v254\n"
    "v_mul_f32 v89, v89, v254\n"
    "v_mul_f32 v90, v90, v254\n"
    "v_mul_f32 v91, v91, v254\n"
    "v_mul_f32 v92, v92, v254\n"
    "v_mul_f32 v93, v93, v254\n"
    "v_mul_f32 v94, v94, v254\n"
    "v_mul_f32 v95, v95, v254\n"
    "v_mul_f32 v104, v104, v254\n"
    "v_mul_f32 v105, v105, v254\n"
    "v_mul_f32 v106, v106, v254\n"
    "v_mul_f32 v107, v107, v254\n"
    "v_mul_f32 v108, v108, v254\n"
    "v_mul_f32 v109, v109, v254\n"
    "v_mul_f32 v110, v110, v254\n"
    "v_mul_f32 v111, v111, v254\n"
    "v_mul_f32 v120, v120, v254\n"
    "v_mul_f32 v121, v121, v254\n"
    "v_mul_f32 v122, v122, v254\n"
    "v_mul_f32 v123, v123, v254\n"
    "v_mul_f32 v124, v124, v254\n"
    "v_mul_f32 v125, v125, v254\n"
    "v_mul_f32 v126, v126, v254\n"
    "v_mul_f32 v127, v127, v254\n"
    "v_exp_f32 v168, v128\n"
    "v_exp_f32 v169, v129\n"
    "v_exp_f32 v170, v130\n"
    "v_exp_f32 v171, v131\n"
    "v_cvt_pk_bf16_f32 v152, v168, v169\n"
    "v_cvt_pk_bf16_f32 v153, v170, v171\n"
    "v_exp_f32 v172, v132\n"
    "v_exp_f32 v173, v133\n"
    "v_exp_f32 v174, v134\n"
    "v_exp_f32 v175, v135\n"
    "v_cvt_pk_bf16_f32 v156, v172, v173\n"
    "v_cvt_pk_bf16_f32 v157, v174, v175\n"
    "v_exp_f32 v233, v136\n"
    "v_exp_f32 v234, v137\n"
    "v_exp_f32 v235, v138\n"
    "v_exp_f32 v236, v139\n"
    "v_cvt_pk_bf16_f32 v154, v233, v234\n"
    "v_cvt_pk_bf16_f32 v155, v235, v236\n"
    "v_exp_f32 v237, v140\n"
    "v_exp_f32 v238, v141\n"
    "v_exp_f32 v239, v142\n"
    "v_exp_f32 v240, v143\n"
    "v_cvt_pk_bf16_f32 v158, v237, v238\n"
    "v_cvt_pk_bf16_f32 v159, v239, v240\n"
    "s_nop 3\n"
    "s_branch LB30%=\n"
    "LR31%=:\n"
    "s_nop 7\n"
    "s_nop 3\n"
    "v_max3_f32 v253, v128, v129, v130\n"
    "v_max3_f32 v252, v131, v136, v137\n"
    "v_max3_f32 v253, v253, v138, v139\n"
    "v_max_f32 v253, v253, v252\n"
    "v_mov_b32 v252, v253\n"
    "s_nop 1\n"
    "v_permlane32_swap_b32 v253, v252\n"
    "v_max_f32 v253, v253, v252\n"
    "v_mov_b32 v252, v253\n"
    "s_nop 1\n"
    "v_permlane16_swap_b32 v253, v252\n"
    "v_max_f32 v253, v253, v252\n"
    "v_add_f32 v253, 0x40e00000, v253\n"
    "v_max_f32 v253, 0, v253\n"
    "v_sub_f32 v254, 0, v253\n"
    "v_exp_f32 v254, v254\n"
    "v_sub_f32 v128, v128, v253\n"
    "v_sub_f32 v129, v129, v253\n"
    "v_sub_f32 v130, v130, v253\n"
    "v_sub_f32 v131, v131, v253\n"
    "v_sub_f32 v136, v136, v253\n"
    "v_sub_f32 v137, v137, v253\n"
    "v_sub_f32 v138, v138, v253\n"
    "v_sub_f32 v139, v139, v253\n"
    "v_sub_f32 v144, v144, v253\n"
    "v_sub_f32 v145, v145, v253\n"
    "v_sub_f32 v146, v146, v253\n"
    "v_sub_f32 v147, v147, v253\n"
    "v_mul_f32 v176, v176, v254\n"
    "v_mul_f32 v177, v177, v254\n"
    "v_mul_f32 v178, v178, v254\n"
    "v_mul_f32 v179, v179, v254\n"
    "v_mul_f32 v0, v0, v254\n"
    "v_mul_f32 v1, v1, v254\n"
    "v_mul_f32 v2, v2, v254\n"
    "v_mul_f32 v3, v3, v254\n"
    "v_mul_f32 v4, v4, v254\n"
    "v_mul_f32 v5, v5, v254\n"
    "v_mul_f32 v6, v6, v254\n"
    "v_mul_f32 v7, v7, v254\n"
    "v_mul_f32 v16, v16, v254\n"
    "v_mul_f32 v17, v17, v254\n"
    "v_mul_f32 v18, v18, v254\n"
    "v_mul_f32 v19, v19, v254\n"
    "v_mul_f32 v20, v20, v254\n"
    "v_mul_f32 v21, v21, v254\n"
    "v_mul_f32 v22, v22, v254\n"
    "v_mul_f32 v23, v23, v254\n"
    "v_mul_f32 v32, v32, v254\n"
    "v_mul_f32 v33, v33, v254\n"
    "v_mul_f32 v34, v34, v254\n"
    "v_mul_f32 v35, v35, v254\n"
    "v_mul_f32 v36, v36, v254\n"
    "v_mul_f32 v37, v37, v254\n"
    "v_mul_f32 v38, v38, v254\n"
    "v_mul_f32 v39, v39, v254\n"
    "v_mul_f32 v48, v48, v254\n"
    "v_mul_f32 v49, v49, v254\n"
    "v_mul_f32 v50, v50, v254\n"
    "v_mul_f32 v51, v51, v254\n"
    "v_mul_f32 v52, v52, v254\n"
    "v_mul_f32 v53, v53, v254\n"
    "v_mul_f32 v54, v54, v254\n"
    "v_mul_f32 v55, v55, v254\n"
    "v_mul_f32 v64, v64, v254\n"
    "v_mul_f32 v65, v65, v254\n"
    "v_mul_f32 v66, v66, v254\n"
    "v_mul_f32 v67, v67, v254\n"
    "v_mul_f32 v68, v68, v254\n"
    "v_mul_f32 v69, v69, v254\n"
    "v_mul_f32 v70, v70, v254\n"
    "v_mul_f32 v71, v71, v254\n"
    "v_mul_f32 v80, v80, v254\n"
    "v_mul_f32 v81, v81, v254\n"
    "v_mul_f32 v82, v82, v254\n"
    "v_mul_f32 v83, v83, v254\n"
    "v_mul_f32 v84, v84, v254\n"
    "v_mul_f32 v85, v85, v254\n"
    "v_mul_f32 v86, v86, v254\n"
    "v_mul_f32 v87, v87, v254\n"
    "v_mul_f32 v96, v96, v254\n"
    "v_mul_f32 v97, v97, v254\n"
    "v_mul_f32 v98, v98, v254\n"
    "v_mul_f32 v99, v99, v254\n"
    "v_mul_f32 v100, v100, v254\n"
    "v_mul_f32 v101, v101, v254\n"
    "v_mul_f32 v102, v102, v254\n"
    "v_mul_f32 v103, v103, v254\n"
    "v_mul_f32 v112, v112, v254\n"
    "v_mul_f32 v113, v113, v254\n"
    "v_mul_f32 v114, v114, v254\n"
    "v_mul_f32 v115, v115, v254\n"
    "v_mul_f32 v116, v116, v254\n"
    "v_mul_f32 v117, v117, v254\n"
    "v_mul_f32 v118, v118, v254\n"
    "v_mul_f32 v119, v119, v254\n"
    "v_max3_f32 v253, v132, v133, v134\n"
    "v_max3_f32 v252, v135, v140, v141\n"
    "v_max3_f32 v253, v253, v142, v143\n"
    "v_max_f32 v253, v253, v252\n"
    "v_mov_b32 v252, v253\n"
    "s_nop 1\n"
    "v_permlane32_swap_b32 v253, v252\n"
    "v_max_f32 v253, v253, v252\n"
    "v_mov_b32 v252, v253\n"
    "s_nop 1\n"
    "v_permlane16_swap_b32 v253, v252\n"
    "v_max_f32 v253, v253, v252\n"
    "v_add_f32 v253, 0x40e00000, v253\n"
    "v_max_f32 v253, 0, v253\n"
    "v_sub_f32 v254, 0, v253\n"
    "v_exp_f32 v254, v254\n"
    "v_sub_f32 v132, v132, v253\n"
    "v_sub_f32 v133, v133, v253\n"
    "v_sub_f32 v134, v134, v253\n"
    "v_sub_f32 v135, v135, v253\n"
    "v_sub_f32 v140, v140, v253\n"
    "v_sub_f32 v141, v141, v253\n"
    "v_sub_f32 v142, v142, v253\n"
    "v_sub_f32 v143, v143, v253\n"
    "v_sub_f32 v148, v148, v253\n"
    "v_sub_f32 v149, v149, v253\n"
    "v_sub_f32 v150, v150, v253\n"
    "v_sub_f32 v151, v151, v253\n"
    "v_mul_f32 v180, v180, v254\n"
    "v_mul_f32 v181, v181, v254\n"
    "v_mul_f32 v182, v182, v254\n"
    "v_mul_f32 v183, v183, v254\n"
    "v_mul_f32 v8, v8, v254\n"
    "v_mul_f32 v9, v9, v254\n"
    "v_mul_f32 v10, v10, v254\n"
    "v_mul_f32 v11, v11, v254\n"
    "v_mul_f32 v12, v12, v254\n"
    "v_mul_f32 v13, v13, v254\n"
    "v_mul_f32 v14, v14, v254\n"
    "v_mul_f32 v15, v15, v254\n"
    "v_mul_f32 v24, v24, v254\n"
    "v_mul_f32 v25, v25, v254\n"
    "v_mul_f32 v26, v26, v254\n"
    "v_mul_f32 v27, v27, v254\n"
    "v_mul_f32 v28, v28, v254\n"
    "v_mul_f32 v29, v29, v254\n"
    "v_mul_f32 v30, v30, v254\n"
    "v_mul_f32 v31, v31, v254\n"
    "v_mul_f32 v40, v40, v254\n"
    "v_mul_f32 v41, v41, v254\n"
    "v_mul_f32 v42, v42, v254\n"
    "v_mul_f32 v43, v43, v254\n"
    "v_mul_f32 v44, v44, v254\n"
    "v_mul_f32 v45, v45, v254\n"
    "v_mul_f32 v46, v46, v254\n"
    "v_mul_f32 v47, v47, v254\n"
    "v_mul_f32 v56, v56, v254\n"
    "v_mul_f32 v57, v57, v254\n"
    "v_mul_f32 v58, v58, v254\n"
    "v_mul_f32 v59, v59, v254\n"
    "v_mul_f32 v60, v60, v254\n"
    "v_mul_f32 v61, v61, v254\n"
    "v_mul_f32 v62, v62, v254\n"
    "v_mul_f32 v63, v63, v254\n"
    "v_mul_f32 v72, v72, v254\n"
    "v_mul_f32 v73, v73, v254\n"
    "v_mul_f32 v74, v74, v254\n"
    "v_mul_f32 v75, v75, v254\n"
    "v_mul_f32 v76, v76, v254\n"
    "v_mul_f32 v77, v77, v254\n"
    "v_mul_f32 v78, v78, v254\n"
    "v_mul_f32 v79, v79, v254\n"
    "v_mul_f32 v88, v88, v254\n"
    "v_mul_f32 v89, v89, v254\n"
    "v_mul_f32 v90, v90, v254\n"
    "v_mul_f32 v91, v91, v254\n"
    "v_mul_f32 v92, v92, v254\n"
    "v_mul_f32 v93, v93, v254\n"
    "v_mul_f32 v94, v94, v254\n"
    "v_mul_f32 v95, v95, v254\n"
    "v_mul_f32 v104, v104, v254\n"
    "v_mul_f32 v105, v105, v254\n"
    "v_mul_f32 v106, v106, v254\n"
    "v_mul_f32 v107, v107, v254\n"
    "v_mul_f32 v108, v108, v254\n"
    "v_mul_f32 v109, v109, v254\n"
    "v_mul_f32 v110, v110, v254\n"
    "v_mul_f32 v111, v111, v254\n"
    "v_mul_f32 v120, v120, v254\n"
    "v_mul_f32 v121, v121, v254\n"
    "v_mul_f32 v122, v122, v254\n"
    "v_mul_f32 v123, v123, v254\n"
    "v_mul_f32 v124, v124, v254\n"
    "v_mul_f32 v125, v125, v254\n"
    "v_mul_f32 v126, v126, v254\n"
    "v_mul_f32 v127, v127, v254\n"
    "v_exp_f32 v168, v128\n"
    "v_exp_f32 v169, v129\n"
    "v_exp_f32 v170, v130\n"
    "v_exp_f32 v171, v131\n"
    "v_cvt_pk_bf16_f32 v160, v168, v169\n"
    "v_cvt_pk_bf16_f32 v161, v170, v171\n"
    "v_exp_f32 v172, v132\n"
    "v_exp_f32 v173, v133\n"
    "v_exp_f32 v174, v134\n"
    "v_exp_f32 v175, v135\n"
    "v_cvt_pk_bf16_f32 v164, v172, v173\n"
    "v_cvt_pk_bf16_f32 v165, v174, v175\n"
    "v_exp_f32 v233, v136\n"
    "v_exp_f32 v234, v137\n"
    "v_exp_f32 v235, v138\n"
    "v_exp_f32 v236, v139\n"
    "v_cvt_pk_bf16_f32 v162, v233, v234\n"
    "v_cvt_pk_bf16_f32 v163, v235, v236\n"
    "v_exp_f32 v237, v140\n"
    "v_exp_f32 v238, v141\n"
    "v_exp_f32 v239, v142\n"
    "v_exp_f32 v240, v143\n"
    "v_cvt_pk_bf16_f32 v166, v237, v238\n"
    "v_cvt_pk_bf16_f32 v167, v239, v240\n"
    "s_nop 3\n"
    "s_branch LB31%=\n"
    "LEND%=:\n"
    :
    : "s"(__builtin_amdgcn_kernarg_segment_ptr()), "s"(blockIdx.x), "s"(w)
    : "v0", "v1", "v2", "v3", "v4", "v5", "v6", "v7", "v8", "v9", "v10", "v11", "v12", "v13", "v14", "v15", "v16", "v17", "v18", "v19", "v20", "v21", "v22", "v23", "v24", "v25", "v26", "v27", "v28", "v29", "v30", "v31", "v32", "v33", "v34", "v35", "v36", "v37", "v38", "v39", "v40", "v41", "v42", "v43", "v44", "v45", "v46", "v47", "v48", "v49", "v50", "v51", "v52", "v53", "v54", "v55", "v56", "v57", "v58", "v59", "v60", "v61", "v62", "v63", "v64", "v65", "v66", "v67", "v68", "v69", "v70", "v71", "v72", "v73", "v74", "v75", "v76", "v77", "v78", "v79", "v80", "v81", "v82", "v83", "v84", "v85", "v86", "v87", "v88", "v89", "v90", "v91", "v92", "v93", "v94", "v95", "v96", "v97", "v98", "v99", "v100", "v101", "v102", "v103", "v104", "v105", "v106", "v107", "v108", "v109", "v110", "v111", "v112", "v113", "v114", "v115", "v116", "v117", "v118", "v119", "v120", "v121", "v122", "v123", "v124", "v125", "v126", "v127", "v128", "v129", "v130", "v131", "v132", "v133", "v134", "v135", "v136", "v137", "v138", "v139", "v140", "v141", "v142", "v143", "v144", "v145", "v146", "v147", "v148", "v149", "v150", "v151", "v152", "v153", "v154", "v155", "v156", "v157", "v158", "v159", "v160", "v161", "v162", "v163", "v164", "v165", "v166", "v167", "v168", "v169", "v170", "v171", "v172", "v173", "v174", "v175", "v176", "v177", "v178", "v179", "v180", "v181", "v182", "v183", "v184", "v185", "v186", "v187", "v188", "v189", "v190", "v191", "v192", "v193", "v194", "v195", "v196", "v197", "v198", "v199", "v200", "v201", "v202", "v203", "v204", "v205", "v206", "v207", "v208", "v209", "v210", "v211", "v212", "v213", "v214", "v215", "v216", "v217", "v218", "v219", "v220", "v221", "v222", "v223", "v224", "v225", "v226", "v227", "v228", "v229", "v230", "v231", "v232", "v233", "v234", "v235", "v236", "v237", "v238", "v239", "v240", "v241", "v242", "v243", "v244", "v245", "v246", "v247", "v248", "v249", "v250", "v251", "v252", "v253", "v254", "v255", "a0", "a1", "a2", "a3", "a4", "a5", "a6", "a7", "a8", "a9", "a10", "a11", "a12", "a13", "a14", "a15", "a16", "a17", "a18", "a19", "a20", "a21", "a22", "a23", "a24", "a25", "a26", "a27", "a28", "a29", "a30", "a31", "a32", "a33", "a34", "a35", "a36", "a37", "a38", "a39", "a40", "a41", "a42", "a43", "a44", "a45", "a46", "a47", "a48", "a49", "a50", "a51", "a52", "a53", "a54", "a55", "a56", "a57", "a58", "a59", "a60", "a61", "a62", "a63", "a64", "a65", "a66", "a67", "a68", "a69", "a70", "a71", "a72", "a73", "a74", "a75", "a76", "a77", "a78", "a79", "a80", "a81", "a82", "a83", "a84", "a85", "a86", "a87", "a88", "a89", "a90", "a91", "a92", "a93", "a94", "a95", "a96", "a97", "a98", "a99", "a100", "a101", "a102", "a103", "a104", "a105", "a106", "a107", "a108", "a109", "a110", "a111", "a112", "a113", "a114", "a115", "a116", "a117", "a118", "a119", "a120", "a121", "a122", "a123", "a124", "a125", "a126", "a127", "a128", "a129", "a130", "a131", "a132", "a133", "a134", "a135", "a136", "a137", "a138", "a139", "a140", "a141", "a142", "a143", "a144", "a145", "a146", "a147", "a148", "a149", "a150", "a151", "a152", "a153", "a154", "a155", "a156", "a157", "a158", "a159", "a160", "a161", "a162", "a163", "a164", "a165", "a166", "a167", "a168", "a169", "a170", "a171", "a172", "a173", "a174", "a175", "a176", "a177", "a178", "a179", "a180", "a181", "a182", "a183", "a184", "a185", "a186", "a187", "a188", "a189", "a190", "a191", "a192", "a193", "a194", "a195", "a196", "a197", "a198", "a199", "a200", "a201", "a202", "a203", "a204", "a205", "a206", "a207", "a208", "a209", "a210", "a211", "a212", "a213", "a214", "a215", "a216", "a217", "a218", "a219", "a220", "a221", "a222", "a223", "a224", "a225", "a226", "a227", "a228", "a229", "a230", "a231", "a232", "a233", "a234", "a235", "a236", "a237", "a238", "a239", "a240", "a241", "a242", "a243", "a244", "a245", "a246", "a247", "a248", "a249", "a250", "a251", "a252", "a253", "a254", "a255", "s4", "s5", "s6", "s7", "s8", "s9", "s10", "s11", "s12", "s13", "s14", "s15", "s16", "s17", "s18", "s19", "s20", "s21", "s22", "s23", "s24", "s25", "s26", "s27", "s28", "s29", "s30", "s31", "s34", "s35", "s36", "s37", "s38", "s39", "s40", "s41", "s42", "s43", "s44", "s45", "s46", "s47", "s48", "s49", "s50", "s51", "s52", "s53", "s54", "s55", "s56", "s57", "s58", "s59", "s60", "s61", "s62", "s63", "s64", "s65", "s66", "s67", "s68", "s69", "s70", "s71", "s72", "s73", "s74", "s75", "s76", "s77", "s78", "s79", "s80", "s81", "s82", "s83", "s84", "s85", "s86", "s87", "s88", "s89", "s90", "s91", "s92", "s93", "s94", "s95", "vcc", "scc", "memory");
}

extern "C" void kernel_launch(void* const* d_in, const int* in_sizes, int n_in, void* d_out, int out_size, void* d_ws, size_t ws_size,
                              hipStream_t stream) {
  mha_main<<<256, 256, 0, stream>>>((const char*)d_in[0], (const char*)d_in[1], (const char*)d_in[2], (const char*)d_in[3], (const char*)d_in[4],
                                    (char*)d_ws, (char*)d_out, (const char*)d_ws);
}
```
